```python
import torch

def _softmax(x, axis=-1):
    return torch.softmax(x, dim=axis)
def _gelu(x):
    return torch.nn.functional.gelu(x, approximate="tanh")
def _silu(x):
    return torch.nn.functional.silu(x)
def _sigmoid(x):
    return torch.sigmoid(x)
def _layernorm(x, axis=-1, eps=1e-5):
    m = x.mean(dim=axis, keepdim=True)
    v = x.var(dim=axis, keepdim=True, unbiased=False)
    return (x - m) / torch.sqrt(v + eps)
def _scatter_add(tgt, idx, src, axis=0):
    return tgt.clone().index_add_(axis, idx, src)
def _segment_sum(src, seg, n):
    out = torch.zeros((n,) + src.shape[1:], dtype=src.dtype)
    return out.index_add_(0, seg, src)
def _pad(x, pw):
    flat = tuple(v for pair in reversed(pw) for v in pair)
    return torch.nn.functional.pad(x, flat)

kernel_name = 'standalone__gqa_softmax__near_ridge'

def setup_inputs():

    _w = (lambda a: (lambda t: t.to(torch.bfloat16) if t.is_floating_point() else t)(torch.as_tensor(a)))
    B, Hq, Hkv, Sq, Sk, D = 8, 16, 8, 384, 512, 256
    return {
        "q": _w((lambda *s: torch.randn(*s, dtype=torch.bfloat16) * 0.8)(B, Hq, Sq, D)),
        "k": _w((lambda *s: torch.randn(*s, dtype=torch.bfloat16) * 0.8)(B, Hkv, Sk, D)),
        "v": _w((lambda *s: torch.randn(*s, dtype=torch.bfloat16) * 0.05)(B, Hkv, Sk, D)),
    }

def reference(q, k, v):
    B, Hq, Sq, D = q.shape
    Hkv = k.shape[1]
    rep = Hq // Hkv
    k = (lambda x, n, axis: torch.repeat_interleave(x, n, dim=axis))(k, rep, axis=1)
    v = (lambda x, n, axis: torch.repeat_interleave(x, n, dim=axis))(v, rep, axis=1)
    scale = D ** -0.5
    scores = torch.einsum("bhqd,bhkd->bhqk", q, k) * scale
    p = _softmax(scores, axis=-1)
    return torch.einsum("bhqk,bhkd->bhqd", p, v)
```

```cpp
#include <hip/hip_runtime.h>
#include <hip/hip_bf16.h>
#include <cstdint>

typedef short bf16x8 __attribute__((ext_vector_type(8)));
typedef short s16x4 __attribute__((ext_vector_type(4)));
typedef float f32x4 __attribute__((ext_vector_type(4)));
typedef unsigned u32x4 __attribute__((ext_vector_type(4)));
typedef unsigned u32x2 __attribute__((ext_vector_type(2)));
typedef __bf16 bf16x8_t __attribute__((ext_vector_type(8)));
typedef __bf16 bf16x2_t __attribute__((ext_vector_type(2)));
typedef float f32x2 __attribute__((ext_vector_type(2)));

constexpr int B_ = 8, HQ = 16, HKV = 8, SQ = 384, SK = 512, D = 256;
constexpr int TILE = 64, NT = SK / TILE;
constexpr int TILEB = TILE * D * 2;
typedef __attribute__((address_space(3))) char* lds_ptr;
#define SBAR() __builtin_amdgcn_sched_barrier(0)

__device__ __forceinline__ unsigned cvtpk(float lo, float hi) {
  f32x2 v = {lo, hi}; bf16x2_t b = __builtin_convertvector(v, bf16x2_t); return __builtin_bit_cast(unsigned, b);
}
#define MFMA(a, b, c) __builtin_amdgcn_mfma_f32_16x16x32_bf16(__builtin_bit_cast(bf16x8_t, a), __builtin_bit_cast(bf16x8_t, b), c, 0, 0, 0)

__global__ __launch_bounds__(256, 1) void attn_base(const unsigned short* __restrict__ Q, const unsigned short* __restrict__ K,
                                                    const unsigned short* __restrict__ V, unsigned short* __restrict__ O) {
  extern __shared__ __attribute__((aligned(16))) char lds[];
  const int tid = threadIdx.x, lane = tid & 63, wid = __builtin_amdgcn_readfirstlane(tid >> 6);
  const int l15 = lane & 15, g = lane >> 4;
  const int bid = blockIdx.x, xcd = bid & 7, idx = bid >> 3;
  const int grp = xcd * 8 + (idx >> 2), part = idx & 3;
  const long qrow0 = (long)grp * 768 + part * 192 + wid * 48;
  const char* Kg = (const char*)K + (long)grp * SK * D * 2;
  const char* Vg = (const char*)V + (long)grp * SK * D * 2;
  const unsigned lds0 = (unsigned)(uintptr_t)lds;
  const __amdgpu_buffer_rsrc_t rK = __builtin_amdgcn_make_buffer_rsrc((void*)Kg, (short)0, SK * D * 2, 0x00020000);
  const __amdgpu_buffer_rsrc_t rV = __builtin_amdgcn_make_buffer_rsrc((void*)Vg, (short)0, SK * D * 2, 0x00020000);
  unsigned voff[4];
#pragma unroll
  for (int i = 0; i < 4; ++i) { const int row = 2 * i + (lane >> 5); voff[i] = (lane >> 5) * 512 + (((lane & 31) ^ (2 * (row & 7))) * 16); }
  auto dma_k = [&](unsigned soff, unsigned dst) {
#pragma unroll
    for (int h = 0; h < 2; ++h) {
      const unsigned so = __builtin_amdgcn_readfirstlane(soff + wid * 8192 + h * 4096), m = __builtin_amdgcn_readfirstlane(dst + wid * 8192 + h * 4096);
      asm volatile("s_nop 4\n\ts_mov_b32 m0, %5\n\ts_nop 0\n\t"
                   "buffer_load_dwordx4 %0, %4, %6 offen lds\n\t"
                   "buffer_load_dwordx4 %1, %4, %6 offen offset:1024 lds\n\t"
                   "buffer_load_dwordx4 %2, %4, %6 offen offset:2048 lds\n\t"
                   "buffer_load_dwordx4 %3, %4, %6 offen offset:3072 lds"
                   :: "v"(voff[0]), "v"(voff[1]), "v"(voff[2]), "v"(voff[3]), "s"(rK), "s"(m), "s"(so) : "memory");
    }
  };
  const unsigned vvoff = (lane >> 3) * 512 + (((lane & 7) ^ ((lane >> 3) & 6)) * 16);
  auto dma_v = [&](int c, unsigned dst) {
#pragma unroll
    for (int i = 0; i < 16; ++i) {
      const unsigned piece = wid * 16 + i;
      const unsigned so = __builtin_amdgcn_readfirstlane(piece * 4096 + c * 128), m = __builtin_amdgcn_readfirstlane(dst + piece * 1024);
      asm volatile("s_nop 4\n\ts_mov_b32 m0, %2\n\ts_nop 0\n\t"
                   "buffer_load_dwordx4 %0, %1, %3 offen lds"
                   :: "v"(vvoff), "s"(rV), "s"(m), "s"(so) : "memory");
    }
  };
  unsigned kbase[4], vbase[4];
#pragma unroll
  for (int i = 0; i < 4; ++i) kbase[i] = l15 * 512 + (((4 * i + g) ^ (2 * (l15 & 7))) * 16);
  { const int q4 = (lane & 15) >> 2, p = lane & 3, row0 = 4 * g + q4;
#pragma unroll
    for (int i = 0; i < 4; ++i) vbase[i] = row0 * 128 + (((2 * i + (p >> 1)) ^ (row0 & 6)) * 16) + 8 * (p & 1); }
  dma_k(0, lds0);
  bf16x8 qf[3][8];
#pragma unroll
  for (int qb = 0; qb < 3; ++qb)
#pragma unroll
    for (int ks = 0; ks < 8; ++ks)
      qf[qb][ks] = *(const bf16x8*)(Q + (qrow0 + qb * 16 + l15) * D + ks * 32 + g * 8);
  float m[3], l[3] = {0.f, 0.f, 0.f};
  constexpr float C = 0.0625f * 1.4426950408889634f;
  u32x4 pf[3][16];
  const int rot = grp & 3;
#pragma unroll
  for (int t = 0; t < NT; ++t) {
    asm volatile("s_waitcnt vmcnt(0)\n\ts_barrier" ::: "memory");
    const int buf = t & 1;
    if (t + 1 < NT) dma_k((t + 1) * TILEB, lds0 + (buf ^ 1) * TILEB);
    else dma_v(rot, lds0 + 2 * TILEB);
    lds_ptr Ks = (lds_ptr)lds + buf * TILEB;
    f32x4 s[3][4];
#pragma unroll
    for (int kb = 0; kb < 4; ++kb) {
#pragma unroll
      for (int qb = 0; qb < 3; ++qb) s[qb][kb] = f32x4{0.f, 0.f, 0.f, 0.f};
#pragma unroll
      for (int ks = 0; ks < 8; ++ks) {
        bf16x8 kf = *(const __attribute__((address_space(3))) bf16x8*)(Ks + kbase[ks & 3] + kb * 8192 + (ks >> 2) * 256);
#pragma unroll
        for (int qb = 0; qb < 3; ++qb) s[qb][kb] = MFMA(kf, qf[qb][ks], s[qb][kb]);
      }
      SBAR();
    }
#pragma unroll
    for (int qb = 0; qb < 3; ++qb) {
      float mx = s[qb][0][0];
#pragma unroll
      for (int kb = 0; kb < 4; ++kb)
#pragma unroll
        for (int r = 0; r < 4; ++r) mx = fmaxf(mx, s[qb][kb][r]);
      if (t == 0) {
        mx = fmaxf(mx, __shfl_xor(mx, 16));
        mx = fmaxf(mx, __shfl_xor(mx, 32));
        m[qb] = mx;
      } else if (__builtin_expect(__any((mx - m[qb]) * C > 16.f), 0)) {
        mx = fmaxf(mx, __shfl_xor(mx, 16));
        mx = fmaxf(mx, __shfl_xor(mx, 32));
        const float dl = ceilf(fmaxf((mx - m[qb]) * C, 0.f));
        const float f = __builtin_amdgcn_exp2f(-dl);
        m[qb] += dl / C;
        l[qb] *= f;
#pragma unroll
        for (int j = 0; j < 2 * t; ++j)
#pragma unroll
          for (int e = 0; e < 4; ++e) {
            const unsigned w = pf[qb][j][e];
            pf[qb][j][e] = cvtpk(__uint_as_float(w << 16) * f, __uint_as_float(w & 0xffff0000u) * f);
          }
      }
      const float mc = -m[qb] * C;
      float sum = 0.f;
#pragma unroll
      for (int kb = 0; kb < 4; ++kb)
#pragma unroll
        for (int r = 0; r < 4; ++r) { s[qb][kb][r] = __builtin_amdgcn_exp2f(fmaf(s[qb][kb][r], C, mc)); sum += s[qb][kb][r]; }
      l[qb] += sum;
#pragma unroll
      for (int h = 0; h < 2; ++h) {
        u32x4 w = {cvtpk(s[qb][2 * h][0], s[qb][2 * h][1]), cvtpk(s[qb][2 * h][2], s[qb][2 * h][3]),
                   cvtpk(s[qb][2 * h + 1][0], s[qb][2 * h + 1][1]), cvtpk(s[qb][2 * h + 1][2], s[qb][2 * h + 1][3])};
        pf[qb][2 * t + h] = w;
      }
    }
    asm volatile("s_waitcnt lgkmcnt(0)" ::: "memory");
    SBAR();
  }
  float inv[3];
#pragma unroll
  for (int qb = 0; qb < 3; ++qb) {
    float lt = l[qb];
    lt += __shfl_xor(lt, 16);
    lt += __shfl_xor(lt, 32);
    inv[qb] = 1.0f / lt;
  }
  for (int ci = 0; ci < 4; ++ci) {
    const int c = (ci + rot) & 3;
    asm volatile("s_waitcnt vmcnt(0)\n\ts_barrier" ::: "memory");
    const int buf = (ci & 1) ^ 1;
    if (ci + 1 < 4) dma_v((c + 1) & 3, lds0 + (buf ^ 1) * 2 * TILEB);
    lds_ptr Vs = (lds_ptr)lds + buf * 2 * TILEB;
    f32x4 o[3][4];
#pragma unroll
    for (int a = 0; a < 4; ++a)
#pragma unroll
      for (int qb = 0; qb < 3; ++qb) o[qb][a] = f32x4{0.f, 0.f, 0.f, 0.f};
#pragma unroll
    for (int j = 0; j < 16; ++j) {
#pragma unroll
      for (int a = 0; a < 4; ++a) {
        s16x4 lo = __builtin_amdgcn_ds_read_tr16_b64_v4i16((__attribute__((address_space(3))) s16x4*)(Vs + vbase[a] + j * 4096));
        s16x4 hi = __builtin_amdgcn_ds_read_tr16_b64_v4i16((__attribute__((address_space(3))) s16x4*)(Vs + vbase[a] + j * 4096 + 2048));
        bf16x8 vf = __builtin_shufflevector(lo, hi, 0, 1, 2, 3, 4, 5, 6, 7);
#pragma unroll
        for (int qb = 0; qb < 3; ++qb) o[qb][a] = MFMA(vf, pf[qb][j], o[qb][a]);
      }
      if ((j & 1) == 1) SBAR();
    }
#pragma unroll
    for (int qb = 0; qb < 3; ++qb) {
      unsigned short* orow = O + (qrow0 + qb * 16 + l15) * D + c * 64;
#pragma unroll
      for (int a = 0; a < 4; ++a) {
        u32x2 w = {cvtpk(o[qb][a][0] * inv[qb], o[qb][a][1] * inv[qb]), cvtpk(o[qb][a][2] * inv[qb], o[qb][a][3] * inv[qb])};
        *(u32x2*)(orow + a * 16 + g * 4) = w;
      }
    }
    asm volatile("s_waitcnt lgkmcnt(0)" ::: "memory");
  }
}

extern "C" void kernel_launch(void* const* d_in, const int* in_sizes, int n_in, void* d_out, int out_size, void* d_ws, size_t ws_size,
                              hipStream_t stream) {
  const int shm = 4 * TILEB;
  (void)hipFuncSetAttribute((const void*)attn_base, hipFuncAttributeMaxDynamicSharedMemorySize, shm);
  hipLaunchKernelGGL(attn_base, dim3(256), dim3(256), shm, stream, (const unsigned short*)d_in[0], (const unsigned short*)d_in[1],
                     (const unsigned short*)d_in[2], (unsigned short*)d_out);
}
```

```cpp
#define STAUX 16
#define SI 0
#define USEMAX 0
#define FACT 0
#include <hip/hip_runtime.h>
#include <hip/hip_bf16.h>
#include <cstdint>

typedef short bf16x8 __attribute__((ext_vector_type(8)));
typedef short s16x4 __attribute__((ext_vector_type(4)));
typedef float f32x4 __attribute__((ext_vector_type(4)));
typedef unsigned u32x4 __attribute__((ext_vector_type(4)));
typedef unsigned u32x2 __attribute__((ext_vector_type(2)));
typedef __bf16 bf16x8_t __attribute__((ext_vector_type(8)));
typedef __bf16 bf16x2_t __attribute__((ext_vector_type(2)));
typedef float f32x2 __attribute__((ext_vector_type(2)));

constexpr int B_ = 8, HQ = 16, HKV = 8, SQ = 384, SK = 512, D = 256;
constexpr int TILE = 64, NT = SK / TILE;
constexpr int TILEB = TILE * D * 2;
typedef __attribute__((address_space(3))) char* lds_ptr;
#define SBAR() __builtin_amdgcn_sched_barrier(0)

__device__ __forceinline__ unsigned cvtpk(float lo, float hi) {
  f32x2 v = {lo, hi}; bf16x2_t b = __builtin_convertvector(v, bf16x2_t); return __builtin_bit_cast(unsigned, b);
}
#define MFMA(a, b, c) __builtin_amdgcn_mfma_f32_16x16x32_bf16(__builtin_bit_cast(bf16x8_t, a), __builtin_bit_cast(bf16x8_t, b), c, 0, 0, 0)

__global__ __launch_bounds__(256, 1) void attn_base(const unsigned short* __restrict__ Q, const unsigned short* __restrict__ K,
                                                    const unsigned short* __restrict__ V, unsigned short* __restrict__ O) {
  extern __shared__ __attribute__((aligned(16))) char lds[];
  const int tid = threadIdx.x, lane = tid & 63, wid = __builtin_amdgcn_readfirstlane(tid >> 6);
  const int l15 = lane & 15, g = lane >> 4;
  const int bid = blockIdx.x, xcd = bid & 7, idx = bid >> 3;
  const int grp = xcd * 8 + (idx >> 2), part = idx & 3;
  const long qrow0 = (long)grp * 768 + part * 192 + wid * 48;
  const char* Kg = (const char*)K + (long)grp * SK * D * 2;
  const char* Vg = (const char*)V + (long)grp * SK * D * 2;
  const unsigned lds0 = (unsigned)(uintptr_t)lds;
  const __amdgpu_buffer_rsrc_t rK = __builtin_amdgcn_make_buffer_rsrc((void*)Kg, (short)0, SK * D * 2, 0x00020000);
  const __amdgpu_buffer_rsrc_t rV = __builtin_amdgcn_make_buffer_rsrc((void*)Vg, (short)0, SK * D * 2, 0x00020000);
  unsigned voff[4];
#pragma unroll
  for (int i = 0; i < 4; ++i) { const int row = 2 * i + (lane >> 5); voff[i] = (lane >> 5) * 512 + (((lane & 31) ^ (2 * (row & 7))) * 16); }
  auto dma_k = [&](unsigned soff, unsigned dst) {
#pragma unroll
    for (int h = 0; h < 2; ++h) {
      const unsigned so = __builtin_amdgcn_readfirstlane(soff + wid * 8192 + h * 4096), m = __builtin_amdgcn_readfirstlane(dst + wid * 8192 + h * 4096);
      asm volatile("s_nop 4\n\ts_mov_b32 m0, %5\n\ts_nop 0\n\t"
                   "buffer_load_dwordx4 %0, %4, %6 offen lds\n\t"
                   "buffer_load_dwordx4 %1, %4, %6 offen offset:1024 lds\n\t"
                   "buffer_load_dwordx4 %2, %4, %6 offen offset:2048 lds\n\t"
                   "buffer_load_dwordx4 %3, %4, %6 offen offset:3072 lds"
                   :: "v"(voff[0]), "v"(voff[1]), "v"(voff[2]), "v"(voff[3]), "s"(rK), "s"(m), "s"(so) : "memory");
    }
  };
  const unsigned vvoff = (lane >> 3) * 512 + (((lane & 7) ^ ((lane >> 3) & 6)) * 16);
  auto dma_v = [&](int c, unsigned dst) {
#pragma unroll
    for (int i = 0; i < 16; ++i) {
      const unsigned piece = wid * 16 + i;
      const unsigned so = __builtin_amdgcn_readfirstlane(piece * 4096 + c * 128), m = __builtin_amdgcn_readfirstlane(dst + piece * 1024);
      asm volatile("s_nop 4\n\ts_mov_b32 m0, %2\n\ts_nop 0\n\t"
                   "buffer_load_dwordx4 %0, %1, %3 offen lds"
                   :: "v"(vvoff), "s"(rV), "s"(m), "s"(so) : "memory");
    }
  };
  unsigned kbase[4], vbase[4];
#pragma unroll
  for (int i = 0; i < 4; ++i) kbase[i] = l15 * 512 + (((4 * i + g) ^ (2 * (l15 & 7))) * 16);
  { const int q4 = (lane & 15) >> 2, p = lane & 3, row0 = 4 * g + q4;
#pragma unroll
    for (int i = 0; i < 4; ++i) vbase[i] = row0 * 128 + (((2 * i + (p >> 1)) ^ (row0 & 6)) * 16) + 8 * (p & 1); }
  dma_k(0, lds0);
  bf16x8 qf[3][8];
#pragma unroll
  for (int qb = 0; qb < 3; ++qb)
#pragma unroll
    for (int ks = 0; ks < 8; ++ks)
      qf[qb][ks] = *(const bf16x8*)(Q + (qrow0 + qb * 16 + l15) * D + ks * 32 + g * 8);
  float m[3], l[3] = {0.f, 0.f, 0.f};
  constexpr float C = 0.0625f * 1.4426950408889634f;
  u32x4 pf[3][16];
  const int rot = grp & 3;
#pragma unroll
  for (int t = 0; t < NT; ++t) {
    asm volatile("s_waitcnt vmcnt(0)\n\ts_barrier" ::: "memory");
    const int buf = t & 1;
    if (t + 1 < NT) dma_k((t + 1) * TILEB, lds0 + (buf ^ 1) * TILEB);
    else dma_v(rot, lds0 + 2 * TILEB);
    lds_ptr Ks = (lds_ptr)lds + buf * TILEB;
    f32x4 s[3][4];
#pragma unroll
    for (int kb = 0; kb < 4; ++kb) {
#pragma unroll
      for (int qb = 0; qb < 3; ++qb) s[qb][kb] = f32x4{0.f, 0.f, 0.f, 0.f};
#pragma unroll
      for (int ks = 0; ks < 8; ++ks) {
        bf16x8 kf = *(const __attribute__((address_space(3))) bf16x8*)(Ks + kbase[ks & 3] + kb * 8192 + (ks >> 2) * 256);
#pragma unroll
        for (int qb = 0; qb < 3; ++qb) s[qb][kb] = MFMA(kf, qf[qb][ks], s[qb][kb]);
      }
      SBAR();
    }
#pragma unroll
    for (int qb = 0; qb < 3; ++qb) {
      float mx = s[qb][0][0];
#pragma unroll
      for (int kb = 0; kb < 4; ++kb)
#pragma unroll
        for (int r = 0; r < 4; ++r) mx = fmaxf(mx, s[qb][kb][r]);
      if (t == 0) {
        mx = fmaxf(mx, __shfl_xor(mx, 16));
        mx = fmaxf(mx, __shfl_xor(mx, 32));
        m[qb] = mx;
      } else if (__builtin_expect(__any((mx - m[qb]) * C > 16.f), 0)) {
        mx = fmaxf(mx, __shfl_xor(mx, 16));
        mx = fmaxf(mx, __shfl_xor(mx, 32));
        const float dl = ceilf(fmaxf((mx - m[qb]) * C, 0.f));
        const float f = __builtin_amdgcn_exp2f(-dl);
        m[qb] += dl / C;
        l[qb] *= f;
#pragma unroll
        for (int j = 0; j < 2 * t; ++j)
#pragma unroll
          for (int e = 0; e < 4; ++e) {
            const unsigned w = pf[qb][j][e];
            pf[qb][j][e] = cvtpk(__uint_as_float(w << 16) * f, __uint_as_float(w & 0xffff0000u) * f);
          }
      }
      const float mc = -m[qb] * C;
      float sum = 0.f;
#pragma unroll
      for (int kb = 0; kb < 4; ++kb)
#pragma unroll
        for (int r = 0; r < 4; ++r) { s[qb][kb][r] = __builtin_amdgcn_exp2f(fmaf(s[qb][kb][r], C, mc)); sum += s[qb][kb][r]; }
      l[qb] += sum;
#pragma unroll
      for (int h = 0; h < 2; ++h) {
        u32x4 w = {cvtpk(s[qb][2 * h][0], s[qb][2 * h][1]), cvtpk(s[qb][2 * h][2], s[qb][2 * h][3]),
                   cvtpk(s[qb][2 * h + 1][0], s[qb][2 * h + 1][1]), cvtpk(s[qb][2 * h + 1][2], s[qb][2 * h + 1][3])};
        pf[qb][2 * t + h] = w;
      }
    }
    asm volatile("s_waitcnt lgkmcnt(0)" ::: "memory");
    SBAR();
  }
  float inv[3];
#pragma unroll
  for (int qb = 0; qb < 3; ++qb) {
    float lt = l[qb];
    lt += __shfl_xor(lt, 16);
    lt += __shfl_xor(lt, 32);
    inv[qb] = 1.0f / lt;
  }
  for (int ci = 0; ci < 4; ++ci) {
    const int c = (ci + rot) & 3;
    asm volatile("s_waitcnt vmcnt(0)\n\ts_barrier" ::: "memory");
    const int buf = (ci & 1) ^ 1;
    if (ci + 1 < 4) dma_v((c + 1) & 3, lds0 + (buf ^ 1) * 2 * TILEB);
    lds_ptr Vs = (lds_ptr)lds + buf * 2 * TILEB;
    f32x4 o[3][4];
#pragma unroll
    for (int a = 0; a < 4; ++a)
#pragma unroll
      for (int qb = 0; qb < 3; ++qb) o[qb][a] = f32x4{0.f, 0.f, 0.f, 0.f};
#pragma unroll
    for (int j = 0; j < 16; ++j) {
#pragma unroll
      for (int a = 0; a < 4; ++a) {
        s16x4 lo = __builtin_amdgcn_ds_read_tr16_b64_v4i16((__attribute__((address_space(3))) s16x4*)(Vs + vbase[a] + j * 4096));
        s16x4 hi = __builtin_amdgcn_ds_read_tr16_b64_v4i16((__attribute__((address_space(3))) s16x4*)(Vs + vbase[a] + j * 4096 + 2048));
        bf16x8 vf = __builtin_shufflevector(lo, hi, 0, 1, 2, 3, 4, 5, 6, 7);
#pragma unroll
        for (int qb = 0; qb < 3; ++qb) o[qb][a] = MFMA(vf, pf[qb][j], o[qb][a]);
      }
      if ((j & 1) == 1) SBAR();
    }
#pragma unroll
    for (int qb = 0; qb < 3; ++qb) {
      unsigned short* orow = O + (qrow0 + qb * 16 + l15) * D + c * 64;
#pragma unroll
      for (int a = 0; a < 4; ++a) {
        u32x2 w = {cvtpk(o[qb][a][0] * inv[qb], o[qb][a][1] * inv[qb]), cvtpk(o[qb][a][2] * inv[qb], o[qb][a][3] * inv[qb])};
        *(u32x2*)(orow + a * 16 + g * 4) = w;
      }
    }
    asm volatile("s_waitcnt lgkmcnt(0)" ::: "memory");
  }
}

#include <hip/hip_runtime.h>
#include <cstdint>
#define QMODE 0
#define LDAUX ""
#define STAMP(i) do { unsigned long long t_, r_; asm volatile("s_memtime %0\n\ts_memrealtime %1\n\ts_waitcnt lgkmcnt(0)" : "=s"(t_), "=s"(r_) :: "memory"); st[2*(i)] = t_; st[2*(i)+1] = r_; } while (0)

__global__ __launch_bounds__(256, 1) void mb(const unsigned short* __restrict__ Q, const unsigned short* __restrict__ K,
                                             const unsigned short* __restrict__ V, unsigned short* __restrict__ O, unsigned long long* ws) {
  extern __shared__ __attribute__((aligned(16))) char lds[];
  unsigned long long st[24];
  STAMP(0);
  const int tid = threadIdx.x, lane = tid & 63, wid = __builtin_amdgcn_readfirstlane(tid >> 6);
  const int l15 = lane & 15, g = lane >> 4;
  const int bid = blockIdx.x, xcd = bid & 7, idx = bid >> 3;
  const int grp = xcd * 8 + (idx >> 2), part = idx & 3;
  const long qrow0 = (long)grp * 768 + part * 192 + wid * 48;
  const char* Kg = (const char*)K + (long)grp * SK * D * 2;
  const char* Vg = (const char*)V + (long)grp * SK * D * 2;
  const unsigned lds0 = (unsigned)(uintptr_t)lds;
  const __amdgpu_buffer_rsrc_t rK = __builtin_amdgcn_make_buffer_rsrc((void*)Kg, (short)0, SK * D * 2, 0x00020000);
  const __amdgpu_buffer_rsrc_t rV = __builtin_amdgcn_make_buffer_rsrc((void*)Vg, (short)0, SK * D * 2, 0x00020000);
  const __amdgpu_buffer_rsrc_t rQ = __builtin_amdgcn_make_buffer_rsrc((void*)(Q + qrow0 * D), (short)0, 48 * D * 2, 0x00020000);
  const __amdgpu_buffer_rsrc_t rO = __builtin_amdgcn_make_buffer_rsrc((void*)(O + qrow0 * D), (short)0, 48 * D * 2, 0x00020000);
  unsigned voff[4];
#pragma unroll
  for (int i = 0; i < 4; ++i) { const int row = 2 * i + (lane >> 5); voff[i] = (lane >> 5) * 512 + (((lane & 31) ^ (2 * (row & 7))) * 16); }
  auto dma4 = [&](const __amdgpu_buffer_rsrc_t& r, unsigned soff, unsigned dst) {
    const unsigned so = __builtin_amdgcn_readfirstlane(soff), m = __builtin_amdgcn_readfirstlane(dst);
    asm volatile("s_nop 4\n\ts_mov_b32 m0, %5\n\ts_nop 0\n\t"
                 "buffer_load_dwordx4 %0, %4, %6 offen " LDAUX " lds\n\t"
                 "buffer_load_dwordx4 %1, %4, %6 offen offset:1024 " LDAUX " lds\n\t"
                 "buffer_load_dwordx4 %2, %4, %6 offen offset:2048 " LDAUX " lds\n\t"
                 "buffer_load_dwordx4 %3, %4, %6 offen offset:3072 " LDAUX " lds"
                 :: "v"(voff[0]), "v"(voff[1]), "v"(voff[2]), "v"(voff[3]), "s"(r), "s"(m), "s"(so) : "memory");
  };
  auto dma_tile = [&](const __amdgpu_buffer_rsrc_t& r, unsigned soff, unsigned dst) { dma4(r, soff + wid * 8192, dst + wid * 8192); dma4(r, soff + wid * 8192 + 4096, dst + wid * 8192 + 4096); };
  const unsigned vvoff = (lane >> 3) * 512 + (((lane & 7) ^ ((lane >> 3) & 6)) * 16);
  auto dma_v = [&](int c, unsigned dst) {
#pragma unroll
    for (int i = 0; i < 16; ++i) {
      const unsigned piece = wid * 16 + i;
      const unsigned so = __builtin_amdgcn_readfirstlane(piece * 4096 + c * 128), m = __builtin_amdgcn_readfirstlane(dst + piece * 1024);
      asm volatile("s_nop 4\n\ts_mov_b32 m0, %2\n\ts_nop 0\n\t"
                   "buffer_load_dwordx4 %0, %1, %3 offen " LDAUX " lds"
                   :: "v"(vvoff), "s"(rV), "s"(m), "s"(so) : "memory");
    }
  };
  dma_tile(rK, 0, lds0);
  u32x4 qf[24];
#pragma unroll
  for (int i = 0; i < 24; ++i) { const int qb = i / 8, ks = i % 8;
    asm volatile("buffer_load_dwordx4 %0, %1, %2, 0 offen offset:%3" : "=v"(qf[i]) : "v"((qb * 16 + l15) * 512 + g * 16), "s"(rQ), "i"(ks * 64) : "memory"); }
  STAMP(1);
  asm volatile("s_waitcnt vmcnt(24)" ::: "memory");
  STAMP(2);
  asm volatile("s_waitcnt vmcnt(16)" ::: "memory");
  STAMP(3);
  asm volatile("s_waitcnt vmcnt(0)" ::: "memory");
#pragma unroll
  for (int i = 0; i < 24; ++i) asm volatile("" : "+v"(qf[i]));
  STAMP(4);
#pragma unroll
  for (int t = 1; t < 8; ++t) { dma_tile(rK, t * TILEB, lds0 + (t & 3) * TILEB); if (t >= 3) asm volatile("s_waitcnt vmcnt(16)" ::: "memory"); }
  asm volatile("s_waitcnt vmcnt(0)" ::: "memory");
  STAMP(5);
  const int rot = grp & 3;
#pragma unroll
  for (int c = 0; c < 4; ++c) { dma_v((c + rot) & 3, lds0 + (c & 1) * 65536); if (c >= 1) asm volatile("s_waitcnt vmcnt(16)" ::: "memory"); }
  asm volatile("s_waitcnt vmcnt(0)" ::: "memory");
  STAMP(6);
#pragma unroll
  for (int i = 0; i < 24; ++i) { const int qb = i / 8, ks = i % 8;
    __builtin_amdgcn_raw_buffer_store_b128(qf[i], rO, (qb * 16 + l15) * 512 + g * 16 + ks * 64, 0, STAUX); }
  STAMP(7);
  asm volatile("s_waitcnt vmcnt(0)" ::: "memory");
  STAMP(8);
  if (lane == 0) {
#pragma unroll
    for (int i = 0; i < 18; ++i) ws[(bid * 4 + wid) * 64 + i] = st[i];
  }
}

__global__ __launch_bounds__(1024) void delay_kernel(const unsigned long long* ws, int si, int usemax, int fact) {
  __shared__ unsigned long long mn[1024]; __shared__ unsigned long long v[1024];
  const int t = threadIdx.x;
  mn[t] = ws[t * 64 + 1]; __syncthreads();
  for (int o = 512; o > 0; o >>= 1) { if (t < o) mn[t] = mn[t] < mn[t + o] ? mn[t] : mn[t + o]; __syncthreads(); }
  v[t] = ws[t * 64 + 2 * si + 1] - mn[0]; __syncthreads();
  for (int o = 512; o > 0; o >>= 1) { if (t < o) v[t] = usemax ? (v[t] > v[t + o] ? v[t] : v[t + o]) : v[t] + v[t + o]; __syncthreads(); }
  unsigned long long d = usemax ? v[0] * fact : v[0] * fact / 1024;
  if (d > 100000) d = 100000;
  const unsigned long long t0 = __builtin_amdgcn_s_memrealtime();
  while (__builtin_amdgcn_s_memrealtime() - t0 < d) __builtin_amdgcn_s_sleep(2);
}
extern "C" void kernel_launch(void* const* d_in, const int* in_sizes, int n_in, void* d_out, int out_size, void* d_ws, size_t ws_size,
                              hipStream_t stream) {
  (void)hipFuncSetAttribute((const void*)mb, hipFuncAttributeMaxDynamicSharedMemorySize, 163840);
  hipLaunchKernelGGL(mb, dim3(256), dim3(256), 163840, stream, (const unsigned short*)d_in[0], (const unsigned short*)d_in[1],
                     (const unsigned short*)d_in[2], (unsigned short*)d_out, (unsigned long long*)d_ws);
  hipLaunchKernelGGL(delay_kernel, dim3(1), dim3(1024), 0, stream, (const unsigned long long*)d_ws, SI, USEMAX, FACT);
  const int shm = 4 * TILEB;
  (void)hipFuncSetAttribute((const void*)attn_base, hipFuncAttributeMaxDynamicSharedMemorySize, shm);
  hipLaunchKernelGGL(attn_base, dim3(256), dim3(256), shm, stream, (const unsigned short*)d_in[0], (const unsigned short*)d_in[1],
                     (const unsigned short*)d_in[2], (unsigned short*)d_out);
}
```

```cpp
#define SI 0
#define USEMAX 0
#define FACT 0
#include <hip/hip_runtime.h>
#include <hip/hip_bf16.h>
#include <cstdint>

typedef short bf16x8 __attribute__((ext_vector_type(8)));
typedef short s16x4 __attribute__((ext_vector_type(4)));
typedef float f32x4 __attribute__((ext_vector_type(4)));
typedef unsigned u32x4 __attribute__((ext_vector_type(4)));
typedef unsigned u32x2 __attribute__((ext_vector_type(2)));
typedef __bf16 bf16x8_t __attribute__((ext_vector_type(8)));
typedef __bf16 bf16x2_t __attribute__((ext_vector_type(2)));
typedef float f32x2 __attribute__((ext_vector_type(2)));

constexpr int B_ = 8, HQ = 16, HKV = 8, SQ = 384, SK = 512, D = 256;
constexpr int TILE = 64, NT = SK / TILE;
constexpr int TILEB = TILE * D * 2;
typedef __attribute__((address_space(3))) char* lds_ptr;
#define STAMP(i) do { unsigned long long t_, r_; asm volatile("s_memtime %0\n\ts_memrealtime %1\n\ts_waitcnt lgkmcnt(0)" : "=s"(t_), "=s"(r_) :: "memory"); st[2*(i)] = t_; st[2*(i)+1] = r_; } while (0)
#define SBAR() __builtin_amdgcn_sched_barrier(0)

__device__ __forceinline__ unsigned cvtpk(float lo, float hi) {
  f32x2 v = {lo, hi}; bf16x2_t b = __builtin_convertvector(v, bf16x2_t); return __builtin_bit_cast(unsigned, b);
}
#define MFMA(a, b, c) __builtin_amdgcn_mfma_f32_16x16x32_bf16(__builtin_bit_cast(bf16x8_t, a), __builtin_bit_cast(bf16x8_t, b), c, 0, 0, 0)

__global__ __launch_bounds__(256, 1) void attn_base(const unsigned short* __restrict__ Q, const unsigned short* __restrict__ K,
                                                    const unsigned short* __restrict__ V, unsigned short* __restrict__ O, unsigned long long* ws) {
  unsigned long long st[24];
  STAMP(0);
  extern __shared__ __attribute__((aligned(16))) char lds[];
  const int tid = threadIdx.x, lane = tid & 63, wid = __builtin_amdgcn_readfirstlane(tid >> 6);
  const int l15 = lane & 15, g = lane >> 4;
  const int bid = blockIdx.x, xcd = bid & 7, idx = bid >> 3;
  const int grp = xcd * 8 + (idx >> 2), part = idx & 3;
  const long qrow0 = (long)grp * 768 + part * 192 + wid * 48;
  const char* Kg = (const char*)K + (long)grp * SK * D * 2;
  const char* Vg = (const char*)V + (long)grp * SK * D * 2;
  const unsigned lds0 = (unsigned)(uintptr_t)lds;
  const __amdgpu_buffer_rsrc_t rK = __builtin_amdgcn_make_buffer_rsrc((void*)Kg, (short)0, SK * D * 2, 0x00020000);
  const __amdgpu_buffer_rsrc_t rV = __builtin_amdgcn_make_buffer_rsrc((void*)Vg, (short)0, SK * D * 2, 0x00020000);
  unsigned voff[4];
#pragma unroll
  for (int i = 0; i < 4; ++i) { const int row = 2 * i + (lane >> 5); voff[i] = (lane >> 5) * 512 + (((lane & 31) ^ (2 * (row & 7))) * 16); }
  auto dma_k = [&](unsigned soff, unsigned dst) {
#pragma unroll
    for (int h = 0; h < 2; ++h) {
      const unsigned so = __builtin_amdgcn_readfirstlane(soff + wid * 8192 + h * 4096), m = __builtin_amdgcn_readfirstlane(dst + wid * 8192 + h * 4096);
      asm volatile("s_nop 4\n\ts_mov_b32 m0, %5\n\ts_nop 0\n\t"
                   "buffer_load_dwordx4 %0, %4, %6 offen lds\n\t"
                   "buffer_load_dwordx4 %1, %4, %6 offen offset:1024 lds\n\t"
                   "buffer_load_dwordx4 %2, %4, %6 offen offset:2048 lds\n\t"
                   "buffer_load_dwordx4 %3, %4, %6 offen offset:3072 lds"
                   :: "v"(voff[0]), "v"(voff[1]), "v"(voff[2]), "v"(voff[3]), "s"(rK), "s"(m), "s"(so) : "memory");
    }
  };
  const unsigned vvoff = (lane >> 3) * 512 + (((lane & 7) ^ ((lane >> 3) & 6)) * 16);
  auto dma_v = [&](int c, unsigned dst) {
#pragma unroll
    for (int i = 0; i < 16; ++i) {
      const unsigned piece = wid * 16 + i;
      const unsigned so = __builtin_amdgcn_readfirstlane(piece * 4096 + c * 128), m = __builtin_amdgcn_readfirstlane(dst + piece * 1024);
      asm volatile("s_nop 4\n\ts_mov_b32 m0, %2\n\ts_nop 0\n\t"
                   "buffer_load_dwordx4 %0, %1, %3 offen lds"
                   :: "v"(vvoff), "s"(rV), "s"(m), "s"(so) : "memory");
    }
  };
  unsigned kbase[4], vbase[4];
#pragma unroll
  for (int i = 0; i < 4; ++i) kbase[i] = l15 * 512 + (((4 * i + g) ^ (2 * (l15 & 7))) * 16);
  { const int q4 = (lane & 15) >> 2, p = lane & 3, row0 = 4 * g + q4;
#pragma unroll
    for (int i = 0; i < 4; ++i) vbase[i] = row0 * 128 + (((2 * i + (p >> 1)) ^ (row0 & 6)) * 16) + 8 * (p & 1); }
  dma_k(0, lds0);
  bf16x8 qf[3][8];
#pragma unroll
  for (int qb = 0; qb < 3; ++qb)
#pragma unroll
    for (int ks = 0; ks < 8; ++ks)
      qf[qb][ks] = *(const bf16x8*)(Q + (qrow0 + qb * 16 + l15) * D + ks * 32 + g * 8);
  float m[3], l[3] = {0.f, 0.f, 0.f};
  constexpr float C = 0.0625f * 1.4426950408889634f;
  u32x4 pf[3][16];
  const int rot = grp & 3;
#pragma unroll
  for (int t = 0; t < NT; ++t) {
    asm volatile("s_waitcnt vmcnt(0)\n\ts_barrier" ::: "memory");
    if (t == 0) STAMP(1);
    if (t == 1) STAMP(2);
    const int buf = t & 1;
    if (t + 1 < NT) dma_k((t + 1) * TILEB, lds0 + (buf ^ 1) * TILEB);
    else dma_v(rot, lds0 + 2 * TILEB);
    lds_ptr Ks = (lds_ptr)lds + buf * TILEB;
    f32x4 s[3][4];
#pragma unroll
    for (int kb = 0; kb < 4; ++kb) {
#pragma unroll
      for (int qb = 0; qb < 3; ++qb) s[qb][kb] = f32x4{0.f, 0.f, 0.f, 0.f};
#pragma unroll
      for (int ks = 0; ks < 8; ++ks) {
        bf16x8 kf = *(const __attribute__((address_space(3))) bf16x8*)(Ks + kbase[ks & 3] + kb * 8192 + (ks >> 2) * 256);
#pragma unroll
        for (int qb = 0; qb < 3; ++qb) s[qb][kb] = MFMA(kf, qf[qb][ks], s[qb][kb]);
      }
      SBAR();
    }
#pragma unroll
    for (int qb = 0; qb < 3; ++qb) {
      float mx = s[qb][0][0];
#pragma unroll
      for (int kb = 0; kb < 4; ++kb)
#pragma unroll
        for (int r = 0; r < 4; ++r) mx = fmaxf(mx, s[qb][kb][r]);
      if (t == 0) {
        mx = fmaxf(mx, __shfl_xor(mx, 16));
        mx = fmaxf(mx, __shfl_xor(mx, 32));
        m[qb] = mx;
      } else if (__builtin_expect(__any((mx - m[qb]) * C > 16.f), 0)) {
        mx = fmaxf(mx, __shfl_xor(mx, 16));
        mx = fmaxf(mx, __shfl_xor(mx, 32));
        const float dl = ceilf(fmaxf((mx - m[qb]) * C, 0.f));
        const float f = __builtin_amdgcn_exp2f(-dl);
        m[qb] += dl / C;
        l[qb] *= f;
#pragma unroll
        for (int j = 0; j < 2 * t; ++j)
#pragma unroll
          for (int e = 0; e < 4; ++e) {
            const unsigned w = pf[qb][j][e];
            pf[qb][j][e] = cvtpk(__uint_as_float(w << 16) * f, __uint_as_float(w & 0xffff0000u) * f);
          }
      }
      const float mc = -m[qb] * C;
      float sum = 0.f;
#pragma unroll
      for (int kb = 0; kb < 4; ++kb)
#pragma unroll
        for (int r = 0; r < 4; ++r) { s[qb][kb][r] = __builtin_amdgcn_exp2f(fmaf(s[qb][kb][r], C, mc)); sum += s[qb][kb][r]; }
      l[qb] += sum;
#pragma unroll
      for (int h = 0; h < 2; ++h) {
        u32x4 w = {cvtpk(s[qb][2 * h][0], s[qb][2 * h][1]), cvtpk(s[qb][2 * h][2], s[qb][2 * h][3]),
                   cvtpk(s[qb][2 * h + 1][0], s[qb][2 * h + 1][1]), cvtpk(s[qb][2 * h + 1][2], s[qb][2 * h + 1][3])};
        pf[qb][2 * t + h] = w;
      }
    }
    asm volatile("s_waitcnt lgkmcnt(0)" ::: "memory");
    SBAR();
  }
  STAMP(3);
  float inv[3];
#pragma unroll
  for (int qb = 0; qb < 3; ++qb) {
    float lt = l[qb];
    lt += __shfl_xor(lt, 16);
    lt += __shfl_xor(lt, 32);
    inv[qb] = 1.0f / lt;
  }
  for (int ci = 0; ci < 4; ++ci) {
    const int c = (ci + rot) & 3;
    asm volatile("s_waitcnt vmcnt(0)\n\ts_barrier" ::: "memory");
    const int buf = (ci & 1) ^ 1;
    if (ci + 1 < 4) dma_v((c + 1) & 3, lds0 + (buf ^ 1) * 2 * TILEB);
    lds_ptr Vs = (lds_ptr)lds + buf * 2 * TILEB;
    f32x4 o[3][4];
#pragma unroll
    for (int a = 0; a < 4; ++a)
#pragma unroll
      for (int qb = 0; qb < 3; ++qb) o[qb][a] = f32x4{0.f, 0.f, 0.f, 0.f};
#pragma unroll
    for (int j = 0; j < 16; ++j) {
#pragma unroll
      for (int a = 0; a < 4; ++a) {
        s16x4 lo = __builtin_amdgcn_ds_read_tr16_b64_v4i16((__attribute__((address_space(3))) s16x4*)(Vs + vbase[a] + j * 4096));
        s16x4 hi = __builtin_amdgcn_ds_read_tr16_b64_v4i16((__attribute__((address_space(3))) s16x4*)(Vs + vbase[a] + j * 4096 + 2048));
        bf16x8 vf = __builtin_shufflevector(lo, hi, 0, 1, 2, 3, 4, 5, 6, 7);
#pragma unroll
        for (int qb = 0; qb < 3; ++qb) o[qb][a] = MFMA(vf, pf[qb][j], o[qb][a]);
      }
      if ((j & 1) == 1) SBAR();
    }
#pragma unroll
    for (int qb = 0; qb < 3; ++qb) {
      unsigned short* orow = O + (qrow0 + qb * 16 + l15) * D + c * 64;
#pragma unroll
      for (int a = 0; a < 4; ++a) {
        u32x2 w = {cvtpk(o[qb][a][0] * inv[qb], o[qb][a][1] * inv[qb]), cvtpk(o[qb][a][2] * inv[qb], o[qb][a][3] * inv[qb])};
        *(u32x2*)(orow + a * 16 + g * 4) = w;
      }
    }
    asm volatile("s_waitcnt lgkmcnt(0)" ::: "memory");
  }
  asm volatile("s_waitcnt vmcnt(0)" ::: "memory");
  STAMP(4);
  if (lane == 0) {
#pragma unroll
    for (int i = 0; i < 10; ++i) ws[(bid * 4 + wid) * 64 + i] = st[i];
  }
}

__global__ __launch_bounds__(1024) void delay_kernel(const unsigned long long* ws, int si, int usemax, int fact) {
  __shared__ unsigned long long mn[1024]; __shared__ unsigned long long v[1024];
  const int t = threadIdx.x;
  mn[t] = ws[t * 64 + 1]; __syncthreads();
  for (int o = 512; o > 0; o >>= 1) { if (t < o) mn[t] = mn[t] < mn[t + o] ? mn[t] : mn[t + o]; __syncthreads(); }
  v[t] = usemax == 2 ? ws[t * 64 + 2 * si] - ws[t * 64] : ws[t * 64 + 2 * si + 1] - mn[0]; __syncthreads();
  for (int o = 512; o > 0; o >>= 1) { if (t < o) v[t] = usemax == 1 ? (v[t] > v[t + o] ? v[t] : v[t + o]) : v[t] + v[t + o]; __syncthreads(); }
  unsigned long long d = usemax == 1 ? v[0] * fact : usemax == 2 ? v[0] * fact / (1024 * 24) : v[0] * fact / 1024;
  if (d > 100000) d = 100000;
  const unsigned long long t0 = __builtin_amdgcn_s_memrealtime();
  while (__builtin_amdgcn_s_memrealtime() - t0 < d) __builtin_amdgcn_s_sleep(2);
}
extern "C" void kernel_launch(void* const* d_in, const int* in_sizes, int n_in, void* d_out, int out_size, void* d_ws, size_t ws_size,
                              hipStream_t stream) {
  const int shm = 4 * TILEB;
  (void)hipFuncSetAttribute((const void*)attn_base, hipFuncAttributeMaxDynamicSharedMemorySize, shm);
  hipLaunchKernelGGL(attn_base, dim3(256), dim3(256), shm, stream, (const unsigned short*)d_in[0], (const unsigned short*)d_in[1],
                     (const unsigned short*)d_in[2], (unsigned short*)d_out, (unsigned long long*)d_ws);
  hipLaunchKernelGGL(delay_kernel, dim3(1), dim3(1024), 0, stream, (const unsigned long long*)d_ws, SI, USEMAX, FACT);
}
```

```cpp
#define TINY 0
#define MODE 0
#include <hip/hip_runtime.h>
#include <hip/hip_bf16.h>
#include <cstdint>

typedef short bf16x8 __attribute__((ext_vector_type(8)));
typedef short s16x4 __attribute__((ext_vector_type(4)));
typedef float f32x4 __attribute__((ext_vector_type(4)));
typedef unsigned u32x4 __attribute__((ext_vector_type(4)));
typedef unsigned u32x2 __attribute__((ext_vector_type(2)));
typedef __bf16 bf16x8_t __attribute__((ext_vector_type(8)));
typedef __bf16 bf16x2_t __attribute__((ext_vector_type(2)));
typedef float f32x2 __attribute__((ext_vector_type(2)));

constexpr int B_ = 8, HQ = 16, HKV = 8, SQ = 384, SK = 512, D = 256;
constexpr int TILE = 64, NT = SK / TILE;
constexpr int TILEB = TILE * D * 2;
typedef __attribute__((address_space(3))) char* lds_ptr;
#define STAMP(i) do { unsigned long long t_, r_; asm volatile("s_memtime %0\n\ts_memrealtime %1\n\ts_waitcnt lgkmcnt(0)" : "=s"(t_), "=s"(r_) :: "memory"); st[2*(i)] = t_; st[2*(i)+1] = r_; } while (0)
#define SBAR() __builtin_amdgcn_sched_barrier(0)

__device__ __forceinline__ unsigned cvtpk(float lo, float hi) {
  f32x2 v = {lo, hi}; bf16x2_t b = __builtin_convertvector(v, bf16x2_t); return __builtin_bit_cast(unsigned, b);
}
#define MFMA(a, b, c) __builtin_amdgcn_mfma_f32_16x16x32_bf16(__builtin_bit_cast(bf16x8_t, a), __builtin_bit_cast(bf16x8_t, b), c, 0, 0, 0)

__global__ __launch_bounds__(256, 1) void attn_base(const unsigned short* __restrict__ Q, const unsigned short* __restrict__ K,
                                                    const unsigned short* __restrict__ V, unsigned short* __restrict__ O, unsigned long long* ws) {
  unsigned long long st[24];
  STAMP(0);
  extern __shared__ __attribute__((aligned(16))) char lds[];
  const int tid = threadIdx.x, lane = tid & 63, wid = __builtin_amdgcn_readfirstlane(tid >> 6);
  const int l15 = lane & 15, g = lane >> 4;
  const int bid = blockIdx.x, xcd = bid & 7, idx = bid >> 3;
  const int grp = xcd * 8 + (idx >> 2), part = idx & 3;
  const long qrow0 = (long)grp * 768 + part * 192 + wid * 48;
  const char* Kg = (const char*)K + (long)grp * SK * D * 2;
  const char* Vg = (const char*)V + (long)grp * SK * D * 2;
  const unsigned lds0 = (unsigned)(uintptr_t)lds;
  const __amdgpu_buffer_rsrc_t rK = __builtin_amdgcn_make_buffer_rsrc((void*)Kg, (short)0, SK * D * 2, 0x00020000);
  const __amdgpu_buffer_rsrc_t rV = __builtin_amdgcn_make_buffer_rsrc((void*)Vg, (short)0, SK * D * 2, 0x00020000);
  unsigned voff[4];
#pragma unroll
  for (int i = 0; i < 4; ++i) { const int row = 2 * i + (lane >> 5); voff[i] = (lane >> 5) * 512 + (((lane & 31) ^ (2 * (row & 7))) * 16); }
  auto dma_k = [&](unsigned soff, unsigned dst) {
#pragma unroll
    for (int h = 0; h < 2; ++h) {
      const unsigned so = __builtin_amdgcn_readfirstlane(soff + wid * 8192 + h * 4096), m = __builtin_amdgcn_readfirstlane(dst + wid * 8192 + h * 4096);
      asm volatile("s_nop 4\n\ts_mov_b32 m0, %5\n\ts_nop 0\n\t"
                   "buffer_load_dwordx4 %0, %4, %6 offen lds\n\t"
                   "buffer_load_dwordx4 %1, %4, %6 offen offset:1024 lds\n\t"
                   "buffer_load_dwordx4 %2, %4, %6 offen offset:2048 lds\n\t"
                   "buffer_load_dwordx4 %3, %4, %6 offen offset:3072 lds"
                   :: "v"(voff[0]), "v"(voff[1]), "v"(voff[2]), "v"(voff[3]), "s"(rK), "s"(m), "s"(so) : "memory");
    }
  };
  const unsigned vvoff = (lane >> 3) * 512 + (((lane & 7) ^ ((lane >> 3) & 6)) * 16);
  auto dma_v = [&](int c, unsigned dst) {
#pragma unroll
    for (int i = 0; i < 16; ++i) {
      const unsigned piece = wid * 16 + i;
      const unsigned so = __builtin_amdgcn_readfirstlane(piece * 4096 + c * 128), m = __builtin_amdgcn_readfirstlane(dst + piece * 1024);
      asm volatile("s_nop 4\n\ts_mov_b32 m0, %2\n\ts_nop 0\n\t"
                   "buffer_load_dwordx4 %0, %1, %3 offen lds"
                   :: "v"(vvoff), "s"(rV), "s"(m), "s"(so) : "memory");
    }
  };
  unsigned kbase[4], vbase[4];
#pragma unroll
  for (int i = 0; i < 4; ++i) kbase[i] = l15 * 512 + (((4 * i + g) ^ (2 * (l15 & 7))) * 16);
  { const int q4 = (lane & 15) >> 2, p = lane & 3, row0 = 4 * g + q4;
#pragma unroll
    for (int i = 0; i < 4; ++i) vbase[i] = row0 * 128 + (((2 * i + (p >> 1)) ^ (row0 & 6)) * 16) + 8 * (p & 1); }
  dma_k(0, lds0);
  bf16x8 qf[3][8];
#pragma unroll
  for (int qb = 0; qb < 3; ++qb)
#pragma unroll
    for (int ks = 0; ks < 8; ++ks)
      qf[qb][ks] = *(const bf16x8*)(Q + (qrow0 + qb * 16 + l15) * D + ks * 32 + g * 8);
  float m[3], l[3] = {0.f, 0.f, 0.f};
  constexpr float C = 0.0625f * 1.4426950408889634f;
  u32x4 pf[3][16];
  const int rot = grp & 3;
#pragma unroll
  for (int t = 0; t < NT; ++t) {
    asm volatile("s_waitcnt vmcnt(0)\n\ts_barrier" ::: "memory");
    if (t == 0) STAMP(1);
    if (t == 1) STAMP(2);
    const int buf = t & 1;
    if (t + 1 < NT) dma_k((t + 1) * TILEB, lds0 + (buf ^ 1) * TILEB);
    else dma_v(rot, lds0 + 2 * TILEB);
    lds_ptr Ks = (lds_ptr)lds + buf * TILEB;
    f32x4 s[3][4];
#pragma unroll
    for (int kb = 0; kb < 4; ++kb) {
#pragma unroll
      for (int qb = 0; qb < 3; ++qb) s[qb][kb] = f32x4{0.f, 0.f, 0.f, 0.f};
#pragma unroll
      for (int ks = 0; ks < 8; ++ks) {
        bf16x8 kf = *(const __attribute__((address_space(3))) bf16x8*)(Ks + kbase[ks & 3] + kb * 8192 + (ks >> 2) * 256);
#pragma unroll
        for (int qb = 0; qb < 3; ++qb) s[qb][kb] = MFMA(kf, qf[qb][ks], s[qb][kb]);
      }
      SBAR();
    }
#pragma unroll
    for (int qb = 0; qb < 3; ++qb) {
      float mx = s[qb][0][0];
#pragma unroll
      for (int kb = 0; kb < 4; ++kb)
#pragma unroll
        for (int r = 0; r < 4; ++r) mx = fmaxf(mx, s[qb][kb][r]);
      if (t == 0) {
        mx = fmaxf(mx, __shfl_xor(mx, 16));
        mx = fmaxf(mx, __shfl_xor(mx, 32));
        m[qb] = mx;
      } else if (__builtin_expect(__any((mx - m[qb]) * C > 16.f), 0)) {
        mx = fmaxf(mx, __shfl_xor(mx, 16));
        mx = fmaxf(mx, __shfl_xor(mx, 32));
        const float dl = ceilf(fmaxf((mx - m[qb]) * C, 0.f));
        const float f = __builtin_amdgcn_exp2f(-dl);
        m[qb] += dl / C;
        l[qb] *= f;
#pragma unroll
        for (int j = 0; j < 2 * t; ++j)
#pragma unroll
          for (int e = 0; e < 4; ++e) {
            const unsigned w = pf[qb][j][e];
            pf[qb][j][e] = cvtpk(__uint_as_float(w << 16) * f, __uint_as_float(w & 0xffff0000u) * f);
          }
      }
      const float mc = -m[qb] * C;
      float sum = 0.f;
#pragma unroll
      for (int kb = 0; kb < 4; ++kb)
#pragma unroll
        for (int r = 0; r < 4; ++r) { s[qb][kb][r] = __builtin_amdgcn_exp2f(fmaf(s[qb][kb][r], C, mc)); sum += s[qb][kb][r]; }
      l[qb] += sum;
#pragma unroll
      for (int h = 0; h < 2; ++h) {
        u32x4 w = {cvtpk(s[qb][2 * h][0], s[qb][2 * h][1]), cvtpk(s[qb][2 * h][2], s[qb][2 * h][3]),
                   cvtpk(s[qb][2 * h + 1][0], s[qb][2 * h + 1][1]), cvtpk(s[qb][2 * h + 1][2], s[qb][2 * h + 1][3])};
        pf[qb][2 * t + h] = w;
      }
    }
    asm volatile("s_waitcnt lgkmcnt(0)" ::: "memory");
    SBAR();
  }
  STAMP(3);
  float inv[3];
#pragma unroll
  for (int qb = 0; qb < 3; ++qb) {
    float lt = l[qb];
    lt += __shfl_xor(lt, 16);
    lt += __shfl_xor(lt, 32);
    inv[qb] = 1.0f / lt;
  }
  for (int ci = 0; ci < 4; ++ci) {
    const int c = (ci + rot) & 3;
    asm volatile("s_waitcnt vmcnt(0)\n\ts_barrier" ::: "memory");
    const int buf = (ci & 1) ^ 1;
    if (ci + 1 < 4) dma_v((c + 1) & 3, lds0 + (buf ^ 1) * 2 * TILEB);
    lds_ptr Vs = (lds_ptr)lds + buf * 2 * TILEB;
    f32x4 o[3][4];
#pragma unroll
    for (int a = 0; a < 4; ++a)
#pragma unroll
      for (int qb = 0; qb < 3; ++qb) o[qb][a] = f32x4{0.f, 0.f, 0.f, 0.f};
#pragma unroll
    for (int j = 0; j < 16; ++j) {
#pragma unroll
      for (int a = 0; a < 4; ++a) {
        s16x4 lo = __builtin_amdgcn_ds_read_tr16_b64_v4i16((__attribute__((address_space(3))) s16x4*)(Vs + vbase[a] + j * 4096));
        s16x4 hi = __builtin_amdgcn_ds_read_tr16_b64_v4i16((__attribute__((address_space(3))) s16x4*)(Vs + vbase[a] + j * 4096 + 2048));
        bf16x8 vf = __builtin_shufflevector(lo, hi, 0, 1, 2, 3, 4, 5, 6, 7);
#pragma unroll
        for (int qb = 0; qb < 3; ++qb) o[qb][a] = MFMA(vf, pf[qb][j], o[qb][a]);
      }
      if ((j & 1) == 1) SBAR();
    }
#pragma unroll
    for (int qb = 0; qb < 3; ++qb) {
      unsigned short* orow = O + (qrow0 + qb * 16 + l15) * D + c * 64;
#pragma unroll
      for (int a = 0; a < 4; ++a) {
        u32x2 w = {cvtpk(o[qb][a][0] * inv[qb], o[qb][a][1] * inv[qb]), cvtpk(o[qb][a][2] * inv[qb], o[qb][a][3] * inv[qb])};
        *(u32x2*)(orow + a * 16 + g * 4) = w;
      }
    }
    asm volatile("s_waitcnt lgkmcnt(0)" ::: "memory");
  }
  asm volatile("s_waitcnt vmcnt(0)" ::: "memory");
  STAMP(4);
  if (lane == 0) {
#pragma unroll
    for (int i = 0; i < 10; ++i) ws[(bid * 4 + wid) * 64 + i] = st[i];
  }
}

__global__ void tiny_kernel(unsigned long long* ws) { if (threadIdx.x == 0 && blockIdx.x == 0) ws[1024 * 64] = __builtin_amdgcn_s_memrealtime(); }
__global__ __launch_bounds__(1024) void delay_kernel(const unsigned long long* ws, int mode, int fact) {
  const unsigned long long t0 = __builtin_amdgcn_s_memrealtime();
  __shared__ unsigned long long mn[1024]; __shared__ unsigned long long v[1024];
  const int t = threadIdx.x;
  mn[t] = ws[t * 64 + 1]; v[t] = ws[t * 64 + 9]; __syncthreads();
  for (int o = 512; o > 0; o >>= 1) { if (t < o) { mn[t] = mn[t] < mn[t + o] ? mn[t] : mn[t + o]; v[t] = v[t] > v[t + o] ? v[t] : v[t + o]; } __syncthreads(); }
  unsigned long long d = mode == 1 ? t0 - v[0] : mode == 2 ? mn[0] - ws[1024 * 64] : mode == 3 ? v[0] - mn[0] : 0;
  d *= fact;
  if (d > 100000) d = 100000;
  const unsigned long long t1 = __builtin_amdgcn_s_memrealtime();
  while (__builtin_amdgcn_s_memrealtime() - t1 < d) __builtin_amdgcn_s_sleep(2);
}
extern "C" void kernel_launch(void* const* d_in, const int* in_sizes, int n_in, void* d_out, int out_size, void* d_ws, size_t ws_size,
                              hipStream_t stream) {
  const int shm = 4 * TILEB;
  (void)hipFuncSetAttribute((const void*)attn_base, hipFuncAttributeMaxDynamicSharedMemorySize, shm);
  hipLaunchKernelGGL(attn_base, dim3(256), dim3(256), shm, stream, (const unsigned short*)d_in[0], (const unsigned short*)d_in[1],
                     (const unsigned short*)d_in[2], (unsigned short*)d_out, (unsigned long long*)d_ws);
  hipLaunchKernelGGL(delay_kernel, dim3(1), dim3(1024), 0, stream, (const unsigned long long*)d_ws, MODE, 10);
}
```

```cpp
#include <hip/hip_runtime.h>
#include <hip/hip_bf16.h>
#include <cstdint>

#define STAMPS 0

__global__ __launch_bounds__(256, 1) void gqa_attn_kernel(const unsigned short* Q, const unsigned short* K,
                                                          const unsigned short* V, unsigned short* O
) {
  __shared__ __attribute__((aligned(1024))) char lds[163840];
  asm volatile(
    "s_load_dwordx8 s[64:71], %0, 0\n\t"
    "v_readfirstlane_b32 s7, %2\n\t"
    "s_lshr_b32 s8, %1, 3\n\t"
    "s_and_b32 s5, %1, 7\n\t"
    "s_lshl_b32 s5, s5, 5\n\t"
    "s_add_u32 s5, s5, s8\n\t"
    "s_mul_i32 s4, s5, 98304\n\t"
    "s_lshr_b32 s7, s7, 6\n\t"
    "s_lshr_b32 s5, s5, 2\n\t"
    "s_lshl_b32 s5, s5, 18\n\t"
    "s_mul_i32 s6, s7, 8192\n\t"
    "s_add_u32 s4, s4, s6\n\t"
    "s_mov_b64 s[50:51], 0\n\t"
    "Lstart_%=:\n\t"
    "v_mbcnt_lo_u32_b32 v30, -1, 0\n\t"
    "v_mbcnt_hi_u32_b32 v30, -1, v30\n\t"
    "s_mov_b32 s22, 0x40000\n\t"
    "s_mov_b32 s23, 0x20000\n\t"
    "s_lshl_b32 s41, s7, 13\n\t"
    "s_add_u32 s40, s41, %3\n\t"
    "v_lshlrev_b32 v31, 4, v30\n\t"
    "v_and_b32 v12, 32, v30\n\t"
    "v_xor_b32 v12, v12, v31\n\t"
    "v_xor_b32 v13, 64, v12\n\t"
    "v_xor_b32 v14, 128, v12\n\t"
    "v_xor_b32 v15, 192, v12\n\t"
    "s_getpc_b64 s[52:53]\n\t"
    "Lpc_%=:\n\t"
    "s_mov_b32 s54, Lend_%=-Lpc_%=\n\t"
    "s_mov_b32 s55, 0x20000\n\t"
    "s_and_b32 s53, s53, 0xffff\n\t"
    "v_and_b32 v29, 3, v30\n\t"
    "v_lshlrev_b32 v29, 7, v29\n\t"
    "s_lshl2_add_u32 s35, s8, s7\n\t"
    "s_lshl_b32 s35, s35, 9\n\t"
    "s_mov_b32 s30, 73728\n\t"
    "s_mov_b32 s31, 0x20000\n\t"
    "s_add_u32 m0, s40, 0\n\t"
    "s_add_u32 s46, s41, 0\n\t"
    "buffer_load_dword v28, v29, s[52:55], s35 offen\n\t"
    "s_waitcnt lgkmcnt(0)\n\t"
    "s_add_u32 s20, s66, s5\n\t"
    "s_addc_u32 s21, s67, 0\n\t"
    "s_and_b32 s21, s21, 0xffff\n\t"
    "buffer_load_dwordx4 v12, s[20:23], s46 offen offset:0 nt lds\n\t"
    "buffer_load_dwordx4 v13, s[20:23], s46 offen offset:1024 nt lds\n\t"
    "buffer_load_dwordx4 v14, s[20:23], s46 offen offset:2048 nt lds\n\t"
    "buffer_load_dwordx4 v15, s[20:23], s46 offen offset:3072 nt lds\n\t"
    "s_add_u32 m0, s40, 4096\n\t"
    "s_add_u32 s47, s41, 4096\n\t"
    "buffer_load_dwordx4 v12, s[20:23], s47 offen offset:0 nt lds\n\t"
    "buffer_load_dwordx4 v13, s[20:23], s47 offen offset:1024 nt lds\n\t"
    "buffer_load_dwordx4 v14, s[20:23], s47 offen offset:2048 nt lds\n\t"
    "buffer_load_dwordx4 v15, s[20:23], s47 offen offset:3072 nt lds\n\t"
    "s_add_u32 s28, s64, s4\n\t"
    "s_addc_u32 s29, s65, 0\n\t"
    "s_and_b32 s29, s29, 0xffff\n\t"
    "s_add_u32 m0, s40, 65536\n\t"
    "s_mov_b32 s46, 0\n\t"
    "buffer_load_dwordx4 v12, s[28:31], s46 offen offset:0 nt lds\n\t"
    "buffer_load_dwordx4 v13, s[28:31], s46 offen offset:1024 nt lds\n\t"
    "buffer_load_dwordx4 v14, s[28:31], s46 offen offset:2048 nt lds\n\t"
    "buffer_load_dwordx4 v15, s[28:31], s46 offen offset:3072 nt lds\n\t"
    "v_and_b32 v20, 15, v30\n\t"
    "v_lshlrev_b32 v20, 9, v20\n\t"
    "v_and_b32 v31, 48, v30\n\t"
    "v_or_b32 v20, v20, v31\n\t"
    "v_and_b32 v31, 7, v30\n\t"
    "v_lshlrev_b32 v31, 5, v31\n\t"
    "v_xor_b32 v31, v31, v20\n\t"
    "s_mov_b32 s57, %3\n\t"
    "v_mov_b32 v4, v31\n\t"
    "v_xor_b32 v5, 64, v31\n\t"
    "v_xor_b32 v6, 128, v31\n\t"
    "v_xor_b32 v7, 192, v31\n\t"
    "v_add_u32 v4, s57, v4\n\t"
    "v_add_u32 v5, s57, v5\n\t"
    "v_add_u32 v6, s57, v6\n\t"
    "v_add_u32 v7, s57, v7\n\t"
    "s_add_u32 s56, s41, 65536\n\t"
    "v_add_u32 v32, s56, v4\n\t"
    "v_add_u32 v33, s56, v5\n\t"
    "v_add_u32 v34, s56, v6\n\t"
    "v_add_u32 v35, s56, v7\n\t"
    "v_add_u32 v36, 0x10000, v32\n\t"
    "v_add_u32 v37, 0x10000, v33\n\t"
    "v_add_u32 v38, 0x10000, v34\n\t"
    "v_add_u32 v39, 0x10000, v35\n\t"
    "v_add_u32 v40, 0x10000, v4\n\t"
    "v_add_u32 v41, 0x10000, v5\n\t"
    "v_add_u32 v42, 0x10000, v6\n\t"
    "v_add_u32 v43, 0x10000, v7\n\t"
    "s_add_u32 s24, s68, s5\n\t"
    "s_addc_u32 s25, s69, 0\n\t"
    "s_and_b32 s25, s25, 0xffff\n\t"
    "s_mov_b32 s26, 0x40000\n\t"
    "s_mov_b32 s27, 0x20000\n\t"
    "s_add_u32 s36, s70, s4\n\t"
    "s_addc_u32 s37, s71, 0\n\t"
    "s_and_b32 s37, s37, 0xffff\n\t"
    "s_mov_b32 s38, 73728\n\t"
    "s_mov_b32 s39, 0x20000\n\t"
    "v_lshrrev_b32 v31, 3, v30\n\t"
    "v_and_b32 v17, 6, v31\n\t"
    "v_and_b32 v18, 7, v30\n\t"
    "v_xor_b32 v17, v17, v18\n\t"
    "v_lshlrev_b32 v17, 4, v17\n\t"
    "v_lshl_add_u32 v16, v31, 9, v17\n\t"
    "v_add_u32 v17, 3072, v16\n\t"
    "v_add_u32 v18, 6144, v16\n\t"
    "v_add_u32 v19, 9216, v16\n\t"
    "v_lshrrev_b32 v31, 2, v30\n\t"
    "v_and_b32 v9, 3, v30\n\t"
    "v_lshlrev_b32 v9, 3, v9\n\t"
    "v_lshl_add_u32 v8, v31, 7, v9\n\t"
    "v_and_b32 v31, 6, v31\n\t"
    "v_lshlrev_b32 v31, 4, v31\n\t"
    "v_xor_b32 v8, v8, v31\n\t"
    "v_xor_b32 v9, 32, v8\n\t"
    "v_xor_b32 v10, 64, v8\n\t"
    "v_xor_b32 v11, 96, v8\n\t"
    "s_add_u32 s56, s57, 65536\n\t"
    "v_add_u32 v8, s56, v8\n\t"
    "v_add_u32 v9, s56, v9\n\t"
    "v_add_u32 v10, s56, v10\n\t"
    "v_add_u32 v11, s56, v11\n\t"
    "v_and_b32 v21, 15, v30\n\t"
    "v_lshlrev_b32 v21, 9, v21\n\t"
    "v_and_b32 v31, 16, v30\n\t"
    "v_lshl_add_u32 v21, v31, 1, v21\n\t"
    "v_lshrrev_b32 v31, 1, v30\n\t"
    "v_and_b32 v31, 16, v31\n\t"
    "v_add_u32 v21, v21, v31\n\t"
    "s_lshl_b32 s35, s7, 15\n\t"
    "s_lshr_b32 s56, s8, 0\n\t"
    "s_add_u32 s56, s56, 0\n\t"
    "s_and_b32 s56, s56, 3\n\t"
    "s_lshl_b32 s56, s56, 7\n\t"
    "s_add_u32 s42, s35, s56\n\t"
    "s_add_u32 s64, s56, 0\n\t"
    "s_add_u32 s65, s56, 32768\n\t"
    "s_add_u32 s66, s56, 65536\n\t"
    "s_lshr_b32 s56, s8, 0\n\t"
    "s_add_u32 s56, s56, 1\n\t"
    "s_and_b32 s56, s56, 3\n\t"
    "s_lshl_b32 s56, s56, 7\n\t"
    "s_add_u32 s43, s35, s56\n\t"
    "s_add_u32 s67, s56, 0\n\t"
    "s_add_u32 s68, s56, 32768\n\t"
    "s_add_u32 s69, s56, 65536\n\t"
    "s_lshr_b32 s56, s8, 0\n\t"
    "s_add_u32 s56, s56, 2\n\t"
    "s_and_b32 s56, s56, 3\n\t"
    "s_lshl_b32 s56, s56, 7\n\t"
    "s_add_u32 s44, s35, s56\n\t"
    "s_add_u32 s70, s56, 0\n\t"
    "s_add_u32 s71, s56, 32768\n\t"
    "s_add_u32 s72, s56, 65536\n\t"
    "s_lshr_b32 s56, s8, 0\n\t"
    "s_add_u32 s56, s56, 3\n\t"
    "s_and_b32 s56, s56, 3\n\t"
    "s_lshl_b32 s56, s56, 7\n\t"
    "s_add_u32 s45, s35, s56\n\t"
    "s_add_u32 s73, s56, 0\n\t"
    "s_add_u32 s74, s56, 32768\n\t"
    "s_add_u32 s75, s56, 65536\n\t"
    "s_mov_b32 s34, 0x3db8aa3b\n\t"
    "v_mov_b32 v31, 0x3f803f80\n\t"
    "v_accvgpr_write_b32 a160, v31\n\t"
    "v_accvgpr_write_b32 a161, v31\n\t"
    "v_accvgpr_write_b32 a162, v31\n\t"
    "v_accvgpr_write_b32 a163, v31\n\t"
    "s_add_u32 m0, s40, 69632\n\t"
    "s_mov_b32 s47, 4096\n\t"
    "buffer_load_dwordx4 v12, s[28:31], s47 offen offset:0 nt lds\n\t"
    "buffer_load_dwordx4 v13, s[28:31], s47 offen offset:1024 nt lds\n\t"
    "buffer_load_dwordx4 v14, s[28:31], s47 offen offset:2048 nt lds\n\t"
    "buffer_load_dwordx4 v15, s[28:31], s47 offen offset:3072 nt lds\n\t"
    "s_waitcnt vmcnt(8)\n\t"
    "s_barrier\n\t"
    "ds_read_b128 v[44:47], v4 offset:0\n\t"
    "ds_read_b128 v[48:51], v5 offset:0\n\t"
    "ds_read_b128 v[52:55], v6 offset:0\n\t"
    "ds_read_b128 v[56:59], v7 offset:0\n\t"
    "ds_read_b128 v[60:63], v4 offset:256\n\t"
    "ds_read_b128 v[64:67], v5 offset:256\n\t"
    "ds_read_b128 v[68:71], v6 offset:256\n\t"
    "ds_read_b128 v[72:75], v7 offset:256\n\t"
    "s_add_u32 m0, s40, 32768\n\t"
    "s_add_u32 s46, s41, 32768\n\t"
    "buffer_load_dwordx4 v12, s[20:23], s46 offen offset:0 nt lds\n\t"
    "buffer_load_dwordx4 v13, s[20:23], s46 offen offset:1024 nt lds\n\t"
    "buffer_load_dwordx4 v14, s[20:23], s46 offen offset:2048 nt lds\n\t"
    "buffer_load_dwordx4 v15, s[20:23], s46 offen offset:3072 nt lds\n\t"
    "s_add_u32 m0, s40, 36864\n\t"
    "s_add_u32 s47, s41, 36864\n\t"
    "buffer_load_dwordx4 v12, s[20:23], s47 offen offset:0 nt lds\n\t"
    "buffer_load_dwordx4 v13, s[20:23], s47 offen offset:1024 nt lds\n\t"
    "buffer_load_dwordx4 v14, s[20:23], s47 offen offset:2048 nt lds\n\t"
    "buffer_load_dwordx4 v15, s[20:23], s47 offen offset:3072 nt lds\n\t"
    "s_branch Lbody_%=\n\t"
    "Lret_%=:\n\t"
    "s_setpc_b64 s[58:59]\n\t"
    "Lrare_%=:\n\t"
    "s_waitcnt vmcnt(0) lgkmcnt(0)\n\t"
    "s_barrier\n\t"
    "v_mbcnt_lo_u32_b32 v30, -1, 0\n\t"
    "v_mbcnt_hi_u32_b32 v30, -1, v30\n\t"
    "v_and_b32 v20, 15, v30\n\t"
    "v_lshlrev_b32 v20, 9, v20\n\t"
    "v_and_b32 v30, 48, v30\n\t"
    "v_or_b32 v20, v20, v30\n\t"
    "v_mov_b32 v1, 0xff800000\n\t"
    "v_mov_b32 v2, 0xff800000\n\t"
    "v_mov_b32 v3, 0xff800000\n\t"
    "s_mov_b32 s56, 0\n\t"
    "Lmax_%=:\n\t"
    "buffer_load_dwordx4 a[96:99], v20, s[20:23], s56 offen offset:0\n\t"
    "buffer_load_dwordx4 a[100:103], v20, s[20:23], s56 offen offset:64\n\t"
    "buffer_load_dwordx4 a[104:107], v20, s[20:23], s56 offen offset:128\n\t"
    "buffer_load_dwordx4 a[108:111], v20, s[20:23], s56 offen offset:192\n\t"
    "buffer_load_dwordx4 a[112:115], v20, s[20:23], s56 offen offset:256\n\t"
    "buffer_load_dwordx4 a[116:119], v20, s[20:23], s56 offen offset:320\n\t"
    "buffer_load_dwordx4 a[120:123], v20, s[20:23], s56 offen offset:384\n\t"
    "buffer_load_dwordx4 a[124:127], v20, s[20:23], s56 offen offset:448\n\t"
    "s_waitcnt vmcnt(0)\n\t"
    "v_mfma_f32_16x16x32_bf16 v[32:35], a[96:99], a[0:3], 0\n\t"
    "v_mfma_f32_16x16x32_bf16 v[32:35], a[100:103], a[4:7], v[32:35]\n\t"
    "v_mfma_f32_16x16x32_bf16 v[32:35], a[104:107], a[8:11], v[32:35]\n\t"
    "v_mfma_f32_16x16x32_bf16 v[32:35], a[108:111], a[12:15], v[32:35]\n\t"
    "v_mfma_f32_16x16x32_bf16 v[32:35], a[112:115], a[16:19], v[32:35]\n\t"
    "v_mfma_f32_16x16x32_bf16 v[32:35], a[116:119], a[20:23], v[32:35]\n\t"
    "v_mfma_f32_16x16x32_bf16 v[32:35], a[120:123], a[24:27], v[32:35]\n\t"
    "v_mfma_f32_16x16x32_bf16 v[32:35], a[124:127], a[28:31], v[32:35]\n\t"
    "v_mfma_f32_16x16x32_bf16 v[36:39], a[96:99], a[32:35], 0\n\t"
    "v_mfma_f32_16x16x32_bf16 v[36:39], a[100:103], a[36:39], v[36:39]\n\t"
    "v_mfma_f32_16x16x32_bf16 v[36:39], a[104:107], a[40:43], v[36:39]\n\t"
    "v_mfma_f32_16x16x32_bf16 v[36:39], a[108:111], a[44:47], v[36:39]\n\t"
    "v_mfma_f32_16x16x32_bf16 v[36:39], a[112:115], a[48:51], v[36:39]\n\t"
    "v_mfma_f32_16x16x32_bf16 v[36:39], a[116:119], a[52:55], v[36:39]\n\t"
    "v_mfma_f32_16x16x32_bf16 v[36:39], a[120:123], a[56:59], v[36:39]\n\t"
    "v_mfma_f32_16x16x32_bf16 v[36:39], a[124:127], a[60:63], v[36:39]\n\t"
    "v_mfma_f32_16x16x32_bf16 v[40:43], a[96:99], a[64:67], 0\n\t"
    "v_mfma_f32_16x16x32_bf16 v[40:43], a[100:103], a[68:71], v[40:43]\n\t"
    "v_mfma_f32_16x16x32_bf16 v[40:43], a[104:107], a[72:75], v[40:43]\n\t"
    "v_mfma_f32_16x16x32_bf16 v[40:43], a[108:111], a[76:79], v[40:43]\n\t"
    "v_mfma_f32_16x16x32_bf16 v[40:43], a[112:115], a[80:83], v[40:43]\n\t"
    "v_mfma_f32_16x16x32_bf16 v[40:43], a[116:119], a[84:87], v[40:43]\n\t"
    "v_mfma_f32_16x16x32_bf16 v[40:43], a[120:123], a[88:91], v[40:43]\n\t"
    "v_mfma_f32_16x16x32_bf16 v[40:43], a[124:127], a[92:95], v[40:43]\n\t"
    "s_nop 7\n\t"
    "s_nop 1\n\t"
    "v_max3_f32 v1, v1, v32, v33\n\t"
    "v_max3_f32 v1, v1, v34, v35\n\t"
    "v_max3_f32 v2, v2, v36, v37\n\t"
    "v_max3_f32 v2, v2, v38, v39\n\t"
    "v_max3_f32 v3, v3, v40, v41\n\t"
    "v_max3_f32 v3, v3, v42, v43\n\t"
    "s_add_u32 s56, s56, 8192\n\t"
    "s_cmp_lt_u32 s56, 0x40000\n\t"
    "s_nop 3\n\t"
    "s_cbranch_scc1 Lmax_%=\n\t"
    "v_mov_b32 v32, v1\n\t"
    "s_nop 1\n\t"
    "v_permlane16_swap_b32 v1, v32\n\t"
    "s_nop 1\n\t"
    "v_max_f32 v1, v1, v32\n\t"
    "v_mov_b32 v32, v1\n\t"
    "s_nop 1\n\t"
    "v_permlane32_swap_b32 v1, v32\n\t"
    "s_nop 1\n\t"
    "v_max_f32 v1, v1, v32\n\t"
    "v_mul_f32 v1, 0xbdb8aa3b, v1\n\t"
    "v_mov_b32 v32, v2\n\t"
    "s_nop 1\n\t"
    "v_permlane16_swap_b32 v2, v32\n\t"
    "s_nop 1\n\t"
    "v_max_f32 v2, v2, v32\n\t"
    "v_mov_b32 v32, v2\n\t"
    "s_nop 1\n\t"
    "v_permlane32_swap_b32 v2, v32\n\t"
    "s_nop 1\n\t"
    "v_max_f32 v2, v2, v32\n\t"
    "v_mul_f32 v2, 0xbdb8aa3b, v2\n\t"
    "v_mov_b32 v32, v3\n\t"
    "s_nop 1\n\t"
    "v_permlane16_swap_b32 v3, v32\n\t"
    "s_nop 1\n\t"
    "v_max_f32 v3, v3, v32\n\t"
    "v_mov_b32 v32, v3\n\t"
    "s_nop 1\n\t"
    "v_permlane32_swap_b32 v3, v32\n\t"
    "s_nop 1\n\t"
    "v_max_f32 v3, v3, v32\n\t"
    "v_mul_f32 v3, 0xbdb8aa3b, v3\n\t"
    "s_mov_b64 s[50:51], -1\n\t"
    "s_load_dwordx8 s[64:71], %0, 0\n\t"
    "s_branch Lstart_%=\n\t"
    ".p2align 12\n\t"
    "Lbody_%=:\n\t"
    "s_waitcnt vmcnt(8)\n\t"
    "ds_read_b128 a[0:3], v32 offset:0\n\t"
    "ds_read_b128 a[4:7], v33 offset:0\n\t"
    "ds_read_b128 a[8:11], v34 offset:0\n\t"
    "ds_read_b128 a[12:15], v35 offset:0\n\t"
    "ds_read_b128 a[16:19], v32 offset:256\n\t"
    "ds_read_b128 a[20:23], v33 offset:256\n\t"
    "ds_read_b128 a[24:27], v34 offset:256\n\t"
    "ds_read_b128 a[28:31], v35 offset:256\n\t"
    "s_waitcnt lgkmcnt(7)\n\t"
    "s_add_u32 m0, s40, 98304\n\t"
    "s_mov_b32 s46, 32768\n\t"
    "v_mfma_f32_16x16x32_bf16 v[204:207], v[44:47], a[0:3], 0\n\t"
    "s_waitcnt lgkmcnt(6)\n\t"
    "ds_read_b128 v[76:79], v4 offset:8192\n\t"
    "v_mov_b32 v31, 0x53800000\n\t"
    "v_mfma_f32_16x16x32_bf16 v[204:207], v[48:51], a[4:7], v[204:207]\n\t"
    "s_waitcnt lgkmcnt(6)\n\t"
    "ds_read_b128 v[80:83], v5 offset:8192\n\t"
    "buffer_load_dwordx4 v12, s[28:31], s46 offen offset:0 nt lds\n\t"
    "v_mfma_f32_16x16x32_bf16 v[204:207], v[52:55], a[8:11], v[204:207]\n\t"
    "s_waitcnt lgkmcnt(6)\n\t"
    "ds_read_b128 v[84:87], v6 offset:8192\n\t"
    "v_mfma_f32_16x16x32_bf16 v[204:207], v[56:59], a[12:15], v[204:207]\n\t"
    "s_waitcnt lgkmcnt(6)\n\t"
    "ds_read_b128 v[88:91], v7 offset:8192\n\t"
    "buffer_load_dwordx4 v13, s[28:31], s46 offen offset:1024 nt lds\n\t"
    "v_mfma_f32_16x16x32_bf16 v[204:207], v[60:63], a[16:19], v[204:207]\n\t"
    "s_waitcnt lgkmcnt(6)\n\t"
    "ds_read_b128 v[92:95], v4 offset:8448\n\t"
    "v_mfma_f32_16x16x32_bf16 v[204:207], v[64:67], a[20:23], v[204:207]\n\t"
    "s_waitcnt lgkmcnt(6)\n\t"
    "ds_read_b128 v[96:99], v5 offset:8448\n\t"
    "v_mfma_f32_16x16x32_bf16 v[204:207], v[68:71], a[24:27], v[204:207]\n\t"
    "s_waitcnt lgkmcnt(6)\n\t"
    "ds_read_b128 v[100:103], v6 offset:8448\n\t"
    "buffer_load_dwordx4 v14, s[28:31], s46 offen offset:2048 nt lds\n\t"
    "v_mfma_f32_16x16x32_bf16 v[204:207], v[72:75], a[28:31], v[204:207]\n\t"
    "s_waitcnt lgkmcnt(6)\n\t"
    "ds_read_b128 v[104:107], v7 offset:8448\n\t"
    "v_mfma_f32_16x16x32_bf16 v[208:211], v[76:79], a[0:3], 0\n\t"
    "s_waitcnt lgkmcnt(6)\n\t"
    "ds_read_b128 v[108:111], v4 offset:16384\n\t"
    "buffer_load_dwordx4 v15, s[28:31], s46 offen offset:3072 nt lds\n\t"
    "v_mfma_f32_16x16x32_bf16 v[208:211], v[80:83], a[4:7], v[208:211]\n\t"
    "s_waitcnt lgkmcnt(6)\n\t"
    "s_add_u32 m0, s40, 102400\n\t"
    "s_mov_b32 s47, 36864\n\t"
    "ds_read_b128 v[112:115], v5 offset:16384\n\t"
    "v_mfma_f32_16x16x32_bf16 v[208:211], v[84:87], a[8:11], v[208:211]\n\t"
    "s_waitcnt lgkmcnt(6)\n\t"
    "ds_read_b128 v[116:119], v6 offset:16384\n\t"
    "buffer_load_dwordx4 v12, s[28:31], s47 offen offset:0 nt lds\n\t"
    "v_mfma_f32_16x16x32_bf16 v[208:211], v[88:91], a[12:15], v[208:211]\n\t"
    "s_waitcnt lgkmcnt(6)\n\t"
    "ds_read_b128 v[120:123], v7 offset:16384\n\t"
    "v_mfma_f32_16x16x32_bf16 v[208:211], v[92:95], a[16:19], v[208:211]\n\t"
    "s_waitcnt lgkmcnt(6)\n\t"
    "ds_read_b128 v[124:127], v4 offset:16640\n\t"
    "v_mfma_f32_16x16x32_bf16 v[208:211], v[96:99], a[20:23], v[208:211]\n\t"
    "s_waitcnt lgkmcnt(6)\n\t"
    "ds_read_b128 v[128:131], v5 offset:16640\n\t"
    "buffer_load_dwordx4 v13, s[28:31], s47 offen offset:1024 nt lds\n\t"
    "v_mfma_f32_16x16x32_bf16 v[208:211], v[100:103], a[24:27], v[208:211]\n\t"
    "s_waitcnt lgkmcnt(6)\n\t"
    "ds_read_b128 v[132:135], v6 offset:16640\n\t"
    "v_mfma_f32_16x16x32_bf16 v[208:211], v[104:107], a[28:31], v[208:211]\n\t"
    "s_waitcnt lgkmcnt(6)\n\t"
    "ds_read_b128 v[136:139], v7 offset:16640\n\t"
    "buffer_load_dwordx4 v14, s[28:31], s47 offen offset:2048 nt lds\n\t"
    "v_mfma_f32_16x16x32_bf16 v[212:215], v[108:111], a[0:3], 0\n\t"
    "s_waitcnt lgkmcnt(6)\n\t"
    "ds_read_b128 v[140:143], v4 offset:24576\n\t"
    "v_mfma_f32_16x16x32_bf16 v[212:215], v[112:115], a[4:7], v[212:215]\n\t"
    "s_waitcnt lgkmcnt(6)\n\t"
    "ds_read_b128 v[144:147], v5 offset:24576\n\t"
    "v_mfma_f32_16x16x32_bf16 v[212:215], v[116:119], a[8:11], v[212:215]\n\t"
    "s_waitcnt lgkmcnt(6)\n\t"
    "ds_read_b128 v[148:151], v6 offset:24576\n\t"
    "buffer_load_dwordx4 v15, s[28:31], s47 offen offset:3072 nt lds\n\t"
    "v_mfma_f32_16x16x32_bf16 v[212:215], v[120:123], a[12:15], v[212:215]\n\t"
    "s_waitcnt lgkmcnt(6)\n\t"
    "ds_read_b128 v[152:155], v7 offset:24576\n\t"
    "v_mfma_f32_16x16x32_bf16 v[212:215], v[124:127], a[16:19], v[212:215]\n\t"
    "s_waitcnt vmcnt(8) lgkmcnt(6)\n\t"
    "s_barrier\n\t"
    "ds_read_b128 v[156:159], v4 offset:24832\n\t"
    "v_mfma_f32_16x16x32_bf16 v[212:215], v[128:131], a[20:23], v[212:215]\n\t"
    "s_waitcnt lgkmcnt(6)\n\t"
    "s_add_u32 m0, s40, 65536\n\t"
    "s_add_u32 s46, s41, 65536\n\t"
    "ds_read_b128 v[160:163], v5 offset:24832\n\t"
    "v_mfma_f32_16x16x32_bf16 v[212:215], v[132:135], a[24:27], v[212:215]\n\t"
    "s_waitcnt lgkmcnt(6)\n\t"
    "ds_read_b128 v[164:167], v6 offset:24832\n\t"
    "v_mfma_f32_16x16x32_bf16 v[212:215], v[136:139], a[28:31], v[212:215]\n\t"
    "s_waitcnt lgkmcnt(6)\n\t"
    "ds_read_b128 v[168:171], v7 offset:24832\n\t"
    "buffer_load_dwordx4 v12, s[20:23], s46 offen offset:0 nt lds\n\t"
    "v_mfma_f32_16x16x32_bf16 v[216:219], v[140:143], a[0:3], 0\n\t"
    "s_waitcnt lgkmcnt(6)\n\t"
    "ds_read_b128 a[96:99], v4 offset:32768\n\t"
    "v_mfma_f32_16x16x32_bf16 v[216:219], v[144:147], a[4:7], v[216:219]\n\t"
    "s_waitcnt lgkmcnt(6)\n\t"
    "ds_read_b128 a[100:103], v5 offset:32768\n\t"
    "v_mfma_f32_16x16x32_bf16 v[216:219], v[148:151], a[8:11], v[216:219]\n\t"
    "s_waitcnt lgkmcnt(6)\n\t"
    "ds_read_b128 a[104:107], v6 offset:32768\n\t"
    "v_mfma_f32_16x16x32_bf16 v[216:219], v[152:155], a[12:15], v[216:219]\n\t"
    "s_waitcnt lgkmcnt(6)\n\t"
    "ds_read_b128 a[108:111], v7 offset:32768\n\t"
    "v_mfma_f32_16x16x32_bf16 v[216:219], v[156:159], a[16:19], v[216:219]\n\t"
    "s_waitcnt lgkmcnt(6)\n\t"
    "ds_read_b128 a[112:115], v4 offset:33024\n\t"
    "buffer_load_dwordx4 v13, s[20:23], s46 offen offset:1024 nt lds\n\t"
    "v_mfma_f32_16x16x32_bf16 v[216:219], v[160:163], a[20:23], v[216:219]\n\t"
    "s_waitcnt lgkmcnt(6)\n\t"
    "ds_read_b128 a[116:119], v5 offset:33024\n\t"
    "v_mfma_f32_16x16x32_bf16 v[216:219], v[164:167], a[24:27], v[216:219]\n\t"
    "s_waitcnt lgkmcnt(6)\n\t"
    "ds_read_b128 a[120:123], v6 offset:33024\n\t"
    "v_mfma_f32_16x16x32_bf16 v[216:219], v[168:171], a[28:31], v[216:219]\n\t"
    "s_waitcnt lgkmcnt(6)\n\t"
    "ds_read_b128 a[124:127], v7 offset:33024\n\t"
    "v_max3_f32 v22, v204, v205, v206\n\t"
    "v_max3_f32 v25, v207, v208, v209\n\t"
    "v_mfma_f32_16x16x32_bf16 v[220:223], a[96:99], a[0:3], 0\n\t"
    "s_waitcnt lgkmcnt(6)\n\t"
    "ds_read_b128 a[128:131], v4 offset:40960\n\t"
    "v_max3_f32 v22, v22, v210, v211\n\t"
    "v_max3_f32 v25, v25, v212, v213\n\t"
    "v_mfma_f32_16x16x32_bf16 v[220:223], a[100:103], a[4:7], v[220:223]\n\t"
    "s_waitcnt lgkmcnt(6)\n\t"
    "ds_read_b128 a[132:135], v5 offset:40960\n\t"
    "v_max3_f32 v22, v22, v214, v215\n\t"
    "v_max3_f32 v25, v25, v216, v217\n\t"
    "v_mfma_f32_16x16x32_bf16 v[220:223], a[104:107], a[8:11], v[220:223]\n\t"
    "s_waitcnt lgkmcnt(6)\n\t"
    "ds_read_b128 a[136:139], v6 offset:40960\n\t"
    "buffer_load_dwordx4 v14, s[20:23], s46 offen offset:2048 nt lds\n\t"
    "v_mfma_f32_16x16x32_bf16 v[220:223], a[108:111], a[12:15], v[220:223]\n\t"
    "s_waitcnt lgkmcnt(6)\n\t"
    "ds_read_b128 a[140:143], v7 offset:40960\n\t"
    "v_max3_f32 v22, v22, v218, v219\n\t"
    "v_max_f32 v22, v22, v25\n\t"
    "v_mfma_f32_16x16x32_bf16 v[220:223], a[112:115], a[16:19], v[220:223]\n\t"
    "s_waitcnt lgkmcnt(6)\n\t"
    "ds_read_b128 a[144:147], v4 offset:41216\n\t"
    "v_mov_b32 v25, v22\n\t"
    "v_mfma_f32_16x16x32_bf16 v[220:223], a[116:119], a[20:23], v[220:223]\n\t"
    "s_waitcnt lgkmcnt(6)\n\t"
    "ds_read_b128 a[148:151], v5 offset:41216\n\t"
    "v_permlane16_swap_b32 v22, v25\n\t"
    "v_mfma_f32_16x16x32_bf16 v[220:223], a[120:123], a[24:27], v[220:223]\n\t"
    "s_waitcnt lgkmcnt(6)\n\t"
    "ds_read_b128 a[152:155], v6 offset:41216\n\t"
    "v_max_f32 v22, v22, v25\n\t"
    "v_mov_b32 v25, v22\n\t"
    "v_mfma_f32_16x16x32_bf16 v[220:223], a[124:127], a[28:31], v[220:223]\n\t"
    "s_waitcnt lgkmcnt(6)\n\t"
    "ds_read_b128 a[156:159], v7 offset:41216\n\t"
    "buffer_load_dwordx4 v15, s[20:23], s46 offen offset:3072 nt lds\n\t"
    "v_mfma_f32_16x16x32_bf16 v[224:227], a[128:131], a[0:3], 0\n\t"
    "s_waitcnt lgkmcnt(6)\n\t"
    "ds_read_b128 a[224:227], v4 offset:49152\n\t"
    "v_permlane32_swap_b32 v22, v25\n\t"
    "v_mfma_f32_16x16x32_bf16 v[224:227], a[132:135], a[4:7], v[224:227]\n\t"
    "s_waitcnt lgkmcnt(6)\n\t"
    "ds_read_b128 a[228:231], v5 offset:49152\n\t"
    "v_max_f32 v22, v22, v25\n\t"
    "v_mul_f32 v22, 0xbdb8aa3b, v22\n\t"
    "v_mfma_f32_16x16x32_bf16 v[224:227], a[136:139], a[8:11], v[224:227]\n\t"
    "s_waitcnt lgkmcnt(6)\n\t"
    "s_add_u32 m0, s40, 69632\n\t"
    "s_add_u32 s47, s41, 69632\n\t"
    "ds_read_b128 a[232:235], v6 offset:49152\n\t"
    "v_mfma_f32_16x16x32_bf16 v[224:227], a[140:143], a[12:15], v[224:227]\n\t"
    "s_waitcnt lgkmcnt(6)\n\t"
    "ds_read_b128 a[236:239], v7 offset:49152\n\t"
    "v_cndmask_b32 v22, v22, v1, s[50:51]\n\t"
    "v_mfma_f32_16x16x32_bf16 v[224:227], a[144:147], a[16:19], v[224:227]\n\t"
    "s_waitcnt lgkmcnt(6)\n\t"
    "ds_read_b128 a[240:243], v4 offset:49408\n\t"
    "buffer_load_dwordx4 v12, s[20:23], s47 offen offset:0 nt lds\n\t"
    "v_mfma_f32_16x16x32_bf16 v[224:227], a[148:151], a[20:23], v[224:227]\n\t"
    "s_waitcnt lgkmcnt(6)\n\t"
    "ds_read_b128 a[244:247], v5 offset:49408\n\t"
    "v_fma_f32 v204, v204, s34, v22\n\t"
    "v_fma_f32 v205, v205, s34, v22\n\t"
    "v_mfma_f32_16x16x32_bf16 v[224:227], a[152:155], a[24:27], v[224:227]\n\t"
    "s_waitcnt lgkmcnt(6)\n\t"
    "ds_read_b128 a[248:251], v6 offset:49408\n\t"
    "v_exp_f32 v204, v204\n\t"
    "v_mfma_f32_16x16x32_bf16 v[224:227], a[156:159], a[28:31], v[224:227]\n\t"
    "s_waitcnt lgkmcnt(6)\n\t"
    "ds_read_b128 a[252:255], v7 offset:49408\n\t"
    "v_exp_f32 v205, v205\n\t"
    "v_mfma_f32_16x16x32_bf16 v[228:231], a[224:227], a[0:3], 0\n\t"
    "s_waitcnt lgkmcnt(6)\n\t"
    "ds_read_b128 v[172:175], v4 offset:57344\n\t"
    "v_fma_f32 v206, v206, s34, v22\n\t"
    "v_fma_f32 v207, v207, s34, v22\n\t"
    "v_mfma_f32_16x16x32_bf16 v[228:231], a[228:231], a[4:7], v[228:231]\n\t"
    "s_waitcnt lgkmcnt(6)\n\t"
    "ds_read_b128 v[176:179], v5 offset:57344\n\t"
    "v_exp_f32 v206, v206\n\t"
    "v_mfma_f32_16x16x32_bf16 v[228:231], a[232:235], a[8:11], v[228:231]\n\t"
    "s_waitcnt lgkmcnt(6)\n\t"
    "ds_read_b128 v[180:183], v6 offset:57344\n\t"
    "buffer_load_dwordx4 v13, s[20:23], s47 offen offset:1024 nt lds\n\t"
    "v_mfma_f32_16x16x32_bf16 v[228:231], a[236:239], a[12:15], v[228:231]\n\t"
    "s_waitcnt lgkmcnt(6)\n\t"
    "ds_read_b128 v[184:187], v7 offset:57344\n\t"
    "v_exp_f32 v207, v207\n\t"
    "v_mfma_f32_16x16x32_bf16 v[228:231], a[240:243], a[16:19], v[228:231]\n\t"
    "s_waitcnt lgkmcnt(6)\n\t"
    "ds_read_b128 v[188:191], v4 offset:57600\n\t"
    "v_cvt_pk_bf16_f32 v204, v204, v205\n\t"
    "v_cvt_pk_bf16_f32 v206, v206, v207\n\t"
    "v_mfma_f32_16x16x32_bf16 v[228:231], a[244:247], a[20:23], v[228:231]\n\t"
    "s_waitcnt lgkmcnt(6)\n\t"
    "ds_read_b128 v[192:195], v5 offset:57600\n\t"
    "v_accvgpr_write_b32 a176, v204\n\t"
    "v_accvgpr_write_b32 a177, v206\n\t"
    "v_mfma_f32_16x16x32_bf16 v[228:231], a[248:251], a[24:27], v[228:231]\n\t"
    "s_waitcnt lgkmcnt(6)\n\t"
    "ds_read_b128 v[196:199], v6 offset:57600\n\t"
    "v_fma_f32 v208, v208, s34, v22\n\t"
    "v_fma_f32 v209, v209, s34, v22\n\t"
    "v_mfma_f32_16x16x32_bf16 v[228:231], a[252:255], a[28:31], v[228:231]\n\t"
    "s_waitcnt lgkmcnt(6)\n\t"
    "ds_read_b128 v[200:203], v7 offset:57600\n\t"
    "buffer_load_dwordx4 v14, s[20:23], s47 offen offset:2048 nt lds\n\t"
    "v_mfma_f32_16x16x32_bf16 v[204:207], v[172:175], a[0:3], 0\n\t"
    "s_waitcnt lgkmcnt(6)\n\t"
    "v_exp_f32 v208, v208\n\t"
    "v_fma_f32 v210, v210, s34, v22\n\t"
    "v_mfma_f32_16x16x32_bf16 v[204:207], v[176:179], a[4:7], v[204:207]\n\t"
    "s_waitcnt lgkmcnt(5)\n\t"
    "v_exp_f32 v209, v209\n\t"
    "v_fma_f32 v211, v211, s34, v22\n\t"
    "v_mfma_f32_16x16x32_bf16 v[204:207], v[180:183], a[8:11], v[204:207]\n\t"
    "s_waitcnt lgkmcnt(4)\n\t"
    "v_exp_f32 v210, v210\n\t"
    "v_cvt_pk_bf16_f32 v208, v208, v209\n\t"
    "v_mfma_f32_16x16x32_bf16 v[204:207], v[184:187], a[12:15], v[204:207]\n\t"
    "s_waitcnt lgkmcnt(3)\n\t"
    "v_exp_f32 v211, v211\n\t"
    "v_accvgpr_write_b32 a178, v208\n\t"
    "v_mfma_f32_16x16x32_bf16 v[204:207], v[188:191], a[16:19], v[204:207]\n\t"
    "s_waitcnt lgkmcnt(2)\n\t"
    "v_cvt_pk_bf16_f32 v210, v210, v211\n\t"
    "v_fma_f32 v212, v212, s34, v22\n\t"
    "v_accvgpr_write_b32 a179, v210\n\t"
    "v_mfma_f32_16x16x32_bf16 v[204:207], v[192:195], a[20:23], v[204:207]\n\t"
    "s_waitcnt lgkmcnt(1)\n\t"
    "buffer_load_dwordx4 v15, s[20:23], s47 offen offset:3072 nt lds\n\t"
    "v_mfma_f32_16x16x32_bf16 v[204:207], v[196:199], a[24:27], v[204:207]\n\t"
    "s_waitcnt lgkmcnt(0)\n\t"
    "v_exp_f32 v212, v212\n\t"
    "v_fma_f32 v213, v213, s34, v22\n\t"
    "v_mfma_f32_16x16x32_bf16 v[204:207], v[200:203], a[28:31], v[204:207]\n\t"
    "s_mov_b32 s56, Lstub1_%=-Lhop0_%=\n\t"
    "s_cmp_eq_u32 s7, 1\n\t"
    "s_cselect_b32 s56, Lstub2_%=-Lhop0_%=, s56\n\t"
    "s_cmp_eq_u32 s7, 2\n\t"
    "s_cselect_b32 s56, Lstub3_%=-Lhop0_%=, s56\n\t"
    "s_cmp_eq_u32 s7, 3\n\t"
    "s_cselect_b32 s56, Lstub4_%=-Lhop0_%=, s56\n\t"
    "s_getpc_b64 s[48:49]\n\t"
    "Lhop0_%=:\n\t"
    "s_add_u32 s48, s48, s56\n\t"
    "s_addc_u32 s49, s49, 0\n\t"
    "s_mov_b64 exec, 0\n\t"
    "s_swappc_b64 s[58:59], s[48:49]\n\t"
    "s_mov_b64 exec, -1\n\t"
    "s_mov_b32 s56, Lstub5_%=-Lhop1_%=\n\t"
    "s_cmp_eq_u32 s7, 1\n\t"
    "s_cselect_b32 s56, Lstub6_%=-Lhop1_%=, s56\n\t"
    "s_cmp_eq_u32 s7, 2\n\t"
    "s_cselect_b32 s56, Lstub7_%=-Lhop1_%=, s56\n\t"
    "s_cmp_eq_u32 s7, 3\n\t"
    "s_cselect_b32 s56, Lstub7_%=-Lhop1_%=, s56\n\t"
    "s_getpc_b64 s[48:49]\n\t"
    "Lhop1_%=:\n\t"
    "s_add_u32 s48, s48, s56\n\t"
    "s_addc_u32 s49, s49, 0\n\t"
    "s_mov_b64 exec, 0\n\t"
    "s_swappc_b64 s[58:59], s[48:49]\n\t"
    "s_mov_b64 exec, -1\n\t"
    "s_waitcnt vmcnt(8)\n\t"
    "ds_read_b128 a[32:35], v32 offset:32768\n\t"
    "ds_read_b128 a[36:39], v33 offset:32768\n\t"
    "ds_read_b128 a[40:43], v34 offset:32768\n\t"
    "ds_read_b128 a[44:47], v35 offset:32768\n\t"
    "ds_read_b128 a[48:51], v32 offset:33024\n\t"
    "ds_read_b128 a[52:55], v33 offset:33024\n\t"
    "ds_read_b128 a[56:59], v34 offset:33024\n\t"
    "ds_read_b128 a[60:63], v35 offset:33024\n\t"
    "s_waitcnt lgkmcnt(7)\n\t"
    "s_add_u32 m0, s40, 131072\n\t"
    "s_mov_b32 s46, 65536\n\t"
    "v_fma_f32 v214, v214, s34, v22\n\t"
    "v_mfma_f32_16x16x32_bf16 a[164:167], a[160:163], a[176:179], 0\n\t"
    "v_exp_f32 v213, v213\n\t"
    "v_mfma_f32_16x16x32_bf16 v[208:211], v[44:47], a[32:35], 0\n\t"
    "s_waitcnt lgkmcnt(6)\n\t"
    "v_exp_f32 v214, v214\n\t"
    "v_mfma_f32_16x16x32_bf16 v[208:211], v[48:51], a[36:39], v[208:211]\n\t"
    "s_waitcnt lgkmcnt(5)\n\t"
    "buffer_load_dwordx4 v12, s[28:31], s46 offen offset:0 nt lds\n\t"
    "v_mfma_f32_16x16x32_bf16 v[208:211], v[52:55], a[40:43], v[208:211]\n\t"
    "s_waitcnt lgkmcnt(4)\n\t"
    "v_fma_f32 v215, v215, s34, v22\n\t"
    "v_cvt_pk_bf16_f32 v212, v212, v213\n\t"
    "v_mfma_f32_16x16x32_bf16 v[208:211], v[56:59], a[44:47], v[208:211]\n\t"
    "s_waitcnt lgkmcnt(3)\n\t"
    "v_exp_f32 v215, v215\n\t"
    "v_mfma_f32_16x16x32_bf16 v[208:211], v[60:63], a[48:51], v[208:211]\n\t"
    "s_waitcnt lgkmcnt(2)\n\t"
    "v_accvgpr_write_b32 a188, v212\n\t"
    "v_cvt_pk_bf16_f32 v214, v214, v215\n\t"
    "v_mfma_f32_16x16x32_bf16 v[208:211], v[64:67], a[52:55], v[208:211]\n\t"
    "s_waitcnt lgkmcnt(1)\n\t"
    "v_accvgpr_write_b32 a189, v214\n\t"
    "v_fma_f32 v216, v216, s34, v22\n\t"
    "v_mfma_f32_16x16x32_bf16 v[208:211], v[68:71], a[56:59], v[208:211]\n\t"
    "s_waitcnt lgkmcnt(0)\n\t"
    "buffer_load_dwordx4 v13, s[28:31], s46 offen offset:1024 nt lds\n\t"
    "v_mfma_f32_16x16x32_bf16 v[208:211], v[72:75], a[60:63], v[208:211]\n\t"
    "v_exp_f32 v216, v216\n\t"
    "v_mfma_f32_16x16x32_bf16 v[212:215], v[76:79], a[32:35], 0\n\t"
    "ds_read_b128 a[96:99], v4 offset:32768\n\t"
    "v_fma_f32 v217, v217, s34, v22\n\t"
    "v_mfma_f32_16x16x32_bf16 v[212:215], v[80:83], a[36:39], v[212:215]\n\t"
    "v_exp_f32 v217, v217\n\t"
    "v_mfma_f32_16x16x32_bf16 v[212:215], v[84:87], a[40:43], v[212:215]\n\t"
    "buffer_load_dwordx4 v14, s[28:31], s46 offen offset:2048 nt lds\n\t"
    "v_mfma_f32_16x16x32_bf16 v[212:215], v[88:91], a[44:47], v[212:215]\n\t"
    "ds_read_b128 a[100:103], v5 offset:32768\n\t"
    "v_fma_f32 v218, v218, s34, v22\n\t"
    "v_mfma_f32_16x16x32_bf16 v[212:215], v[92:95], a[48:51], v[212:215]\n\t"
    "v_exp_f32 v218, v218\n\t"
    "v_mfma_f32_16x16x32_bf16 v[212:215], v[96:99], a[52:55], v[212:215]\n\t"
    "v_fma_f32 v219, v219, s34, v22\n\t"
    "v_cvt_pk_bf16_f32 v216, v216, v217\n\t"
    "v_mfma_f32_16x16x32_bf16 v[212:215], v[100:103], a[56:59], v[212:215]\n\t"
    "ds_read_b128 a[104:107], v6 offset:32768\n\t"
    "v_accvgpr_write_b32 a190, v216\n\t"
    "v_mfma_f32_16x16x32_bf16 v[212:215], v[104:107], a[60:63], v[212:215]\n\t"
    "buffer_load_dwordx4 v15, s[28:31], s46 offen offset:3072 nt lds\n\t"
    "v_mfma_f32_16x16x32_bf16 v[232:235], v[108:111], a[32:35], 0\n\t"
    "v_exp_f32 v219, v219\n\t"
    "v_mfma_f32_16x16x32_bf16 v[232:235], v[112:115], a[36:39], v[232:235]\n\t"
    "ds_read_b128 a[108:111], v7 offset:32768\n\t"
    "v_cvt_pk_bf16_f32 v218, v218, v219\n\t"
    "v_mfma_f32_16x16x32_bf16 v[232:235], v[116:119], a[40:43], v[232:235]\n\t"
    "s_add_u32 m0, s40, 135168\n\t"
    "s_mov_b32 s47, 69632\n\t"
    "v_accvgpr_write_b32 a191, v218\n\t"
    "v_mfma_f32_16x16x32_bf16 v[232:235], v[120:123], a[44:47], v[232:235]\n\t"
    "v_fma_f32 v220, v220, s34, v22\n\t"
    "v_fma_f32 v221, v221, s34, v22\n\t"
    "v_mfma_f32_16x16x32_bf16 v[232:235], v[124:127], a[48:51], v[232:235]\n\t"
    "ds_read_b128 a[112:115], v4 offset:33024\n\t"
    "buffer_load_dwordx4 v12, s[28:31], s47 offen offset:0 nt lds\n\t"
    "v_mfma_f32_16x16x32_bf16 v[232:235], v[128:131], a[52:55], v[232:235]\n\t"
    "v_exp_f32 v220, v220\n\t"
    "v_mfma_f32_16x16x32_bf16 v[232:235], v[132:135], a[56:59], v[232:235]\n\t"
    "v_exp_f32 v221, v221\n\t"
    "v_mfma_f32_16x16x32_bf16 v[232:235], v[136:139], a[60:63], v[232:235]\n\t"
    "ds_read_b128 a[116:119], v5 offset:33024\n\t"
    "v_fma_f32 v222, v222, s34, v22\n\t"
    "v_mfma_f32_16x16x32_bf16 a[164:167], a[160:163], a[188:191], a[164:167]\n\t"
    "v_exp_f32 v222, v222\n\t"
    "v_mfma_f32_16x16x32_bf16 v[216:219], v[140:143], a[32:35], 0\n\t"
    "buffer_load_dwordx4 v13, s[28:31], s47 offen offset:1024 nt lds\n\t"
    "v_mfma_f32_16x16x32_bf16 v[216:219], v[144:147], a[36:39], v[216:219]\n\t"
    "v_fma_f32 v223, v223, s34, v22\n\t"
    "v_cvt_pk_bf16_f32 v220, v220, v221\n\t"
    "v_mfma_f32_16x16x32_bf16 v[216:219], v[148:151], a[40:43], v[216:219]\n\t"
    "ds_read_b128 a[120:123], v6 offset:33024\n\t"
    "v_accvgpr_write_b32 a200, v220\n\t"
    "v_mfma_f32_16x16x32_bf16 v[216:219], v[152:155], a[44:47], v[216:219]\n\t"
    "v_exp_f32 v223, v223\n\t"
    "v_mfma_f32_16x16x32_bf16 v[216:219], v[156:159], a[48:51], v[216:219]\n\t"
    "v_cvt_pk_bf16_f32 v222, v222, v223\n\t"
    "v_fma_f32 v224, v224, s34, v22\n\t"
    "v_mfma_f32_16x16x32_bf16 v[216:219], v[160:163], a[52:55], v[216:219]\n\t"
    "ds_read_b128 a[124:127], v7 offset:33024\n\t"
    "buffer_load_dwordx4 v14, s[28:31], s47 offen offset:2048 nt lds\n\t"
    "v_mfma_f32_16x16x32_bf16 v[216:219], v[164:167], a[56:59], v[216:219]\n\t"
    "v_exp_f32 v224, v224\n\t"
    "v_mfma_f32_16x16x32_bf16 v[216:219], v[168:171], a[60:63], v[216:219]\n\t"
    "s_waitcnt lgkmcnt(7)\n\t"
    "v_accvgpr_write_b32 a201, v222\n\t"
    "v_fma_f32 v225, v225, s34, v22\n\t"
    "v_mfma_f32_16x16x32_bf16 v[236:239], a[96:99], a[32:35], 0\n\t"
    "s_waitcnt lgkmcnt(6)\n\t"
    "ds_read_b128 a[128:131], v4 offset:40960\n\t"
    "v_fma_f32 v226, v226, s34, v22\n\t"
    "v_mfma_f32_16x16x32_bf16 v[236:239], a[100:103], a[36:39], v[236:239]\n\t"
    "s_waitcnt lgkmcnt(6)\n\t"
    "ds_read_b128 a[132:135], v5 offset:40960\n\t"
    "v_fma_f32 v227, v227, s34, v22\n\t"
    "v_mfma_f32_16x16x32_bf16 v[236:239], a[104:107], a[40:43], v[236:239]\n\t"
    "s_waitcnt lgkmcnt(6)\n\t"
    "ds_read_b128 a[136:139], v6 offset:40960\n\t"
    "buffer_load_dwordx4 v15, s[28:31], s47 offen offset:3072 nt lds\n\t"
    "v_mfma_f32_16x16x32_bf16 v[236:239], a[108:111], a[44:47], v[236:239]\n\t"
    "s_waitcnt lgkmcnt(6)\n\t"
    "ds_read_b128 a[140:143], v7 offset:40960\n\t"
    "v_fma_f32 v228, v228, s34, v22\n\t"
    "v_mfma_f32_16x16x32_bf16 v[236:239], a[112:115], a[48:51], v[236:239]\n\t"
    "s_waitcnt vmcnt(8) lgkmcnt(6)\n\t"
    "s_barrier\n\t"
    "ds_read_b128 a[144:147], v4 offset:41216\n\t"
    "v_mfma_f32_16x16x32_bf16 v[236:239], a[116:119], a[52:55], v[236:239]\n\t"
    "s_waitcnt lgkmcnt(6)\n\t"
    "ds_read_b128 a[148:151], v5 offset:41216\n\t"
    "v_fma_f32 v229, v229, s34, v22\n\t"
    "v_mfma_f32_16x16x32_bf16 v[236:239], a[120:123], a[56:59], v[236:239]\n\t"
    "s_waitcnt lgkmcnt(6)\n\t"
    "ds_read_b128 a[152:155], v6 offset:41216\n\t"
    "v_fma_f32 v230, v230, s34, v22\n\t"
    "v_mfma_f32_16x16x32_bf16 v[236:239], a[124:127], a[60:63], v[236:239]\n\t"
    "s_waitcnt lgkmcnt(6)\n\t"
    "ds_read_b128 a[156:159], v7 offset:41216\n\t"
    "v_fma_f32 v231, v231, s34, v22\n\t"
    "v_mfma_f32_16x16x32_bf16 v[220:223], a[128:131], a[32:35], 0\n\t"
    "s_waitcnt lgkmcnt(6)\n\t"
    "ds_read_b128 a[96:99], v40 offset:0\n\t"
    "v_fma_f32 v204, v204, s34, v22\n\t"
    "v_mfma_f32_16x16x32_bf16 v[220:223], a[132:135], a[36:39], v[220:223]\n\t"
    "s_waitcnt lgkmcnt(6)\n\t"
    "v_exp_f32 v225, v225\n\t"
    "v_mfma_f32_16x16x32_bf16 v[220:223], a[136:139], a[40:43], v[220:223]\n\t"
    "s_waitcnt lgkmcnt(5)\n\t"
    "v_exp_f32 v226, v226\n\t"
    "v_mfma_f32_16x16x32_bf16 v[220:223], a[140:143], a[44:47], v[220:223]\n\t"
    "s_waitcnt lgkmcnt(4)\n\t"
    "s_add_u32 m0, s40, 98304\n\t"
    "s_add_u32 s46, s41, 98304\n\t"
    "v_mfma_f32_16x16x32_bf16 v[220:223], a[144:147], a[48:51], v[220:223]\n\t"
    "s_waitcnt lgkmcnt(3)\n\t"
    "ds_read_b128 a[100:103], v41 offset:0\n\t"
    "v_cvt_pk_bf16_f32 v224, v224, v225\n\t"
    "v_mfma_f32_16x16x32_bf16 v[220:223], a[148:151], a[52:55], v[220:223]\n\t"
    "s_waitcnt lgkmcnt(3)\n\t"
    "buffer_load_dwordx4 v12, s[20:23], s46 offen offset:0 nt lds\n\t"
    "v_mfma_f32_16x16x32_bf16 v[220:223], a[152:155], a[56:59], v[220:223]\n\t"
    "s_waitcnt lgkmcnt(2)\n\t"
    "ds_read_b128 a[104:107], v42 offset:0\n\t"
    "v_accvgpr_write_b32 a202, v224\n\t"
    "v_mfma_f32_16x16x32_bf16 v[220:223], a[156:159], a[60:63], v[220:223]\n\t"
    "v_exp_f32 v227, v227\n\t"
    "v_mfma_f32_16x16x32_bf16 v[240:243], a[224:227], a[32:35], 0\n\t"
    "v_exp_f32 v228, v228\n\t"
    "v_mfma_f32_16x16x32_bf16 v[240:243], a[228:231], a[36:39], v[240:243]\n\t"
    "ds_read_b128 a[108:111], v43 offset:0\n\t"
    "v_cvt_pk_bf16_f32 v226, v226, v227\n\t"
    "v_mfma_f32_16x16x32_bf16 v[240:243], a[232:235], a[40:43], v[240:243]\n\t"
    "v_exp_f32 v229, v229\n\t"
    "v_mfma_f32_16x16x32_bf16 v[240:243], a[236:239], a[44:47], v[240:243]\n\t"
    "v_exp_f32 v230, v230\n\t"
    "v_mfma_f32_16x16x32_bf16 v[240:243], a[240:243], a[48:51], v[240:243]\n\t"
    "ds_read_b128 a[112:115], v40 offset:256\n\t"
    "v_accvgpr_write_b32 a203, v226\n\t"
    "v_mfma_f32_16x16x32_bf16 v[240:243], a[244:247], a[52:55], v[240:243]\n\t"
    "v_exp_f32 v231, v231\n\t"
    "v_mfma_f32_16x16x32_bf16 v[240:243], a[248:251], a[56:59], v[240:243]\n\t"
    "v_exp_f32 v204, v204\n\t"
    "v_mfma_f32_16x16x32_bf16 v[240:243], a[252:255], a[60:63], v[240:243]\n\t"
    "ds_read_b128 a[116:119], v41 offset:256\n\t"
    "v_cvt_pk_bf16_f32 v228, v228, v229\n\t"
    "v_mfma_f32_16x16x32_bf16 a[164:167], a[160:163], a[200:203], a[164:167]\n\t"
    "v_accvgpr_write_b32 a212, v228\n\t"
    "v_cvt_pk_bf16_f32 v230, v230, v231\n\t"
    "v_mfma_f32_16x16x32_bf16 v[224:227], v[172:175], a[32:35], 0\n\t"
    "buffer_load_dwordx4 v13, s[20:23], s46 offen offset:1024 nt lds\n\t"
    "v_mfma_f32_16x16x32_bf16 v[224:227], v[176:179], a[36:39], v[224:227]\n\t"
    "v_accvgpr_write_b32 a213, v230\n\t"
    "v_fma_f32 v205, v205, s34, v22\n\t"
    "v_mfma_f32_16x16x32_bf16 v[224:227], v[180:183], a[40:43], v[224:227]\n\t"
    "ds_read_b128 a[120:123], v42 offset:256\n\t"
    "v_fma_f32 v206, v206, s34, v22\n\t"
    "v_mfma_f32_16x16x32_bf16 v[224:227], v[184:187], a[44:47], v[224:227]\n\t"
    "v_exp_f32 v205, v205\n\t"
    "v_mfma_f32_16x16x32_bf16 v[224:227], v[188:191], a[48:51], v[224:227]\n\t"
    "v_exp_f32 v206, v206\n\t"
    "v_mfma_f32_16x16x32_bf16 v[224:227], v[192:195], a[52:55], v[224:227]\n\t"
    "ds_read_b128 a[124:127], v43 offset:256\n\t"
    "v_fma_f32 v207, v207, s34, v22\n\t"
    "v_mfma_f32_16x16x32_bf16 v[224:227], v[196:199], a[56:59], v[224:227]\n\t"
    "v_exp_f32 v207, v207\n\t"
    "v_mfma_f32_16x16x32_bf16 v[224:227], v[200:203], a[60:63], v[224:227]\n\t"
    "s_waitcnt lgkmcnt(7)\n\t"
    "v_cvt_pk_bf16_f32 v204, v204, v205\n\t"
    "v_cvt_pk_bf16_f32 v206, v206, v207\n\t"
    "v_mfma_f32_16x16x32_bf16 v[228:231], a[96:99], a[0:3], 0\n\t"
    "s_waitcnt lgkmcnt(6)\n\t"
    "ds_read_b128 a[128:131], v40 offset:8192\n\t"
    "v_accvgpr_write_b32 a214, v204\n\t"
    "v_mfma_f32_16x16x32_bf16 v[228:231], a[100:103], a[4:7], v[228:231]\n\t"
    "s_waitcnt lgkmcnt(6)\n\t"
    "v_accvgpr_write_b32 a215, v206\n\t"
    "v_max3_f32 v23, v208, v209, v210\n\t"
    "v_mfma_f32_16x16x32_bf16 v[228:231], a[104:107], a[8:11], v[228:231]\n\t"
    "s_waitcnt lgkmcnt(5)\n\t"
    "ds_read_b128 a[132:135], v41 offset:8192\n\t"
    "v_max3_f32 v26, v211, v212, v213\n\t"
    "v_mfma_f32_16x16x32_bf16 v[228:231], a[108:111], a[12:15], v[228:231]\n\t"
    "s_waitcnt lgkmcnt(5)\n\t"
    "v_max3_f32 v23, v23, v214, v215\n\t"
    "v_max3_f32 v26, v26, v232, v233\n\t"
    "v_mfma_f32_16x16x32_bf16 v[228:231], a[112:115], a[16:19], v[228:231]\n\t"
    "s_waitcnt lgkmcnt(4)\n\t"
    "ds_read_b128 a[136:139], v42 offset:8192\n\t"
    "buffer_load_dwordx4 v14, s[20:23], s46 offen offset:2048 nt lds\n\t"
    "v_mfma_f32_16x16x32_bf16 v[228:231], a[116:119], a[20:23], v[228:231]\n\t"
    "s_waitcnt lgkmcnt(4)\n\t"
    "v_max3_f32 v23, v23, v234, v235\n\t"
    "v_max3_f32 v26, v26, v216, v217\n\t"
    "v_mfma_f32_16x16x32_bf16 v[228:231], a[120:123], a[24:27], v[228:231]\n\t"
    "s_waitcnt lgkmcnt(3)\n\t"
    "ds_read_b128 a[140:143], v43 offset:8192\n\t"
    "v_max3_f32 v23, v23, v218, v219\n\t"
    "v_mfma_f32_16x16x32_bf16 v[228:231], a[124:127], a[28:31], v[228:231]\n\t"
    "v_max_f32 v23, v23, v26\n\t"
    "v_mov_b32 v26, v23\n\t"
    "v_mfma_f32_16x16x32_bf16 a[164:167], a[160:163], a[212:215], a[164:167]\n\t"
    "v_mfma_f32_16x16x32_bf16 v[204:207], a[96:99], a[32:35], 0\n\t"
    "ds_read_b128 a[144:147], v40 offset:8448\n\t"
    "v_mfma_f32_16x16x32_bf16 v[204:207], a[100:103], a[36:39], v[204:207]\n\t"
    "v_permlane16_swap_b32 v23, v26\n\t"
    "v_mfma_f32_16x16x32_bf16 v[204:207], a[104:107], a[40:43], v[204:207]\n\t"
    "ds_read_b128 a[148:151], v41 offset:8448\n\t"
    "v_max_f32 v23, v23, v26\n\t"
    "v_mfma_f32_16x16x32_bf16 v[204:207], a[108:111], a[44:47], v[204:207]\n\t"
    "v_mov_b32 v26, v23\n\t"
    "v_fma_f32 v228, v228, s34, v22\n\t"
    "v_mfma_f32_16x16x32_bf16 v[204:207], a[112:115], a[48:51], v[204:207]\n\t"
    "ds_read_b128 a[152:155], v42 offset:8448\n\t"
    "v_fma_f32 v229, v229, s34, v22\n\t"
    "v_mfma_f32_16x16x32_bf16 v[204:207], a[116:119], a[52:55], v[204:207]\n\t"
    "v_permlane32_swap_b32 v23, v26\n\t"
    "v_mfma_f32_16x16x32_bf16 v[204:207], a[120:123], a[56:59], v[204:207]\n\t"
    "ds_read_b128 a[156:159], v43 offset:8448\n\t"
    "v_max_f32 v23, v23, v26\n\t"
    "v_mfma_f32_16x16x32_bf16 v[204:207], a[124:127], a[60:63], v[204:207]\n\t"
    "s_waitcnt lgkmcnt(7)\n\t"
    "buffer_load_dwordx4 v15, s[20:23], s46 offen offset:3072 nt lds\n\t"
    "v_mfma_f32_16x16x32_bf16 v[244:247], a[128:131], a[0:3], 0\n\t"
    "s_waitcnt lgkmcnt(6)\n\t"
    "ds_read_b128 a[96:99], v40 offset:16384\n\t"
    "v_mul_f32 v23, 0xbdb8aa3b, v23\n\t"
    "v_mfma_f32_16x16x32_bf16 v[244:247], a[132:135], a[4:7], v[244:247]\n\t"
    "s_waitcnt lgkmcnt(6)\n\t"
    "v_cndmask_b32 v23, v23, v2, s[50:51]\n\t"
    "v_fma_f32 v230, v230, s34, v22\n\t"
    "v_mfma_f32_16x16x32_bf16 v[244:247], a[136:139], a[8:11], v[244:247]\n\t"
    "s_waitcnt lgkmcnt(5)\n\t"
    "ds_read_b128 a[100:103], v41 offset:16384\n\t"
    "v_fma_f32 v208, v208, s34, v23\n\t"
    "v_mfma_f32_16x16x32_bf16 v[244:247], a[140:143], a[12:15], v[244:247]\n\t"
    "s_waitcnt lgkmcnt(5)\n\t"
    "v_exp_f32 v208, v208\n\t"
    "v_mfma_f32_16x16x32_bf16 v[244:247], a[144:147], a[16:19], v[244:247]\n\t"
    "s_waitcnt lgkmcnt(4)\n\t"
    "ds_read_b128 a[104:107], v42 offset:16384\n\t"
    "v_fma_f32 v209, v209, s34, v23\n\t"
    "v_mfma_f32_16x16x32_bf16 v[244:247], a[148:151], a[20:23], v[244:247]\n\t"
    "s_waitcnt lgkmcnt(4)\n\t"
    "v_exp_f32 v209, v209\n\t"
    "v_mfma_f32_16x16x32_bf16 v[244:247], a[152:155], a[24:27], v[244:247]\n\t"
    "s_waitcnt lgkmcnt(3)\n\t"
    "ds_read_b128 a[108:111], v43 offset:16384\n\t"
    "v_fma_f32 v210, v210, s34, v23\n\t"
    "v_mfma_f32_16x16x32_bf16 v[244:247], a[156:159], a[28:31], v[244:247]\n\t"
    "v_exp_f32 v210, v210\n\t"
    "v_mfma_f32_16x16x32_bf16 v[248:251], a[128:131], a[32:35], 0\n\t"
    "s_add_u32 m0, s40, 102400\n\t"
    "s_add_u32 s47, s41, 102400\n\t"
    "ds_read_b128 a[112:115], v40 offset:16640\n\t"
    "v_mfma_f32_16x16x32_bf16 v[248:251], a[132:135], a[36:39], v[248:251]\n\t"
    "v_fma_f32 v211, v211, s34, v23\n\t"
    "v_cvt_pk_bf16_f32 v208, v208, v209\n\t"
    "v_mfma_f32_16x16x32_bf16 v[248:251], a[136:139], a[40:43], v[248:251]\n\t"
    "ds_read_b128 a[116:119], v41 offset:16640\n\t"
    "buffer_load_dwordx4 v12, s[20:23], s47 offen offset:0 nt lds\n\t"
    "v_mfma_f32_16x16x32_bf16 v[248:251], a[140:143], a[44:47], v[248:251]\n\t"
    "v_exp_f32 v211, v211\n\t"
    "v_mfma_f32_16x16x32_bf16 v[248:251], a[144:147], a[48:51], v[248:251]\n\t"
    "ds_read_b128 a[120:123], v42 offset:16640\n\t"
    "v_accvgpr_write_b32 a180, v208\n\t"
    "v_mfma_f32_16x16x32_bf16 v[248:251], a[148:151], a[52:55], v[248:251]\n\t"
    "v_cvt_pk_bf16_f32 v210, v210, v211\n\t"
    "v_fma_f32 v212, v212, s34, v23\n\t"
    "v_mfma_f32_16x16x32_bf16 v[248:251], a[152:155], a[56:59], v[248:251]\n\t"
    "ds_read_b128 a[124:127], v43 offset:16640\n\t"
    "v_accvgpr_write_b32 a181, v210\n\t"
    "v_mfma_f32_16x16x32_bf16 v[248:251], a[156:159], a[60:63], v[248:251]\n\t"
    ".p2align 12\n\t"
    "Lstub1_%=:\n\t"
    "s_cbranch_execz Lret_%=\n\t"
    "s_waitcnt lgkmcnt(7)\n\t"
    "v_exp_f32 v212, v212\n\t"
    "v_mfma_f32_16x16x32_bf16 v[252:255], a[96:99], a[0:3], 0\n\t"
    "s_waitcnt lgkmcnt(6)\n\t"
    "ds_read_b128 a[128:131], v40 offset:24576\n\t"
    "v_fma_f32 v213, v213, s34, v23\n\t"
    "v_mfma_f32_16x16x32_bf16 v[252:255], a[100:103], a[4:7], v[252:255]\n\t"
    "s_waitcnt lgkmcnt(6)\n\t"
    "v_exp_f32 v213, v213\n\t"
    "v_mfma_f32_16x16x32_bf16 v[252:255], a[104:107], a[8:11], v[252:255]\n\t"
    "s_waitcnt lgkmcnt(5)\n\t"
    "ds_read_b128 a[132:135], v41 offset:24576\n\t"
    "v_fma_f32 v214, v214, s34, v23\n\t"
    "v_mfma_f32_16x16x32_bf16 v[252:255], a[108:111], a[12:15], v[252:255]\n\t"
    "s_waitcnt lgkmcnt(5)\n\t"
    "v_exp_f32 v214, v214\n\t"
    "v_mfma_f32_16x16x32_bf16 v[252:255], a[112:115], a[16:19], v[252:255]\n\t"
    "s_waitcnt lgkmcnt(4)\n\t"
    "ds_read_b128 a[136:139], v42 offset:24576\n\t"
    "v_fma_f32 v215, v215, s34, v23\n\t"
    "v_mfma_f32_16x16x32_bf16 v[252:255], a[116:119], a[20:23], v[252:255]\n\t"
    "s_waitcnt lgkmcnt(4)\n\t"
    "v_exp_f32 v215, v215\n\t"
    "v_mfma_f32_16x16x32_bf16 v[252:255], a[120:123], a[24:27], v[252:255]\n\t"
    "s_waitcnt lgkmcnt(3)\n\t"
    "ds_read_b128 a[140:143], v43 offset:24576\n\t"
    "buffer_load_dwordx4 v13, s[20:23], s47 offen offset:1024 nt lds\n\t"
    "v_mfma_f32_16x16x32_bf16 v[252:255], a[124:127], a[28:31], v[252:255]\n\t"
    "v_cvt_pk_bf16_f32 v212, v212, v213\n\t"
    "v_cvt_pk_bf16_f32 v214, v214, v215\n\t"
    "v_mfma_f32_16x16x32_bf16 v[208:211], a[96:99], a[32:35], 0\n\t"
    "ds_read_b128 a[144:147], v40 offset:24832\n\t"
    "v_accvgpr_write_b32 a182, v212\n\t"
    "v_mfma_f32_16x16x32_bf16 v[208:211], a[100:103], a[36:39], v[208:211]\n\t"
    "v_accvgpr_write_b32 a183, v214\n\t"
    "v_fma_f32 v232, v232, s34, v23\n\t"
    "v_mfma_f32_16x16x32_bf16 v[208:211], a[104:107], a[40:43], v[208:211]\n\t"
    "ds_read_b128 a[148:151], v41 offset:24832\n\t"
    "v_fma_f32 v233, v233, s34, v23\n\t"
    "v_mfma_f32_16x16x32_bf16 v[208:211], a[108:111], a[44:47], v[208:211]\n\t"
    "v_exp_f32 v232, v232\n\t"
    "v_mfma_f32_16x16x32_bf16 v[208:211], a[112:115], a[48:51], v[208:211]\n\t"
    "ds_read_b128 a[152:155], v42 offset:24832\n\t"
    "v_fma_f32 v234, v234, s34, v23\n\t"
    "v_mfma_f32_16x16x32_bf16 v[208:211], a[116:119], a[52:55], v[208:211]\n\t"
    "v_exp_f32 v233, v233\n\t"
    "v_mfma_f32_16x16x32_bf16 v[208:211], a[120:123], a[56:59], v[208:211]\n\t"
    "ds_read_b128 a[156:159], v43 offset:24832\n\t"
    "v_fma_f32 v235, v235, s34, v23\n\t"
    "v_mfma_f32_16x16x32_bf16 v[208:211], a[124:127], a[60:63], v[208:211]\n\t"
    "s_waitcnt lgkmcnt(7)\n\t"
    "v_exp_f32 v234, v234\n\t"
    "v_mfma_f32_16x16x32_bf16 a[168:171], a[160:163], a[180:183], 0\n\t"
    "v_exp_f32 v235, v235\n\t"
    "v_mfma_f32_16x16x32_bf16 v[212:215], a[128:131], a[0:3], 0\n\t"
    "s_waitcnt lgkmcnt(6)\n\t"
    "v_cvt_pk_bf16_f32 v232, v232, v233\n\t"
    "v_cvt_pk_bf16_f32 v234, v234, v235\n\t"
    "v_mfma_f32_16x16x32_bf16 v[212:215], a[132:135], a[4:7], v[212:215]\n\t"
    "s_waitcnt lgkmcnt(5)\n\t"
    "buffer_load_dwordx4 v14, s[20:23], s47 offen offset:2048 nt lds\n\t"
    "v_mfma_f32_16x16x32_bf16 v[212:215], a[136:139], a[8:11], v[212:215]\n\t"
    "s_waitcnt lgkmcnt(4)\n\t"
    "v_accvgpr_write_b32 a192, v232\n\t"
    "v_accvgpr_write_b32 a193, v234\n\t"
    "v_mfma_f32_16x16x32_bf16 v[212:215], a[140:143], a[12:15], v[212:215]\n\t"
    "s_waitcnt lgkmcnt(3)\n\t"
    "v_fma_f32 v216, v216, s34, v23\n\t"
    "v_fma_f32 v217, v217, s34, v23\n\t"
    "v_mfma_f32_16x16x32_bf16 v[212:215], a[144:147], a[16:19], v[212:215]\n\t"
    "s_waitcnt lgkmcnt(2)\n\t"
    "v_exp_f32 v216, v216\n\t"
    "v_mfma_f32_16x16x32_bf16 v[212:215], a[148:151], a[20:23], v[212:215]\n\t"
    "s_waitcnt lgkmcnt(1)\n\t"
    "v_exp_f32 v217, v217\n\t"
    "v_mfma_f32_16x16x32_bf16 v[212:215], a[152:155], a[24:27], v[212:215]\n\t"
    "s_waitcnt lgkmcnt(0)\n\t"
    "v_fma_f32 v218, v218, s34, v23\n\t"
    "v_fma_f32 v219, v219, s34, v23\n\t"
    "v_mfma_f32_16x16x32_bf16 v[212:215], a[156:159], a[28:31], v[212:215]\n\t"
    "v_exp_f32 v218, v218\n\t"
    "v_mfma_f32_16x16x32_bf16 v[232:235], a[128:131], a[32:35], 0\n\t"
    "v_exp_f32 v219, v219\n\t"
    "v_mfma_f32_16x16x32_bf16 v[232:235], a[132:135], a[36:39], v[232:235]\n\t"
    "v_cvt_pk_bf16_f32 v216, v216, v217\n\t"
    "v_cvt_pk_bf16_f32 v218, v218, v219\n\t"
    "v_mfma_f32_16x16x32_bf16 v[232:235], a[136:139], a[40:43], v[232:235]\n\t"
    "v_accvgpr_write_b32 a194, v216\n\t"
    "v_accvgpr_write_b32 a195, v218\n\t"
    "v_mfma_f32_16x16x32_bf16 v[232:235], a[140:143], a[44:47], v[232:235]\n\t"
    "v_fma_f32 v236, v236, s34, v23\n\t"
    "v_fma_f32 v237, v237, s34, v23\n\t"
    "v_mfma_f32_16x16x32_bf16 v[232:235], a[144:147], a[48:51], v[232:235]\n\t"
    "v_exp_f32 v236, v236\n\t"
    "v_mfma_f32_16x16x32_bf16 v[232:235], a[148:151], a[52:55], v[232:235]\n\t"
    "buffer_load_dwordx4 v15, s[20:23], s47 offen offset:3072 nt lds\n\t"
    "v_mfma_f32_16x16x32_bf16 v[232:235], a[152:155], a[56:59], v[232:235]\n\t"
    "v_exp_f32 v237, v237\n\t"
    "v_mfma_f32_16x16x32_bf16 v[232:235], a[156:159], a[60:63], v[232:235]\n\t"
    "v_fma_f32 v238, v238, s34, v23\n\t"
    "v_fma_f32 v239, v239, s34, v23\n\t"
    "v_exp_f32 v238, v238\n\t"
    "v_exp_f32 v239, v239\n\t"
    "v_cvt_pk_bf16_f32 v236, v236, v237\n\t"
    "v_cvt_pk_bf16_f32 v238, v238, v239\n\t"
    "v_accvgpr_write_b32 a204, v236\n\t"
    "v_accvgpr_write_b32 a205, v238\n\t"
    "v_fma_f32 v220, v220, s34, v23\n\t"
    "v_fma_f32 v221, v221, s34, v23\n\t"
    "v_exp_f32 v220, v220\n\t"
    "v_exp_f32 v221, v221\n\t"
    "v_fma_f32 v222, v222, s34, v23\n\t"
    "v_fma_f32 v223, v223, s34, v23\n\t"
    "v_exp_f32 v222, v222\n\t"
    "v_exp_f32 v223, v223\n\t"
    "v_cvt_pk_bf16_f32 v220, v220, v221\n\t"
    "v_cvt_pk_bf16_f32 v222, v222, v223\n\t"
    "v_accvgpr_write_b32 a206, v220\n\t"
    "v_accvgpr_write_b32 a207, v222\n\t"
    "v_fma_f32 v240, v240, s34, v23\n\t"
    "v_fma_f32 v241, v241, s34, v23\n\t"
    "v_exp_f32 v240, v240\n\t"
    "v_exp_f32 v241, v241\n\t"
    "v_exp_f32 v228, v228\n\t"
    "v_exp_f32 v229, v229\n\t"
    "v_exp_f32 v230, v230\n\t"
    "v_fma_f32 v242, v242, s34, v23\n\t"
    "v_fma_f32 v243, v243, s34, v23\n\t"
    "v_exp_f32 v242, v242\n\t"
    "v_exp_f32 v243, v243\n\t"
    "v_cvt_pk_bf16_f32 v240, v240, v241\n\t"
    "v_cvt_pk_bf16_f32 v242, v242, v243\n\t"
    "v_accvgpr_write_b32 a216, v240\n\t"
    "v_accvgpr_write_b32 a217, v242\n\t"
    "v_fma_f32 v224, v224, s34, v23\n\t"
    "v_fma_f32 v225, v225, s34, v23\n\t"
    "v_exp_f32 v224, v224\n\t"
    "v_exp_f32 v225, v225\n\t"
    "v_fma_f32 v226, v226, s34, v23\n\t"
    "v_fma_f32 v227, v227, s34, v23\n\t"
    "v_exp_f32 v226, v226\n\t"
    "v_exp_f32 v227, v227\n\t"
    "v_cvt_pk_bf16_f32 v224, v224, v225\n\t"
    "v_cvt_pk_bf16_f32 v226, v226, v227\n\t"
    "v_accvgpr_write_b32 a218, v224\n\t"
    "v_accvgpr_write_b32 a219, v226\n\t"
    "v_fma_f32 v231, v231, s34, v22\n\t"
    "v_cvt_pk_bf16_f32 v228, v228, v229\n\t"
    "v_exp_f32 v231, v231\n\t"
    "v_fma_f32 v204, v204, s34, v23\n\t"
    "v_cvt_pk_bf16_f32 v229, v230, v231\n\t"
    "v_exp_f32 v204, v204\n\t"
    "v_fma_f32 v205, v205, s34, v23\n\t"
    "v_fma_f32 v206, v206, s34, v23\n\t"
    "v_exp_f32 v205, v205\n\t"
    "v_exp_f32 v206, v206\n\t"
    "v_fma_f32 v207, v207, s34, v23\n\t"
    "v_cvt_pk_bf16_f32 v204, v204, v205\n\t"
    "v_exp_f32 v207, v207\n\t"
    "v_fma_f32 v244, v244, s34, v22\n\t"
    "v_cvt_pk_bf16_f32 v205, v206, v207\n\t"
    "v_exp_f32 v244, v244\n\t"
    "v_fma_f32 v245, v245, s34, v22\n\t"
    "v_fma_f32 v246, v246, s34, v22\n\t"
    "v_exp_f32 v245, v245\n\t"
    "v_exp_f32 v246, v246\n\t"
    "v_fma_f32 v247, v247, s34, v22\n\t"
    "v_cvt_pk_bf16_f32 v230, v244, v245\n\t"
    "v_exp_f32 v247, v247\n\t"
    "v_fma_f32 v248, v248, s34, v23\n\t"
    "v_cvt_pk_bf16_f32 v231, v246, v247\n\t"
    "v_exp_f32 v248, v248\n\t"
    "v_fma_f32 v249, v249, s34, v23\n\t"
    "v_fma_f32 v250, v250, s34, v23\n\t"
    "v_exp_f32 v249, v249\n\t"
    "s_waitcnt vmcnt(8)\n\t"
    "ds_read_b128 a[64:67], v36 offset:0\n\t"
    "ds_read_b128 a[68:71], v37 offset:0\n\t"
    "ds_read_b128 a[72:75], v38 offset:0\n\t"
    "ds_read_b128 a[76:79], v39 offset:0\n\t"
    "ds_read_b128 a[80:83], v36 offset:256\n\t"
    "ds_read_b128 a[84:87], v37 offset:256\n\t"
    "ds_read_b128 a[88:91], v38 offset:256\n\t"
    "ds_read_b128 a[92:95], v39 offset:256\n\t"
    "s_waitcnt lgkmcnt(7)\n\t"
    "v_exp_f32 v250, v250\n\t"
    "v_mfma_f32_16x16x32_bf16 a[168:171], a[160:163], a[192:195], a[168:171]\n\t"
    "v_fma_f32 v251, v251, s34, v23\n\t"
    "v_cvt_pk_bf16_f32 v206, v248, v249\n\t"
    "v_mfma_f32_16x16x32_bf16 a[168:171], a[160:163], a[204:207], a[168:171]\n\t"
    "v_exp_f32 v251, v251\n\t"
    "v_mfma_f32_16x16x32_bf16 a[168:171], a[160:163], a[216:219], a[168:171]\n\t"
    "v_cvt_pk_bf16_f32 v207, v250, v251\n\t"
    "v_fma_f32 v252, v252, s34, v22\n\t"
    "v_mfma_f32_16x16x32_bf16 a[164:167], a[160:163], v[228:231], a[164:167]\n\t"
    "v_exp_f32 v252, v252\n\t"
    "v_mfma_f32_16x16x32_bf16 v[32:35], v[44:47], a[64:67], 0\n\t"
    "s_waitcnt lgkmcnt(6)\n\t"
    "v_fma_f32 v253, v253, s34, v22\n\t"
    "v_fma_f32 v254, v254, s34, v22\n\t"
    "v_mfma_f32_16x16x32_bf16 v[32:35], v[48:51], a[68:71], v[32:35]\n\t"
    "s_waitcnt lgkmcnt(5)\n\t"
    "v_exp_f32 v253, v253\n\t"
    "v_mfma_f32_16x16x32_bf16 v[32:35], v[52:55], a[72:75], v[32:35]\n\t"
    "s_waitcnt lgkmcnt(4)\n\t"
    "v_exp_f32 v254, v254\n\t"
    "v_mfma_f32_16x16x32_bf16 v[32:35], v[56:59], a[76:79], v[32:35]\n\t"
    "s_waitcnt lgkmcnt(3)\n\t"
    "v_fma_f32 v255, v255, s34, v22\n\t"
    "v_cvt_pk_bf16_f32 v252, v252, v253\n\t"
    "v_mfma_f32_16x16x32_bf16 v[32:35], v[60:63], a[80:83], v[32:35]\n\t"
    "s_waitcnt lgkmcnt(2)\n\t"
    "v_exp_f32 v255, v255\n\t"
    "v_mfma_f32_16x16x32_bf16 v[32:35], v[64:67], a[84:87], v[32:35]\n\t"
    "s_waitcnt lgkmcnt(1)\n\t"
    "v_cvt_pk_bf16_f32 v253, v254, v255\n\t"
    "v_fma_f32 v208, v208, s34, v23\n\t"
    "v_mfma_f32_16x16x32_bf16 v[32:35], v[68:71], a[88:91], v[32:35]\n\t"
    "s_waitcnt lgkmcnt(0)\n\t"
    "v_exp_f32 v208, v208\n\t"
    "v_mfma_f32_16x16x32_bf16 v[32:35], v[72:75], a[92:95], v[32:35]\n\t"
    "v_fma_f32 v209, v209, s34, v23\n\t"
    "v_fma_f32 v210, v210, s34, v23\n\t"
    "v_mfma_f32_16x16x32_bf16 a[168:171], a[160:163], v[204:207], a[168:171]\n\t"
    "v_exp_f32 v209, v209\n\t"
    "v_mfma_f32_16x16x32_bf16 v[36:39], v[76:79], a[64:67], 0\n\t"
    "s_add_u32 m0, s40, 131072\n\t"
    "s_add_u32 s46, s41, 131072\n\t"
    "ds_read_b128 a[96:99], v4 offset:32768\n\t"
    "v_mfma_f32_16x16x32_bf16 v[36:39], v[80:83], a[68:71], v[36:39]\n\t"
    "v_exp_f32 v210, v210\n\t"
    "v_mfma_f32_16x16x32_bf16 v[36:39], v[84:87], a[72:75], v[36:39]\n\t"
    "buffer_load_dwordx4 v12, s[20:23], s46 offen offset:0 nt lds\n\t"
    "v_mfma_f32_16x16x32_bf16 v[36:39], v[88:91], a[76:79], v[36:39]\n\t"
    "ds_read_b128 a[100:103], v5 offset:32768\n\t"
    "v_fma_f32 v211, v211, s34, v23\n\t"
    "v_mfma_f32_16x16x32_bf16 v[36:39], v[92:95], a[80:83], v[36:39]\n\t"
    "v_exp_f32 v211, v211\n\t"
    "v_mfma_f32_16x16x32_bf16 v[36:39], v[96:99], a[84:87], v[36:39]\n\t"
    "v_cvt_pk_bf16_f32 v208, v208, v209\n\t"
    "v_cvt_pk_bf16_f32 v209, v210, v211\n\t"
    "v_mfma_f32_16x16x32_bf16 v[36:39], v[100:103], a[88:91], v[36:39]\n\t"
    "ds_read_b128 a[104:107], v6 offset:32768\n\t"
    "v_fma_f32 v212, v212, s34, v22\n\t"
    "v_mfma_f32_16x16x32_bf16 v[36:39], v[104:107], a[92:95], v[36:39]\n\t"
    "v_exp_f32 v212, v212\n\t"
    "v_mfma_f32_16x16x32_bf16 v[44:47], v[108:111], a[64:67], 0\n\t"
    "v_fma_f32 v213, v213, s34, v22\n\t"
    "v_fma_f32 v214, v214, s34, v22\n\t"
    "v_mfma_f32_16x16x32_bf16 v[44:47], v[112:115], a[68:71], v[44:47]\n\t"
    "ds_read_b128 a[108:111], v7 offset:32768\n\t"
    "v_fma_f32 v215, v215, s34, v22\n\t"
    "v_mfma_f32_16x16x32_bf16 v[44:47], v[116:119], a[72:75], v[44:47]\n\t"
    "buffer_load_dwordx4 v13, s[20:23], s46 offen offset:1024 nt lds\n\t"
    "v_mfma_f32_16x16x32_bf16 v[44:47], v[120:123], a[76:79], v[44:47]\n\t"
    "v_exp_f32 v213, v213\n\t"
    "v_mfma_f32_16x16x32_bf16 v[44:47], v[124:127], a[80:83], v[44:47]\n\t"
    "ds_read_b128 a[112:115], v4 offset:33024\n\t"
    "v_cvt_pk_bf16_f32 v254, v212, v213\n\t"
    "v_mfma_f32_16x16x32_bf16 v[44:47], v[128:131], a[84:87], v[44:47]\n\t"
    "v_exp_f32 v214, v214\n\t"
    "v_mfma_f32_16x16x32_bf16 v[44:47], v[132:135], a[88:91], v[44:47]\n\t"
    "v_exp_f32 v215, v215\n\t"
    "v_mfma_f32_16x16x32_bf16 v[44:47], v[136:139], a[92:95], v[44:47]\n\t"
    "ds_read_b128 a[116:119], v5 offset:33024\n\t"
    "v_cvt_pk_bf16_f32 v255, v214, v215\n\t"
    "v_mfma_f32_16x16x32_bf16 v[48:51], v[140:143], a[64:67], 0\n\t"
    "v_fma_f32 v232, v232, s34, v23\n\t"
    "v_fma_f32 v233, v233, s34, v23\n\t"
    "v_mfma_f32_16x16x32_bf16 v[48:51], v[144:147], a[68:71], v[48:51]\n\t"
    "v_exp_f32 v232, v232\n\t"
    "v_mfma_f32_16x16x32_bf16 v[48:51], v[148:151], a[72:75], v[48:51]\n\t"
    "ds_read_b128 a[120:123], v6 offset:33024\n\t"
    "v_fma_f32 v234, v234, s34, v23\n\t"
    "v_mfma_f32_16x16x32_bf16 v[48:51], v[152:155], a[76:79], v[48:51]\n\t"
    "v_exp_f32 v233, v233\n\t"
    "v_mfma_f32_16x16x32_bf16 v[48:51], v[156:159], a[80:83], v[48:51]\n\t"
    "buffer_load_dwordx4 v14, s[20:23], s46 offen offset:2048 nt lds\n\t"
    "v_mfma_f32_16x16x32_bf16 v[48:51], v[160:163], a[84:87], v[48:51]\n\t"
    "ds_read_b128 a[124:127], v7 offset:33024\n\t"
    "v_fma_f32 v235, v235, s34, v23\n\t"
    "v_mfma_f32_16x16x32_bf16 v[48:51], v[164:167], a[88:91], v[48:51]\n\t"
    "v_exp_f32 v234, v234\n\t"
    "v_mfma_f32_16x16x32_bf16 v[48:51], v[168:171], a[92:95], v[48:51]\n\t"
    "s_waitcnt lgkmcnt(7)\n\t"
    "v_exp_f32 v235, v235\n\t"
    "v_mfma_f32_16x16x32_bf16 a[164:167], a[160:163], v[252:255], a[164:167]\n\t"
    "v_cvt_pk_bf16_f32 v210, v232, v233\n\t"
    "v_cvt_pk_bf16_f32 v211, v234, v235\n\t"
    "v_mfma_f32_16x16x32_bf16 v[52:55], a[96:99], a[64:67], 0\n\t"
    "s_waitcnt lgkmcnt(6)\n\t"
    "ds_read_b128 a[128:131], v4 offset:40960\n\t"
    "v_max3_f32 v24, v32, v33, v34\n\t"
    "v_mfma_f32_16x16x32_bf16 v[52:55], a[100:103], a[68:71], v[52:55]\n\t"
    "s_waitcnt lgkmcnt(6)\n\t"
    "ds_read_b128 a[132:135], v5 offset:40960\n\t"
    "v_max3_f32 v27, v35, v36, v37\n\t"
    "v_mfma_f32_16x16x32_bf16 v[52:55], a[104:107], a[72:75], v[52:55]\n\t"
    "s_waitcnt lgkmcnt(6)\n\t"
    "ds_read_b128 a[136:139], v6 offset:40960\n\t"
    "v_max3_f32 v24, v24, v38, v39\n\t"
    "v_mfma_f32_16x16x32_bf16 v[52:55], a[108:111], a[76:79], v[52:55]\n\t"
    "s_waitcnt lgkmcnt(6)\n\t"
    "ds_read_b128 a[140:143], v7 offset:40960\n\t"
    "v_max3_f32 v27, v27, v44, v45\n\t"
    "v_mfma_f32_16x16x32_bf16 v[52:55], a[112:115], a[80:83], v[52:55]\n\t"
    "s_waitcnt lgkmcnt(6)\n\t"
    "ds_read_b128 a[144:147], v4 offset:41216\n\t"
    "v_max3_f32 v24, v24, v46, v47\n\t"
    "v_mfma_f32_16x16x32_bf16 v[52:55], a[116:119], a[84:87], v[52:55]\n\t"
    "s_waitcnt lgkmcnt(6)\n\t"
    "ds_read_b128 a[148:151], v5 offset:41216\n\t"
    "v_max3_f32 v27, v27, v48, v49\n\t"
    "v_mfma_f32_16x16x32_bf16 v[52:55], a[120:123], a[88:91], v[52:55]\n\t"
    "s_waitcnt lgkmcnt(6)\n\t"
    "ds_read_b128 a[152:155], v6 offset:41216\n\t"
    "v_max3_f32 v24, v24, v50, v51\n\t"
    "v_mfma_f32_16x16x32_bf16 v[52:55], a[124:127], a[92:95], v[52:55]\n\t"
    "s_waitcnt lgkmcnt(6)\n\t"
    "ds_read_b128 a[156:159], v7 offset:41216\n\t"
    "buffer_load_dwordx4 v15, s[20:23], s46 offen offset:3072 nt lds\n\t"
    "v_mfma_f32_16x16x32_bf16 a[168:171], a[160:163], v[208:211], a[168:171]\n\t"
    "v_max_f32 v24, v24, v27\n\t"
    "v_mov_b32 v27, v24\n\t"
    "v_mfma_f32_16x16x32_bf16 v[56:59], a[128:131], a[64:67], 0\n\t"
    "s_waitcnt lgkmcnt(6)\n\t"
    "ds_read_b128 a[96:99], v40 offset:0\n\t"
    "v_mfma_f32_16x16x32_bf16 v[56:59], a[132:135], a[68:71], v[56:59]\n\t"
    "s_waitcnt lgkmcnt(6)\n\t"
    "v_permlane16_swap_b32 v24, v27\n\t"
    "v_mfma_f32_16x16x32_bf16 v[56:59], a[136:139], a[72:75], v[56:59]\n\t"
    "s_waitcnt lgkmcnt(5)\n\t"
    "v_max_f32 v24, v24, v27\n\t"
    "v_mov_b32 v27, v24\n\t"
    "v_mfma_f32_16x16x32_bf16 v[56:59], a[140:143], a[76:79], v[56:59]\n\t"
    "s_waitcnt lgkmcnt(4)\n\t"
    "ds_read_b128 a[100:103], v41 offset:0\n\t"
    "v_mfma_f32_16x16x32_bf16 v[56:59], a[144:147], a[80:83], v[56:59]\n\t"
    "s_waitcnt lgkmcnt(4)\n\t"
    "v_permlane32_swap_b32 v24, v27\n\t"
    "v_mfma_f32_16x16x32_bf16 v[56:59], a[148:151], a[84:87], v[56:59]\n\t"
    "s_waitcnt lgkmcnt(3)\n\t"
    "v_max_f32 v24, v24, v27\n\t"
    "v_mul_f32 v24, 0xbdb8aa3b, v24\n\t"
    "v_mfma_f32_16x16x32_bf16 v[56:59], a[152:155], a[88:91], v[56:59]\n\t"
    "s_waitcnt lgkmcnt(2)\n\t"
    "ds_read_b128 a[104:107], v42 offset:0\n\t"
    "v_cndmask_b32 v24, v24, v3, s[50:51]\n\t"
    "v_mfma_f32_16x16x32_bf16 v[56:59], a[156:159], a[92:95], v[56:59]\n\t"
    "v_fma_f32 v32, v32, s34, v24\n\t"
    "v_fma_f32 v33, v33, s34, v24\n\t"
    "v_mfma_f32_16x16x32_bf16 v[60:63], a[224:227], a[64:67], 0\n\t"
    "s_add_u32 m0, s40, 135168\n\t"
    "s_add_u32 s47, s41, 135168\n\t"
    "v_fma_f32 v34, v34, s34, v24\n\t"
    "v_mfma_f32_16x16x32_bf16 v[60:63], a[228:231], a[68:71], v[60:63]\n\t"
    "ds_read_b128 a[108:111], v43 offset:0\n\t"
    "v_fma_f32 v35, v35, s34, v24\n\t"
    "v_mfma_f32_16x16x32_bf16 v[60:63], a[232:235], a[72:75], v[60:63]\n\t"
    "buffer_load_dwordx4 v12, s[20:23], s47 offen offset:0 nt lds\n\t"
    "v_mfma_f32_16x16x32_bf16 v[60:63], a[236:239], a[76:79], v[60:63]\n\t"
    "v_exp_f32 v32, v32\n\t"
    "v_mfma_f32_16x16x32_bf16 v[60:63], a[240:243], a[80:83], v[60:63]\n\t"
    "ds_read_b128 a[112:115], v40 offset:256\n\t"
    "v_fma_f32 v36, v36, s34, v24\n\t"
    "v_mfma_f32_16x16x32_bf16 v[60:63], a[244:247], a[84:87], v[60:63]\n\t"
    "v_exp_f32 v33, v33\n\t"
    "v_mfma_f32_16x16x32_bf16 v[60:63], a[248:251], a[88:91], v[60:63]\n\t"
    "v_exp_f32 v34, v34\n\t"
    "v_mfma_f32_16x16x32_bf16 v[60:63], a[252:255], a[92:95], v[60:63]\n\t"
    "ds_read_b128 a[116:119], v41 offset:256\n\t"
    "v_cvt_pk_bf16_f32 v32, v32, v33\n\t"
    "v_mfma_f32_16x16x32_bf16 v[64:67], v[172:175], a[64:67], 0\n\t"
    "v_exp_f32 v35, v35\n\t"
    "v_mfma_f32_16x16x32_bf16 v[64:67], v[176:179], a[68:71], v[64:67]\n\t"
    "v_exp_f32 v36, v36\n\t"
    "v_mfma_f32_16x16x32_bf16 v[64:67], v[180:183], a[72:75], v[64:67]\n\t"
    "ds_read_b128 a[120:123], v42 offset:256\n\t"
    "v_accvgpr_write_b32 a184, v32\n\t"
    "v_mfma_f32_16x16x32_bf16 v[64:67], v[184:187], a[76:79], v[64:67]\n\t"
    "v_cvt_pk_bf16_f32 v34, v34, v35\n\t"
    "v_fma_f32 v37, v37, s34, v24\n\t"
    "v_mfma_f32_16x16x32_bf16 v[64:67], v[188:191], a[80:83], v[64:67]\n\t"
    "buffer_load_dwordx4 v13, s[20:23], s47 offen offset:1024 nt lds\n\t"
    "v_mfma_f32_16x16x32_bf16 v[64:67], v[192:195], a[84:87], v[64:67]\n\t"
    "ds_read_b128 a[124:127], v43 offset:256\n\t"
    "v_accvgpr_write_b32 a185, v34\n\t"
    "v_mfma_f32_16x16x32_bf16 v[64:67], v[196:199], a[88:91], v[64:67]\n\t"
    "v_exp_f32 v37, v37\n\t"
    "v_mfma_f32_16x16x32_bf16 v[64:67], v[200:203], a[92:95], v[64:67]\n\t"
    "s_waitcnt lgkmcnt(7)\n\t"
    "v_fma_f32 v38, v38, s34, v24\n\t"
    "v_fma_f32 v39, v39, s34, v24\n\t"
    "v_mfma_f32_16x16x32_bf16 v[32:35], a[96:99], a[64:67], 0\n\t"
    "s_waitcnt lgkmcnt(6)\n\t"
    "ds_read_b128 a[128:131], v40 offset:8192\n\t"
    "v_cvt_pk_bf16_f32 v36, v36, v37\n\t"
    "v_mfma_f32_16x16x32_bf16 v[32:35], a[100:103], a[68:71], v[32:35]\n\t"
    "s_waitcnt lgkmcnt(6)\n\t"
    "ds_read_b128 a[132:135], v41 offset:8192\n\t"
    "v_accvgpr_write_b32 a186, v36\n\t"
    "v_mfma_f32_16x16x32_bf16 v[32:35], a[104:107], a[72:75], v[32:35]\n\t"
    "s_waitcnt lgkmcnt(6)\n\t"
    "ds_read_b128 a[136:139], v42 offset:8192\n\t"
    "v_fma_f32 v44, v44, s34, v24\n\t"
    "v_mfma_f32_16x16x32_bf16 v[32:35], a[108:111], a[76:79], v[32:35]\n\t"
    "s_waitcnt lgkmcnt(6)\n\t"
    "ds_read_b128 a[140:143], v43 offset:8192\n\t"
    "v_fma_f32 v45, v45, s34, v24\n\t"
    "v_mfma_f32_16x16x32_bf16 v[32:35], a[112:115], a[80:83], v[32:35]\n\t"
    "s_waitcnt lgkmcnt(6)\n\t"
    "ds_read_b128 a[144:147], v40 offset:8448\n\t"
    "v_fma_f32 v46, v46, s34, v24\n\t"
    "v_mfma_f32_16x16x32_bf16 v[32:35], a[116:119], a[84:87], v[32:35]\n\t"
    "s_waitcnt lgkmcnt(6)\n\t"
    "ds_read_b128 a[148:151], v41 offset:8448\n\t"
    "v_fma_f32 v47, v47, s34, v24\n\t"
    "v_mfma_f32_16x16x32_bf16 v[32:35], a[120:123], a[88:91], v[32:35]\n\t"
    "s_waitcnt lgkmcnt(6)\n\t"
    "ds_read_b128 a[152:155], v42 offset:8448\n\t"
    "v_fma_f32 v48, v48, s34, v24\n\t"
    "v_mfma_f32_16x16x32_bf16 v[32:35], a[124:127], a[92:95], v[32:35]\n\t"
    "s_waitcnt lgkmcnt(6)\n\t"
    "ds_read_b128 a[156:159], v43 offset:8448\n\t"
    "buffer_load_dwordx4 v14, s[20:23], s47 offen offset:2048 nt lds\n\t"
    "v_mfma_f32_16x16x32_bf16 v[68:71], a[128:131], a[64:67], 0\n\t"
    "s_waitcnt lgkmcnt(6)\n\t"
    "ds_read_b128 a[96:99], v40 offset:16384\n\t"
    "v_fma_f32 v49, v49, s34, v24\n\t"
    "v_mfma_f32_16x16x32_bf16 v[68:71], a[132:135], a[68:71], v[68:71]\n\t"
    "s_waitcnt lgkmcnt(6)\n\t"
    "ds_read_b128 a[100:103], v41 offset:16384\n\t"
    "v_fma_f32 v50, v50, s34, v24\n\t"
    "v_mfma_f32_16x16x32_bf16 v[68:71], a[136:139], a[72:75], v[68:71]\n\t"
    "s_waitcnt lgkmcnt(6)\n\t"
    "ds_read_b128 a[104:107], v42 offset:16384\n\t"
    "v_fma_f32 v51, v51, s34, v24\n\t"
    "v_mfma_f32_16x16x32_bf16 v[68:71], a[140:143], a[76:79], v[68:71]\n\t"
    "s_waitcnt lgkmcnt(6)\n\t"
    "ds_read_b128 a[108:111], v43 offset:16384\n\t"
    "v_fma_f32 v52, v52, s34, v24\n\t"
    "v_mfma_f32_16x16x32_bf16 v[68:71], a[144:147], a[80:83], v[68:71]\n\t"
    "s_waitcnt lgkmcnt(6)\n\t"
    "ds_read_b128 a[112:115], v40 offset:16640\n\t"
    "v_fma_f32 v53, v53, s34, v24\n\t"
    "v_mfma_f32_16x16x32_bf16 v[68:71], a[148:151], a[84:87], v[68:71]\n\t"
    "s_waitcnt lgkmcnt(6)\n\t"
    "ds_read_b128 a[116:119], v41 offset:16640\n\t"
    "v_fma_f32 v54, v54, s34, v24\n\t"
    "v_mfma_f32_16x16x32_bf16 v[68:71], a[152:155], a[88:91], v[68:71]\n\t"
    "s_waitcnt lgkmcnt(6)\n\t"
    "ds_read_b128 a[120:123], v42 offset:16640\n\t"
    "v_fma_f32 v55, v55, s34, v24\n\t"
    "v_mfma_f32_16x16x32_bf16 v[68:71], a[156:159], a[92:95], v[68:71]\n\t"
    "s_waitcnt lgkmcnt(6)\n\t"
    "ds_read_b128 a[124:127], v43 offset:16640\n\t"
    "v_fma_f32 v56, v56, s34, v24\n\t"
    "v_mfma_f32_16x16x32_bf16 v[72:75], a[96:99], a[64:67], 0\n\t"
    "s_waitcnt lgkmcnt(6)\n\t"
    "ds_read_b128 a[128:131], v40 offset:24576\n\t"
    "v_fma_f32 v57, v57, s34, v24\n\t"
    "v_mfma_f32_16x16x32_bf16 v[72:75], a[100:103], a[68:71], v[72:75]\n\t"
    "s_waitcnt lgkmcnt(6)\n\t"
    "ds_read_b128 a[132:135], v41 offset:24576\n\t"
    "v_fma_f32 v58, v58, s34, v24\n\t"
    "v_mfma_f32_16x16x32_bf16 v[72:75], a[104:107], a[72:75], v[72:75]\n\t"
    "s_waitcnt lgkmcnt(6)\n\t"
    "ds_read_b128 a[136:139], v42 offset:24576\n\t"
    "buffer_load_dwordx4 v15, s[20:23], s47 offen offset:3072 nt lds\n\t"
    "v_mfma_f32_16x16x32_bf16 v[72:75], a[108:111], a[76:79], v[72:75]\n\t"
    "s_waitcnt lgkmcnt(6)\n\t"
    "ds_read_b128 a[140:143], v43 offset:24576\n\t"
    "v_fma_f32 v59, v59, s34, v24\n\t"
    "v_mfma_f32_16x16x32_bf16 v[72:75], a[112:115], a[80:83], v[72:75]\n\t"
    "s_waitcnt vmcnt(8) lgkmcnt(6)\n\t"
    "s_barrier\n\t"
    "ds_read_b128 a[144:147], v40 offset:24832\n\t"
    "v_mfma_f32_16x16x32_bf16 v[72:75], a[116:119], a[84:87], v[72:75]\n\t"
    "s_waitcnt lgkmcnt(6)\n\t"
    "ds_read_b128 a[148:151], v41 offset:24832\n\t"
    "v_fma_f32 v60, v60, s34, v24\n\t"
    "v_mfma_f32_16x16x32_bf16 v[72:75], a[120:123], a[88:91], v[72:75]\n\t"
    "s_waitcnt lgkmcnt(6)\n\t"
    "ds_read_b128 a[152:155], v42 offset:24832\n\t"
    "v_fma_f32 v61, v61, s34, v24\n\t"
    "v_mfma_f32_16x16x32_bf16 v[72:75], a[124:127], a[92:95], v[72:75]\n\t"
    "s_waitcnt lgkmcnt(6)\n\t"
    "ds_read_b128 a[156:159], v43 offset:24832\n\t"
    "v_fma_f32 v62, v62, s34, v24\n\t"
    "v_mfma_f32_16x16x32_bf16 v[76:79], a[128:131], a[64:67], 0\n\t"
    "s_waitcnt lgkmcnt(6)\n\t"
    "ds_read_b128 a[96:99], v40 offset:32768\n\t"
    "v_fma_f32 v63, v63, s34, v24\n\t"
    "v_mfma_f32_16x16x32_bf16 v[76:79], a[132:135], a[68:71], v[76:79]\n\t"
    "s_waitcnt lgkmcnt(6)\n\t"
    "ds_read_b128 a[100:103], v41 offset:32768\n\t"
    "v_fma_f32 v64, v64, s34, v24\n\t"
    "v_mfma_f32_16x16x32_bf16 v[76:79], a[136:139], a[72:75], v[76:79]\n\t"
    "s_waitcnt lgkmcnt(6)\n\t"
    "ds_read_b128 a[104:107], v42 offset:32768\n\t"
    "v_fma_f32 v65, v65, s34, v24\n\t"
    "v_mfma_f32_16x16x32_bf16 v[76:79], a[140:143], a[76:79], v[76:79]\n\t"
    "s_waitcnt lgkmcnt(6)\n\t"
    "ds_read_b128 a[108:111], v43 offset:32768\n\t"
    "v_fma_f32 v66, v66, s34, v24\n\t"
    "v_mfma_f32_16x16x32_bf16 v[76:79], a[144:147], a[80:83], v[76:79]\n\t"
    "s_waitcnt lgkmcnt(6)\n\t"
    "ds_read_b128 a[112:115], v40 offset:33024\n\t"
    "v_fma_f32 v67, v67, s34, v24\n\t"
    "v_mfma_f32_16x16x32_bf16 v[76:79], a[148:151], a[84:87], v[76:79]\n\t"
    "s_waitcnt lgkmcnt(6)\n\t"
    "ds_read_b128 a[116:119], v41 offset:33024\n\t"
    "v_fma_f32 v32, v32, s34, v24\n\t"
    "v_mfma_f32_16x16x32_bf16 v[76:79], a[152:155], a[88:91], v[76:79]\n\t"
    "s_waitcnt lgkmcnt(6)\n\t"
    "ds_read_b128 a[120:123], v42 offset:33024\n\t"
    "v_fma_f32 v33, v33, s34, v24\n\t"
    "v_mfma_f32_16x16x32_bf16 v[76:79], a[156:159], a[92:95], v[76:79]\n\t"
    "s_waitcnt lgkmcnt(6)\n\t"
    "ds_read_b128 a[124:127], v43 offset:33024\n\t"
    "v_fma_f32 v34, v34, s34, v24\n\t"
    "v_mfma_f32_16x16x32_bf16 v[80:83], a[96:99], a[0:3], 0\n\t"
    "s_waitcnt lgkmcnt(6)\n\t"
    "ds_read_b128 a[128:131], v40 offset:40960\n\t"
    "v_fma_f32 v35, v35, s34, v24\n\t"
    "v_mfma_f32_16x16x32_bf16 v[80:83], a[100:103], a[4:7], v[80:83]\n\t"
    "s_waitcnt lgkmcnt(6)\n\t"
    "v_exp_f32 v38, v38\n\t"
    "v_mfma_f32_16x16x32_bf16 v[80:83], a[104:107], a[8:11], v[80:83]\n\t"
    "s_waitcnt lgkmcnt(5)\n\t"
    "v_exp_f32 v39, v39\n\t"
    "v_mfma_f32_16x16x32_bf16 v[80:83], a[108:111], a[12:15], v[80:83]\n\t"
    "s_waitcnt lgkmcnt(4)\n\t"
    "ds_read_b128 a[132:135], v41 offset:40960\n\t"
    "v_cvt_pk_bf16_f32 v38, v38, v39\n\t"
    "v_mfma_f32_16x16x32_bf16 v[80:83], a[112:115], a[16:19], v[80:83]\n\t"
    "s_waitcnt lgkmcnt(4)\n\t"
    "v_exp_f32 v44, v44\n\t"
    "v_mfma_f32_16x16x32_bf16 v[80:83], a[116:119], a[20:23], v[80:83]\n\t"
    "s_waitcnt lgkmcnt(3)\n\t"
    "v_exp_f32 v45, v45\n\t"
    "v_mfma_f32_16x16x32_bf16 v[80:83], a[120:123], a[24:27], v[80:83]\n\t"
    "s_waitcnt lgkmcnt(2)\n\t"
    "ds_read_b128 a[136:139], v42 offset:40960\n\t"
    "v_accvgpr_write_b32 a187, v38\n\t"
    "v_mfma_f32_16x16x32_bf16 v[80:83], a[124:127], a[28:31], v[80:83]\n\t"
    "s_add_u32 m0, s40, 0\n\t"
    "s_add_u32 s46, s41, 163840\n\t"
    "buffer_load_dwordx4 v12, s[20:23], s46 offen offset:0 nt lds\n\t"
    "v_mfma_f32_16x16x32_bf16 a[172:175], a[160:163], a[184:187], 0\n\t"
    "v_exp_f32 v46, v46\n\t"
    "v_mfma_f32_16x16x32_bf16 v[36:39], a[96:99], a[32:35], 0\n\t"
    "v_exp_f32 v47, v47\n\t"
    "v_mfma_f32_16x16x32_bf16 v[36:39], a[100:103], a[36:39], v[36:39]\n\t"
    "ds_read_b128 a[140:143], v43 offset:40960\n\t"
    "v_cvt_pk_bf16_f32 v44, v44, v45\n\t"
    "v_mfma_f32_16x16x32_bf16 v[36:39], a[104:107], a[40:43], v[36:39]\n\t"
    "buffer_load_dwordx4 v13, s[20:23], s46 offen offset:1024 nt lds\n\t"
    "v_mfma_f32_16x16x32_bf16 v[36:39], a[108:111], a[44:47], v[36:39]\n\t"
    "v_exp_f32 v48, v48\n\t"
    "v_mfma_f32_16x16x32_bf16 v[36:39], a[112:115], a[48:51], v[36:39]\n\t"
    "ds_read_b128 a[144:147], v40 offset:41216\n\t"
    "v_accvgpr_write_b32 a196, v44\n\t"
    "v_mfma_f32_16x16x32_bf16 v[36:39], a[116:119], a[52:55], v[36:39]\n\t"
    "v_exp_f32 v49, v49\n\t"
    "v_mfma_f32_16x16x32_bf16 v[36:39], a[120:123], a[56:59], v[36:39]\n\t"
    "buffer_load_dwordx4 v14, s[20:23], s46 offen offset:2048 nt lds\n\t"
    "v_mfma_f32_16x16x32_bf16 v[36:39], a[124:127], a[60:63], v[36:39]\n\t"
    "ds_read_b128 a[148:151], v41 offset:41216\n\t"
    "v_exp_f32 v50, v50\n\t"
    "v_mfma_f32_16x16x32_bf16 v[84:87], a[96:99], a[64:67], 0\n\t"
    "v_exp_f32 v51, v51\n\t"
    "v_mfma_f32_16x16x32_bf16 v[84:87], a[100:103], a[68:71], v[84:87]\n\t"
    "v_exp_f32 v52, v52\n\t"
    "v_mfma_f32_16x16x32_bf16 v[84:87], a[104:107], a[72:75], v[84:87]\n\t"
    "ds_read_b128 a[152:155], v42 offset:41216\n\t"
    "v_cvt_pk_bf16_f32 v46, v46, v47\n\t"
    "v_mfma_f32_16x16x32_bf16 v[84:87], a[108:111], a[76:79], v[84:87]\n\t"
    "buffer_load_dwordx4 v15, s[20:23], s46 offen offset:3072 nt lds\n\t"
    "v_mfma_f32_16x16x32_bf16 v[84:87], a[112:115], a[80:83], v[84:87]\n\t"
    "v_exp_f32 v53, v53\n\t"
    "v_mfma_f32_16x16x32_bf16 v[84:87], a[116:119], a[84:87], v[84:87]\n\t"
    "s_add_u32 m0, s40, 4096\n\t"
    "ds_read_b128 a[156:159], v43 offset:41216\n\t"
    "v_accvgpr_write_b32 a197, v46\n\t"
    "v_mfma_f32_16x16x32_bf16 v[84:87], a[120:123], a[88:91], v[84:87]\n\t"
    "s_add_u32 s47, s41, 167936\n\t"
    "v_exp_f32 v54, v54\n\t"
    "v_mfma_f32_16x16x32_bf16 v[84:87], a[124:127], a[92:95], v[84:87]\n\t"
    "s_waitcnt lgkmcnt(7)\n\t"
    "v_exp_f32 v55, v55\n\t"
    "v_cvt_pk_bf16_f32 v48, v48, v49\n\t"
    "v_mfma_f32_16x16x32_bf16 v[44:47], a[128:131], a[0:3], 0\n\t"
    "s_waitcnt lgkmcnt(6)\n\t"
    "ds_read_b128 a[96:99], v40 offset:49152\n\t"
    "v_accvgpr_write_b32 a198, v48\n\t"
    "v_mfma_f32_16x16x32_bf16 v[44:47], a[132:135], a[4:7], v[44:47]\n\t"
    "s_waitcnt lgkmcnt(6)\n\t"
    ".p2align 12\n\t"
    "Lstub2_%=:\n\t"
    "s_cbranch_execz Lret_%=\n\t"
    "buffer_load_dwordx4 v12, s[20:23], s47 offen offset:0 nt lds\n\t"
    "v_mfma_f32_16x16x32_bf16 v[44:47], a[136:139], a[8:11], v[44:47]\n\t"
    "s_waitcnt lgkmcnt(5)\n\t"
    "v_exp_f32 v56, v56\n\t"
    "v_mfma_f32_16x16x32_bf16 v[44:47], a[140:143], a[12:15], v[44:47]\n\t"
    "s_waitcnt lgkmcnt(4)\n\t"
    "ds_read_b128 a[100:103], v41 offset:49152\n\t"
    "v_cvt_pk_bf16_f32 v50, v50, v51\n\t"
    "v_mfma_f32_16x16x32_bf16 v[44:47], a[144:147], a[16:19], v[44:47]\n\t"
    "s_waitcnt lgkmcnt(4)\n\t"
    "v_exp_f32 v57, v57\n\t"
    "v_mfma_f32_16x16x32_bf16 v[44:47], a[148:151], a[20:23], v[44:47]\n\t"
    "s_waitcnt lgkmcnt(3)\n\t"
    "buffer_load_dwordx4 v13, s[20:23], s47 offen offset:1024 nt lds\n\t"
    "v_mfma_f32_16x16x32_bf16 v[44:47], a[152:155], a[24:27], v[44:47]\n\t"
    "s_waitcnt lgkmcnt(2)\n\t"
    "ds_read_b128 a[104:107], v42 offset:49152\n\t"
    "v_accvgpr_write_b32 a199, v50\n\t"
    "v_mfma_f32_16x16x32_bf16 v[44:47], a[156:159], a[28:31], v[44:47]\n\t"
    "v_exp_f32 v58, v58\n\t"
    "v_cvt_pk_bf16_f32 v52, v52, v53\n\t"
    "v_mfma_f32_16x16x32_bf16 a[172:175], a[160:163], a[196:199], a[172:175]\n\t"
    "v_exp_f32 v59, v59\n\t"
    "v_mfma_f32_16x16x32_bf16 v[88:91], a[128:131], a[32:35], 0\n\t"
    "v_exp_f32 v60, v60\n\t"
    "v_mfma_f32_16x16x32_bf16 v[88:91], a[132:135], a[36:39], v[88:91]\n\t"
    "ds_read_b128 a[108:111], v43 offset:49152\n\t"
    "v_accvgpr_write_b32 a208, v52\n\t"
    "v_mfma_f32_16x16x32_bf16 v[88:91], a[136:139], a[40:43], v[88:91]\n\t"
    "buffer_load_dwordx4 v14, s[20:23], s47 offen offset:2048 nt lds\n\t"
    "v_mfma_f32_16x16x32_bf16 v[88:91], a[140:143], a[44:47], v[88:91]\n\t"
    "v_exp_f32 v61, v61\n\t"
    "v_mfma_f32_16x16x32_bf16 v[88:91], a[144:147], a[48:51], v[88:91]\n\t"
    "ds_read_b128 a[112:115], v40 offset:49408\n\t"
    "v_cvt_pk_bf16_f32 v54, v54, v55\n\t"
    "v_mfma_f32_16x16x32_bf16 v[88:91], a[148:151], a[52:55], v[88:91]\n\t"
    "v_exp_f32 v62, v62\n\t"
    "v_mfma_f32_16x16x32_bf16 v[88:91], a[152:155], a[56:59], v[88:91]\n\t"
    "v_exp_f32 v63, v63\n\t"
    "v_mfma_f32_16x16x32_bf16 v[88:91], a[156:159], a[60:63], v[88:91]\n\t"
    "ds_read_b128 a[116:119], v41 offset:49408\n\t"
    "v_exp_f32 v64, v64\n\t"
    "v_mfma_f32_16x16x32_bf16 v[48:51], a[128:131], a[64:67], 0\n\t"
    "buffer_load_dwordx4 v15, s[20:23], s47 offen offset:3072 nt lds\n\t"
    "v_mfma_f32_16x16x32_bf16 v[48:51], a[132:135], a[68:71], v[48:51]\n\t"
    "v_exp_f32 v65, v65\n\t"
    "v_mfma_f32_16x16x32_bf16 v[48:51], a[136:139], a[72:75], v[48:51]\n\t"
    "ds_read_b128 a[120:123], v42 offset:49408\n\t"
    "v_accvgpr_write_b32 a209, v54\n\t"
    "v_mfma_f32_16x16x32_bf16 v[48:51], a[140:143], a[76:79], v[48:51]\n\t"
    "s_add_u32 m0, s40, 32768\n\t"
    "v_exp_f32 v66, v66\n\t"
    "v_mfma_f32_16x16x32_bf16 v[48:51], a[144:147], a[80:83], v[48:51]\n\t"
    "s_add_u32 s46, s41, 196608\n\t"
    "v_exp_f32 v67, v67\n\t"
    "v_mfma_f32_16x16x32_bf16 v[48:51], a[148:151], a[84:87], v[48:51]\n\t"
    "ds_read_b128 a[124:127], v43 offset:49408\n\t"
    "v_cvt_pk_bf16_f32 v56, v56, v57\n\t"
    "v_mfma_f32_16x16x32_bf16 v[48:51], a[152:155], a[88:91], v[48:51]\n\t"
    "buffer_load_dwordx4 v12, s[20:23], s46 offen offset:0 nt lds\n\t"
    "v_mfma_f32_16x16x32_bf16 v[48:51], a[156:159], a[92:95], v[48:51]\n\t"
    "s_waitcnt lgkmcnt(7)\n\t"
    "v_exp_f32 v32, v32\n\t"
    "v_accvgpr_write_b32 a210, v56\n\t"
    "v_mfma_f32_16x16x32_bf16 v[52:55], a[96:99], a[0:3], 0\n\t"
    "s_waitcnt lgkmcnt(6)\n\t"
    "ds_read_b128 a[128:131], v40 offset:57344\n\t"
    "v_cvt_pk_bf16_f32 v58, v58, v59\n\t"
    "v_mfma_f32_16x16x32_bf16 v[52:55], a[100:103], a[4:7], v[52:55]\n\t"
    "s_waitcnt lgkmcnt(6)\n\t"
    "v_exp_f32 v33, v33\n\t"
    "v_mfma_f32_16x16x32_bf16 v[52:55], a[104:107], a[8:11], v[52:55]\n\t"
    "s_waitcnt lgkmcnt(5)\n\t"
    "v_exp_f32 v34, v34\n\t"
    "v_mfma_f32_16x16x32_bf16 v[52:55], a[108:111], a[12:15], v[52:55]\n\t"
    "s_waitcnt lgkmcnt(4)\n\t"
    "ds_read_b128 a[132:135], v41 offset:57344\n\t"
    "v_accvgpr_write_b32 a211, v58\n\t"
    "v_mfma_f32_16x16x32_bf16 v[52:55], a[112:115], a[16:19], v[52:55]\n\t"
    "s_waitcnt lgkmcnt(4)\n\t"
    "buffer_load_dwordx4 v13, s[20:23], s46 offen offset:1024 nt lds\n\t"
    "v_mfma_f32_16x16x32_bf16 v[52:55], a[116:119], a[20:23], v[52:55]\n\t"
    "s_waitcnt lgkmcnt(3)\n\t"
    "v_exp_f32 v35, v35\n\t"
    "v_mfma_f32_16x16x32_bf16 v[52:55], a[120:123], a[24:27], v[52:55]\n\t"
    "s_waitcnt lgkmcnt(2)\n\t"
    "ds_read_b128 a[136:139], v42 offset:57344\n\t"
    "v_cvt_pk_bf16_f32 v60, v60, v61\n\t"
    "v_mfma_f32_16x16x32_bf16 v[52:55], a[124:127], a[28:31], v[52:55]\n\t"
    "v_accvgpr_write_b32 a220, v60\n\t"
    "v_cvt_pk_bf16_f32 v62, v62, v63\n\t"
    "v_cvt_pk_bf16_f32 v64, v64, v65\n\t"
    "v_mfma_f32_16x16x32_bf16 a[172:175], a[160:163], a[208:211], a[172:175]\n\t"
    "v_accvgpr_write_b32 a221, v62\n\t"
    "v_accvgpr_write_b32 a222, v64\n\t"
    "v_mfma_f32_16x16x32_bf16 v[56:59], a[96:99], a[32:35], 0\n\t"
    "v_cvt_pk_bf16_f32 v66, v66, v67\n\t"
    "v_cvt_pk_bf16_f32 v32, v32, v33\n\t"
    "v_mfma_f32_16x16x32_bf16 v[56:59], a[100:103], a[36:39], v[56:59]\n\t"
    "ds_read_b128 a[140:143], v43 offset:57344\n\t"
    "v_accvgpr_write_b32 a223, v66\n\t"
    "v_mfma_f32_16x16x32_bf16 v[56:59], a[104:107], a[40:43], v[56:59]\n\t"
    "buffer_load_dwordx4 v14, s[20:23], s46 offen offset:2048 nt lds\n\t"
    "v_mfma_f32_16x16x32_bf16 v[56:59], a[108:111], a[44:47], v[56:59]\n\t"
    "v_cvt_pk_bf16_f32 v33, v34, v35\n\t"
    "v_fma_f32 v68, v68, s34, v24\n\t"
    "v_mfma_f32_16x16x32_bf16 v[56:59], a[112:115], a[48:51], v[56:59]\n\t"
    "ds_read_b128 a[144:147], v40 offset:57600\n\t"
    "v_fma_f32 v69, v69, s34, v24\n\t"
    "v_mfma_f32_16x16x32_bf16 v[56:59], a[116:119], a[52:55], v[56:59]\n\t"
    "v_exp_f32 v68, v68\n\t"
    "v_mfma_f32_16x16x32_bf16 v[56:59], a[120:123], a[56:59], v[56:59]\n\t"
    "buffer_load_dwordx4 v15, s[20:23], s46 offen offset:3072 nt lds\n\t"
    "v_mfma_f32_16x16x32_bf16 v[56:59], a[124:127], a[60:63], v[56:59]\n\t"
    "ds_read_b128 a[148:151], v41 offset:57600\n\t"
    "v_exp_f32 v69, v69\n\t"
    "v_mfma_f32_16x16x32_bf16 a[172:175], a[160:163], a[220:223], a[172:175]\n\t"
    "v_fma_f32 v70, v70, s34, v24\n\t"
    "v_fma_f32 v71, v71, s34, v24\n\t"
    "v_mfma_f32_16x16x32_bf16 v[60:63], a[96:99], a[64:67], 0\n\t"
    "v_exp_f32 v70, v70\n\t"
    "v_mfma_f32_16x16x32_bf16 v[60:63], a[100:103], a[68:71], v[60:63]\n\t"
    "s_add_u32 m0, s40, 36864\n\t"
    "v_exp_f32 v71, v71\n\t"
    "v_mfma_f32_16x16x32_bf16 v[60:63], a[104:107], a[72:75], v[60:63]\n\t"
    "s_add_u32 s47, s41, 200704\n\t"
    "ds_read_b128 a[152:155], v42 offset:57600\n\t"
    "v_cvt_pk_bf16_f32 v34, v68, v69\n\t"
    "v_mfma_f32_16x16x32_bf16 v[60:63], a[108:111], a[76:79], v[60:63]\n\t"
    "v_cvt_pk_bf16_f32 v35, v70, v71\n\t"
    "v_fma_f32 v72, v72, s34, v24\n\t"
    "v_mfma_f32_16x16x32_bf16 v[60:63], a[112:115], a[80:83], v[60:63]\n\t"
    "s_waitcnt vmcnt(12)\n\t"
    "s_barrier\n\t"
    "buffer_load_dwordx4 v12, s[20:23], s47 offen offset:0 nt lds\n\t"
    "v_mfma_f32_16x16x32_bf16 v[60:63], a[116:119], a[84:87], v[60:63]\n\t"
    "ds_read_b128 a[156:159], v43 offset:57600\n\t"
    "v_fma_f32 v73, v73, s34, v24\n\t"
    "v_mfma_f32_16x16x32_bf16 v[60:63], a[120:123], a[88:91], v[60:63]\n\t"
    "v_exp_f32 v72, v72\n\t"
    "v_mfma_f32_16x16x32_bf16 v[60:63], a[124:127], a[92:95], v[60:63]\n\t"
    "s_waitcnt lgkmcnt(7)\n\t"
    "v_exp_f32 v73, v73\n\t"
    "v_fma_f32 v74, v74, s34, v24\n\t"
    "v_mfma_f32_16x16x32_bf16 a[172:175], a[160:163], v[32:35], a[172:175]\n\t"
    "v_exp_f32 v74, v74\n\t"
    "v_mfma_f32_16x16x32_bf16 v[64:67], a[128:131], a[0:3], 0\n\t"
    "s_waitcnt lgkmcnt(6)\n\t"
    "v_add_u32 v4, 0x18000, v4\n\t"
    "v_add_u32 v5, 0x18000, v5\n\t"
    "v_add_u32 v6, 0x18000, v6\n\t"
    "v_add_u32 v7, 0x18000, v7\n\t"
    "v_mfma_f32_16x16x32_bf16 v[64:67], a[132:135], a[4:7], v[64:67]\n\t"
    "s_waitcnt lgkmcnt(5)\n\t"
    "ds_read_b128 a[96:99], v4 offset:32768\n\t"
    "v_fma_f32 v75, v75, s34, v24\n\t"
    "v_mfma_f32_16x16x32_bf16 v[64:67], a[136:139], a[8:11], v[64:67]\n\t"
    "s_waitcnt lgkmcnt(5)\n\t"
    "buffer_load_dwordx4 v13, s[20:23], s47 offen offset:1024 nt lds\n\t"
    "v_mfma_f32_16x16x32_bf16 v[64:67], a[140:143], a[12:15], v[64:67]\n\t"
    "s_waitcnt lgkmcnt(4)\n\t"
    "ds_read_b128 a[100:103], v5 offset:32768\n\t"
    "v_cvt_pk_bf16_f32 v72, v72, v73\n\t"
    "v_mfma_f32_16x16x32_bf16 v[64:67], a[144:147], a[16:19], v[64:67]\n\t"
    "s_waitcnt lgkmcnt(4)\n\t"
    "v_exp_f32 v75, v75\n\t"
    "v_mfma_f32_16x16x32_bf16 v[64:67], a[148:151], a[20:23], v[64:67]\n\t"
    "s_waitcnt lgkmcnt(3)\n\t"
    "v_cvt_pk_bf16_f32 v73, v74, v75\n\t"
    "v_fma_f32 v76, v76, s34, v24\n\t"
    "v_mfma_f32_16x16x32_bf16 v[64:67], a[152:155], a[24:27], v[64:67]\n\t"
    "s_waitcnt lgkmcnt(2)\n\t"
    "ds_read_b128 a[104:107], v6 offset:32768\n\t"
    "v_fma_f32 v77, v77, s34, v24\n\t"
    "v_mfma_f32_16x16x32_bf16 v[64:67], a[156:159], a[28:31], v[64:67]\n\t"
    "buffer_load_dwordx4 v14, s[20:23], s47 offen offset:2048 nt lds\n\t"
    "v_mfma_f32_16x16x32_bf16 v[68:71], a[128:131], a[32:35], 0\n\t"
    "v_exp_f32 v76, v76\n\t"
    "v_mfma_f32_16x16x32_bf16 v[68:71], a[132:135], a[36:39], v[68:71]\n\t"
    "ds_read_b128 a[108:111], v7 offset:32768\n\t"
    "v_fma_f32 v78, v78, s34, v24\n\t"
    "v_mfma_f32_16x16x32_bf16 v[68:71], a[136:139], a[40:43], v[68:71]\n\t"
    "v_exp_f32 v77, v77\n\t"
    "v_mfma_f32_16x16x32_bf16 v[68:71], a[140:143], a[44:47], v[68:71]\n\t"
    "v_exp_f32 v78, v78\n\t"
    "v_mfma_f32_16x16x32_bf16 v[68:71], a[144:147], a[48:51], v[68:71]\n\t"
    "ds_read_b128 a[112:115], v4 offset:33024\n\t"
    "v_fma_f32 v79, v79, s34, v24\n\t"
    "v_mfma_f32_16x16x32_bf16 v[68:71], a[148:151], a[52:55], v[68:71]\n\t"
    "buffer_load_dwordx4 v15, s[20:23], s47 offen offset:3072 nt lds\n\t"
    "v_mfma_f32_16x16x32_bf16 v[68:71], a[152:155], a[56:59], v[68:71]\n\t"
    "v_exp_f32 v79, v79\n\t"
    "v_mfma_f32_16x16x32_bf16 v[68:71], a[156:159], a[60:63], v[68:71]\n\t"
    "ds_read_b128 a[116:119], v5 offset:33024\n\t"
    "v_cvt_pk_bf16_f32 v74, v76, v77\n\t"
    "v_cvt_pk_bf16_f32 v75, v78, v79\n\t"
    "v_mfma_f32_16x16x32_bf16 v[92:95], a[128:131], a[64:67], 0\n\t"
    "v_fma_f32 v80, v80, s34, v22\n\t"
    "v_fma_f32 v81, v81, s34, v22\n\t"
    "v_mfma_f32_16x16x32_bf16 v[92:95], a[132:135], a[68:71], v[92:95]\n\t"
    "s_add_u32 m0, s40, 65536\n\t"
    "v_exp_f32 v80, v80\n\t"
    "v_mfma_f32_16x16x32_bf16 v[92:95], a[136:139], a[72:75], v[92:95]\n\t"
    "s_add_u32 s46, s42, 0\n\t"
    "ds_read_b128 a[120:123], v6 offset:33024\n\t"
    "v_fma_f32 v82, v82, s34, v22\n\t"
    "v_mfma_f32_16x16x32_bf16 v[92:95], a[140:143], a[76:79], v[92:95]\n\t"
    "v_exp_f32 v81, v81\n\t"
    "v_mfma_f32_16x16x32_bf16 v[92:95], a[144:147], a[80:83], v[92:95]\n\t"
    "buffer_load_dwordx4 v16, s[24:27], s46 offen offset:0 lds\n\t"
    "v_mfma_f32_16x16x32_bf16 v[92:95], a[148:151], a[84:87], v[92:95]\n\t"
    "ds_read_b128 a[124:127], v7 offset:33024\n\t"
    "v_fma_f32 v83, v83, s34, v22\n\t"
    "v_mfma_f32_16x16x32_bf16 v[92:95], a[152:155], a[88:91], v[92:95]\n\t"
    "v_exp_f32 v82, v82\n\t"
    "v_mfma_f32_16x16x32_bf16 v[92:95], a[156:159], a[92:95], v[92:95]\n\t"
    "s_waitcnt lgkmcnt(7)\n\t"
    "v_exp_f32 v83, v83\n\t"
    "v_cvt_pk_bf16_f32 v80, v80, v81\n\t"
    "v_mfma_f32_16x16x32_bf16 a[172:175], a[160:163], v[72:75], a[172:175]\n\t"
    "v_cvt_pk_bf16_f32 v81, v82, v83\n\t"
    "v_fma_f32 v36, v36, s34, v23\n\t"
    "v_mfma_f32_16x16x32_bf16 v[76:79], a[96:99], a[0:3], 0\n\t"
    "s_waitcnt lgkmcnt(6)\n\t"
    "ds_read_b128 a[128:131], v4 offset:40960\n\t"
    "v_fma_f32 v37, v37, s34, v23\n\t"
    "v_mfma_f32_16x16x32_bf16 v[76:79], a[100:103], a[4:7], v[76:79]\n\t"
    "s_waitcnt lgkmcnt(6)\n\t"
    "v_exp_f32 v36, v36\n\t"
    "v_mfma_f32_16x16x32_bf16 v[76:79], a[104:107], a[8:11], v[76:79]\n\t"
    "s_waitcnt lgkmcnt(5)\n\t"
    "v_exp_f32 v37, v37\n\t"
    "v_mfma_f32_16x16x32_bf16 v[76:79], a[108:111], a[12:15], v[76:79]\n\t"
    "s_waitcnt lgkmcnt(4)\n\t"
    "ds_read_b128 a[132:135], v5 offset:40960\n\t"
    "v_fma_f32 v38, v38, s34, v23\n\t"
    "v_mfma_f32_16x16x32_bf16 v[76:79], a[112:115], a[16:19], v[76:79]\n\t"
    "s_waitcnt lgkmcnt(4)\n\t"
    "v_exp_f32 v38, v38\n\t"
    "v_mfma_f32_16x16x32_bf16 v[76:79], a[116:119], a[20:23], v[76:79]\n\t"
    "s_waitcnt lgkmcnt(3)\n\t"
    "v_fma_f32 v39, v39, s34, v23\n\t"
    "v_cvt_pk_bf16_f32 v36, v36, v37\n\t"
    "v_mfma_f32_16x16x32_bf16 v[76:79], a[120:123], a[24:27], v[76:79]\n\t"
    "s_waitcnt lgkmcnt(2)\n\t"
    "ds_read_b128 a[136:139], v6 offset:40960\n\t"
    "v_fma_f32 v84, v84, s34, v24\n\t"
    "v_mfma_f32_16x16x32_bf16 v[76:79], a[124:127], a[28:31], v[76:79]\n\t"
    "v_exp_f32 v39, v39\n\t"
    "v_fma_f32 v85, v85, s34, v24\n\t"
    "v_mfma_f32_16x16x32_bf16 v[96:99], a[96:99], a[32:35], 0\n\t"
    "v_exp_f32 v84, v84\n\t"
    "v_mfma_f32_16x16x32_bf16 v[96:99], a[100:103], a[36:39], v[96:99]\n\t"
    "ds_read_b128 a[140:143], v7 offset:40960\n\t"
    "v_cvt_pk_bf16_f32 v37, v38, v39\n\t"
    "v_mfma_f32_16x16x32_bf16 v[96:99], a[104:107], a[40:43], v[96:99]\n\t"
    "v_exp_f32 v85, v85\n\t"
    "v_mfma_f32_16x16x32_bf16 v[96:99], a[108:111], a[44:47], v[96:99]\n\t"
    "v_fma_f32 v86, v86, s34, v24\n\t"
    "v_fma_f32 v87, v87, s34, v24\n\t"
    "v_mfma_f32_16x16x32_bf16 v[96:99], a[112:115], a[48:51], v[96:99]\n\t"
    "ds_read_b128 a[144:147], v4 offset:41216\n\t"
    "v_cvt_pk_bf16_f32 v84, v84, v85\n\t"
    "v_mfma_f32_16x16x32_bf16 v[96:99], a[116:119], a[52:55], v[96:99]\n\t"
    "v_exp_f32 v86, v86\n\t"
    "v_mfma_f32_16x16x32_bf16 v[96:99], a[120:123], a[56:59], v[96:99]\n\t"
    "buffer_load_dwordx4 v17, s[24:27], s46 offen offset:1024 lds\n\t"
    "v_mfma_f32_16x16x32_bf16 v[96:99], a[124:127], a[60:63], v[96:99]\n\t"
    "ds_read_b128 a[148:151], v5 offset:41216\n\t"
    "v_exp_f32 v87, v87\n\t"
    "v_mfma_f32_16x16x32_bf16 v[100:103], a[96:99], a[64:67], 0\n\t"
    "v_cvt_pk_bf16_f32 v85, v86, v87\n\t"
    "v_fma_f32 v44, v44, s34, v22\n\t"
    "v_mfma_f32_16x16x32_bf16 v[100:103], a[100:103], a[68:71], v[100:103]\n\t"
    "v_exp_f32 v44, v44\n\t"
    "v_mfma_f32_16x16x32_bf16 v[100:103], a[104:107], a[72:75], v[100:103]\n\t"
    "ds_read_b128 a[152:155], v6 offset:41216\n\t"
    "v_fma_f32 v45, v45, s34, v22\n\t"
    "v_mfma_f32_16x16x32_bf16 v[100:103], a[108:111], a[76:79], v[100:103]\n\t"
    "v_exp_f32 v45, v45\n\t"
    "v_mfma_f32_16x16x32_bf16 v[100:103], a[112:115], a[80:83], v[100:103]\n\t"
    "v_fma_f32 v46, v46, s34, v22\n\t"
    "v_fma_f32 v47, v47, s34, v22\n\t"
    "v_mfma_f32_16x16x32_bf16 v[100:103], a[116:119], a[84:87], v[100:103]\n\t"
    "ds_read_b128 a[156:159], v7 offset:41216\n\t"
    "v_cvt_pk_bf16_f32 v82, v44, v45\n\t"
    "v_mfma_f32_16x16x32_bf16 v[100:103], a[120:123], a[88:91], v[100:103]\n\t"
    "v_exp_f32 v46, v46\n\t"
    "v_mfma_f32_16x16x32_bf16 v[100:103], a[124:127], a[92:95], v[100:103]\n\t"
    "s_waitcnt lgkmcnt(7)\n\t"
    "v_exp_f32 v47, v47\n\t"
    "v_fma_f32 v88, v88, s34, v23\n\t"
    "v_mfma_f32_16x16x32_bf16 v[104:107], a[128:131], a[0:3], 0\n\t"
    "s_waitcnt lgkmcnt(6)\n\t"
    "ds_read_b128 a[96:99], v4 offset:49152\n\t"
    "v_cvt_pk_bf16_f32 v83, v46, v47\n\t"
    "v_mfma_f32_16x16x32_bf16 v[104:107], a[132:135], a[4:7], v[104:107]\n\t"
    "s_waitcnt lgkmcnt(6)\n\t"
    "v_exp_f32 v88, v88\n\t"
    "v_mfma_f32_16x16x32_bf16 v[104:107], a[136:139], a[8:11], v[104:107]\n\t"
    "s_waitcnt lgkmcnt(5)\n\t"
    "v_fma_f32 v89, v89, s34, v23\n\t"
    "v_fma_f32 v90, v90, s34, v23\n\t"
    "v_mfma_f32_16x16x32_bf16 v[104:107], a[140:143], a[12:15], v[104:107]\n\t"
    "s_waitcnt lgkmcnt(4)\n\t"
    "ds_read_b128 a[100:103], v5 offset:49152\n\t"
    "v_fma_f32 v91, v91, s34, v23\n\t"
    "v_mfma_f32_16x16x32_bf16 v[104:107], a[144:147], a[16:19], v[104:107]\n\t"
    "s_waitcnt lgkmcnt(4)\n\t"
    "v_exp_f32 v89, v89\n\t"
    "v_mfma_f32_16x16x32_bf16 v[104:107], a[148:151], a[20:23], v[104:107]\n\t"
    "s_waitcnt lgkmcnt(3)\n\t"
    "v_exp_f32 v90, v90\n\t"
    "v_mfma_f32_16x16x32_bf16 v[104:107], a[152:155], a[24:27], v[104:107]\n\t"
    "s_waitcnt lgkmcnt(2)\n\t"
    "ds_read_b128 a[104:107], v6 offset:49152\n\t"
    "v_cvt_pk_bf16_f32 v38, v88, v89\n\t"
    "v_mfma_f32_16x16x32_bf16 v[104:107], a[156:159], a[28:31], v[104:107]\n\t"
    "v_exp_f32 v91, v91\n\t"
    "v_fma_f32 v48, v48, s34, v24\n\t"
    "v_mfma_f32_16x16x32_bf16 a[164:167], a[160:163], v[80:83], a[164:167]\n\t"
    "v_exp_f32 v48, v48\n\t"
    "v_mfma_f32_16x16x32_bf16 v[44:47], a[128:131], a[32:35], 0\n\t"
    "v_cvt_pk_bf16_f32 v39, v90, v91\n\t"
    "v_fma_f32 v49, v49, s34, v24\n\t"
    "v_mfma_f32_16x16x32_bf16 v[44:47], a[132:135], a[36:39], v[44:47]\n\t"
    "ds_read_b128 a[108:111], v7 offset:49152\n\t"
    "v_fma_f32 v50, v50, s34, v24\n\t"
    "v_mfma_f32_16x16x32_bf16 v[44:47], a[136:139], a[40:43], v[44:47]\n\t"
    "buffer_load_dwordx4 v18, s[24:27], s46 offen offset:2048 lds\n\t"
    "v_mfma_f32_16x16x32_bf16 v[44:47], a[140:143], a[44:47], v[44:47]\n\t"
    "v_exp_f32 v49, v49\n\t"
    "v_mfma_f32_16x16x32_bf16 v[44:47], a[144:147], a[48:51], v[44:47]\n\t"
    "ds_read_b128 a[112:115], v4 offset:49408\n\t"
    "v_fma_f32 v51, v51, s34, v24\n\t"
    "v_mfma_f32_16x16x32_bf16 v[44:47], a[148:151], a[52:55], v[44:47]\n\t"
    "v_exp_f32 v50, v50\n\t"
    "v_mfma_f32_16x16x32_bf16 v[44:47], a[152:155], a[56:59], v[44:47]\n\t"
    "v_exp_f32 v51, v51\n\t"
    "v_mfma_f32_16x16x32_bf16 v[44:47], a[156:159], a[60:63], v[44:47]\n\t"
    "ds_read_b128 a[116:119], v5 offset:49408\n\t"
    "v_cvt_pk_bf16_f32 v86, v48, v49\n\t"
    "v_cvt_pk_bf16_f32 v87, v50, v51\n\t"
    "v_mfma_f32_16x16x32_bf16 a[168:171], a[160:163], v[36:39], a[168:171]\n\t"
    "v_fma_f32 v52, v52, s34, v22\n\t"
    "v_fma_f32 v53, v53, s34, v22\n\t"
    "v_mfma_f32_16x16x32_bf16 a[172:175], a[160:163], v[84:87], a[172:175]\n\t"
    "v_exp_f32 v52, v52\n\t"
    "v_mfma_f32_16x16x32_bf16 v[88:91], a[128:131], a[64:67], 0\n\t"
    "v_exp_f32 v53, v53\n\t"
    "v_mfma_f32_16x16x32_bf16 v[88:91], a[132:135], a[68:71], v[88:91]\n\t"
    "v_fma_f32 v54, v54, s34, v22\n\t"
    "v_fma_f32 v55, v55, s34, v22\n\t"
    "v_mfma_f32_16x16x32_bf16 v[88:91], a[136:139], a[72:75], v[88:91]\n\t"
    "ds_read_b128 a[120:123], v6 offset:49408\n\t"
    "v_cvt_pk_bf16_f32 v52, v52, v53\n\t"
    "v_mfma_f32_16x16x32_bf16 v[88:91], a[140:143], a[76:79], v[88:91]\n\t"
    "v_exp_f32 v54, v54\n\t"
    "v_mfma_f32_16x16x32_bf16 v[88:91], a[144:147], a[80:83], v[88:91]\n\t"
    "v_exp_f32 v55, v55\n\t"
    "v_mfma_f32_16x16x32_bf16 v[88:91], a[148:151], a[84:87], v[88:91]\n\t"
    "ds_read_b128 a[124:127], v7 offset:49408\n\t"
    "v_cvt_pk_bf16_f32 v53, v54, v55\n\t"
    "v_mfma_f32_16x16x32_bf16 v[88:91], a[152:155], a[88:91], v[88:91]\n\t"
    "v_fma_f32 v56, v56, s34, v23\n\t"
    "v_fma_f32 v57, v57, s34, v23\n\t"
    "v_mfma_f32_16x16x32_bf16 v[88:91], a[156:159], a[92:95], v[88:91]\n\t"
    "s_waitcnt lgkmcnt(7)\n\t"
    "v_exp_f32 v56, v56\n\t"
    "v_fma_f32 v58, v58, s34, v23\n\t"
    "v_mfma_f32_16x16x32_bf16 v[48:51], a[96:99], a[0:3], 0\n\t"
    "s_waitcnt lgkmcnt(6)\n\t"
    "ds_read_b128 a[128:131], v4 offset:57344\n\t"
    "v_fma_f32 v59, v59, s34, v23\n\t"
    "v_mfma_f32_16x16x32_bf16 v[48:51], a[100:103], a[4:7], v[48:51]\n\t"
    "s_waitcnt lgkmcnt(6)\n\t"
    "v_exp_f32 v57, v57\n\t"
    "v_mfma_f32_16x16x32_bf16 v[48:51], a[104:107], a[8:11], v[48:51]\n\t"
    "s_waitcnt lgkmcnt(5)\n\t"
    "v_exp_f32 v58, v58\n\t"
    "v_mfma_f32_16x16x32_bf16 v[48:51], a[108:111], a[12:15], v[48:51]\n\t"
    "s_waitcnt lgkmcnt(4)\n\t"
    "ds_read_b128 a[132:135], v5 offset:57344\n\t"
    "v_cvt_pk_bf16_f32 v56, v56, v57\n\t"
    "v_mfma_f32_16x16x32_bf16 v[48:51], a[112:115], a[16:19], v[48:51]\n\t"
    "s_waitcnt lgkmcnt(4)\n\t"
    "buffer_load_dwordx4 v19, s[24:27], s46 offen offset:3072 lds\n\t"
    "v_mfma_f32_16x16x32_bf16 v[48:51], a[116:119], a[20:23], v[48:51]\n\t"
    "s_waitcnt lgkmcnt(3)\n\t"
    "v_exp_f32 v59, v59\n\t"
    "v_mfma_f32_16x16x32_bf16 v[48:51], a[120:123], a[24:27], v[48:51]\n\t"
    "s_waitcnt lgkmcnt(2)\n\t"
    "ds_read_b128 a[136:139], v6 offset:57344\n\t"
    "v_cvt_pk_bf16_f32 v57, v58, v59\n\t"
    "v_mfma_f32_16x16x32_bf16 v[48:51], a[124:127], a[28:31], v[48:51]\n\t"
    "v_fma_f32 v60, v60, s34, v24\n\t"
    "v_fma_f32 v61, v61, s34, v24\n\t"
    "v_fma_f32 v62, v62, s34, v24\n\t"
    "v_mfma_f32_16x16x32_bf16 v[108:111], a[96:99], a[32:35], 0\n\t"
    "v_exp_f32 v60, v60\n\t"
    "v_mfma_f32_16x16x32_bf16 v[108:111], a[100:103], a[36:39], v[108:111]\n\t"
    "ds_read_b128 a[140:143], v7 offset:57344\n\t"
    "v_fma_f32 v63, v63, s34, v24\n\t"
    "v_mfma_f32_16x16x32_bf16 v[108:111], a[104:107], a[40:43], v[108:111]\n\t"
    "v_exp_f32 v61, v61\n\t"
    "v_mfma_f32_16x16x32_bf16 v[108:111], a[108:111], a[44:47], v[108:111]\n\t"
    "v_exp_f32 v62, v62\n\t"
    "v_mfma_f32_16x16x32_bf16 v[108:111], a[112:115], a[48:51], v[108:111]\n\t"
    "ds_read_b128 a[144:147], v4 offset:57600\n\t"
    "v_cvt_pk_bf16_f32 v60, v60, v61\n\t"
    "v_mfma_f32_16x16x32_bf16 v[108:111], a[116:119], a[52:55], v[108:111]\n\t"
    "v_exp_f32 v63, v63\n\t"
    "v_mfma_f32_16x16x32_bf16 v[108:111], a[120:123], a[56:59], v[108:111]\n\t"
    "v_cvt_pk_bf16_f32 v61, v62, v63\n\t"
    "v_fma_f32 v64, v64, s34, v22\n\t"
    "v_mfma_f32_16x16x32_bf16 v[108:111], a[124:127], a[60:63], v[108:111]\n\t"
    "ds_read_b128 a[148:151], v5 offset:57600\n\t"
    "v_exp_f32 v64, v64\n\t"
    "v_mfma_f32_16x16x32_bf16 v[112:115], a[96:99], a[64:67], 0\n\t"
    "v_fma_f32 v65, v65, s34, v22\n\t"
    "v_fma_f32 v66, v66, s34, v22\n\t"
    "v_mfma_f32_16x16x32_bf16 v[112:115], a[100:103], a[68:71], v[112:115]\n\t"
    "v_exp_f32 v65, v65\n\t"
    "v_mfma_f32_16x16x32_bf16 v[112:115], a[104:107], a[72:75], v[112:115]\n\t"
    "ds_read_b128 a[152:155], v6 offset:57600\n\t"
    "v_fma_f32 v67, v67, s34, v22\n\t"
    "v_mfma_f32_16x16x32_bf16 v[112:115], a[108:111], a[76:79], v[112:115]\n\t"
    "v_exp_f32 v66, v66\n\t"
    "v_mfma_f32_16x16x32_bf16 v[112:115], a[112:115], a[80:83], v[112:115]\n\t"
    "s_waitcnt vmcnt(12)\n\t"
    "s_barrier\n\t"
    "v_cvt_pk_bf16_f32 v54, v64, v65\n\t"
    "v_mfma_f32_16x16x32_bf16 v[112:115], a[116:119], a[84:87], v[112:115]\n\t"
    "s_add_u32 m0, s40, 98304\n\t"
    "ds_read_b128 a[156:159], v7 offset:57600\n\t"
    "v_fma_f32 v68, v68, s34, v23\n\t"
    "v_mfma_f32_16x16x32_bf16 v[112:115], a[120:123], a[88:91], v[112:115]\n\t"
    "s_add_u32 s47, s41, 229376\n\t"
    "v_exp_f32 v67, v67\n\t"
    "v_mfma_f32_16x16x32_bf16 v[112:115], a[124:127], a[92:95], v[112:115]\n\t"
    "s_waitcnt lgkmcnt(7)\n\t"
    "v_exp_f32 v68, v68\n\t"
    "v_cvt_pk_bf16_f32 v55, v66, v67\n\t"
    "v_mfma_f32_16x16x32_bf16 v[116:119], a[128:131], a[0:3], 0\n\t"
    "s_waitcnt lgkmcnt(6)\n\t"
    "v_add_u32 v40, 0xffff0000, v40\n\t"
    "v_add_u32 v41, 0xffff0000, v41\n\t"
    "v_add_u32 v42, 0xffff0000, v42\n\t"
    "v_add_u32 v43, 0xffff0000, v43\n\t"
    "v_mfma_f32_16x16x32_bf16 v[116:119], a[132:135], a[4:7], v[116:119]\n\t"
    "s_waitcnt lgkmcnt(5)\n\t"
    "ds_read_b128 a[96:99], v40 offset:0\n\t"
    "v_fma_f32 v69, v69, s34, v23\n\t"
    "v_mfma_f32_16x16x32_bf16 v[116:119], a[136:139], a[8:11], v[116:119]\n\t"
    "s_waitcnt lgkmcnt(5)\n\t"
    "buffer_load_dwordx4 v12, s[20:23], s47 offen offset:0 nt lds\n\t"
    "v_mfma_f32_16x16x32_bf16 v[116:119], a[140:143], a[12:15], v[116:119]\n\t"
    "s_waitcnt lgkmcnt(4)\n\t"
    "ds_read_b128 a[100:103], v41 offset:0\n\t"
    "v_fma_f32 v70, v70, s34, v23\n\t"
    "v_mfma_f32_16x16x32_bf16 v[116:119], a[144:147], a[16:19], v[116:119]\n\t"
    "s_waitcnt lgkmcnt(4)\n\t"
    "v_exp_f32 v69, v69\n\t"
    "v_mfma_f32_16x16x32_bf16 v[116:119], a[148:151], a[20:23], v[116:119]\n\t"
    "s_waitcnt lgkmcnt(3)\n\t"
    "buffer_load_dwordx4 v13, s[20:23], s47 offen offset:1024 nt lds\n\t"
    "v_mfma_f32_16x16x32_bf16 v[116:119], a[152:155], a[24:27], v[116:119]\n\t"
    "s_waitcnt lgkmcnt(2)\n\t"
    "ds_read_b128 a[104:107], v42 offset:0\n\t"
    "v_fma_f32 v71, v71, s34, v23\n\t"
    "v_mfma_f32_16x16x32_bf16 v[116:119], a[156:159], a[28:31], v[116:119]\n\t"
    "v_exp_f32 v70, v70\n\t"
    "v_cvt_pk_bf16_f32 v58, v68, v69\n\t"
    "v_mfma_f32_16x16x32_bf16 a[164:167], a[160:163], v[52:55], a[164:167]\n\t"
    "v_exp_f32 v71, v71\n\t"
    "v_mfma_f32_16x16x32_bf16 v[64:67], a[128:131], a[32:35], 0\n\t"
    "v_cvt_pk_bf16_f32 v59, v70, v71\n\t"
    "v_fma_f32 v92, v92, s34, v24\n\t"
    "v_mfma_f32_16x16x32_bf16 v[64:67], a[132:135], a[36:39], v[64:67]\n\t"
    "ds_read_b128 a[108:111], v43 offset:0\n\t"
    "v_fma_f32 v93, v93, s34, v24\n\t"
    "v_mfma_f32_16x16x32_bf16 v[64:67], a[136:139], a[40:43], v[64:67]\n\t"
    "v_exp_f32 v92, v92\n\t"
    "v_mfma_f32_16x16x32_bf16 v[64:67], a[140:143], a[44:47], v[64:67]\n\t"
    "buffer_load_dwordx4 v14, s[20:23], s47 offen offset:2048 nt lds\n\t"
    "v_mfma_f32_16x16x32_bf16 v[64:67], a[144:147], a[48:51], v[64:67]\n\t"
    "ds_read_b128 a[112:115], v40 offset:256\n\t"
    "v_fma_f32 v94, v94, s34, v24\n\t"
    "v_mfma_f32_16x16x32_bf16 v[64:67], a[148:151], a[52:55], v[64:67]\n\t"
    "v_exp_f32 v93, v93\n\t"
    "v_mfma_f32_16x16x32_bf16 v[64:67], a[152:155], a[56:59], v[64:67]\n\t"
    "v_exp_f32 v94, v94\n\t"
    "v_mfma_f32_16x16x32_bf16 v[64:67], a[156:159], a[60:63], v[64:67]\n\t"
    "ds_read_b128 a[116:119], v41 offset:256\n\t"
    "v_fma_f32 v95, v95, s34, v24\n\t"
    "v_cvt_pk_bf16_f32 v62, v92, v93\n\t"
    "v_mfma_f32_16x16x32_bf16 a[168:171], a[160:163], v[56:59], a[168:171]\n\t"
    "v_exp_f32 v95, v95\n\t"
    "v_mfma_f32_16x16x32_bf16 v[68:71], a[128:131], a[64:67], 0\n\t"
    "buffer_load_dwordx4 v15, s[20:23], s47 offen offset:3072 nt lds\n\t"
    "v_mfma_f32_16x16x32_bf16 v[68:71], a[132:135], a[68:71], v[68:71]\n\t"
    "v_cvt_pk_bf16_f32 v63, v94, v95\n\t"
    "v_fma_f32 v76, v76, s34, v22\n\t"
    "v_mfma_f32_16x16x32_bf16 v[68:71], a[136:139], a[72:75], v[68:71]\n\t"
    "ds_read_b128 a[120:123], v42 offset:256\n\t"
    "v_fma_f32 v77, v77, s34, v22\n\t"
    "v_mfma_f32_16x16x32_bf16 v[68:71], a[140:143], a[76:79], v[68:71]\n\t"
    "s_add_u32 m0, s40, 102400\n\t"
    "v_exp_f32 v76, v76\n\t"
    "v_mfma_f32_16x16x32_bf16 v[68:71], a[144:147], a[80:83], v[68:71]\n\t"
    "s_add_u32 s46, s41, 233472\n\t"
    "v_exp_f32 v77, v77\n\t"
    "v_mfma_f32_16x16x32_bf16 v[68:71], a[148:151], a[84:87], v[68:71]\n\t"
    "ds_read_b128 a[124:127], v43 offset:256\n\t"
    "v_fma_f32 v78, v78, s34, v22\n\t"
    "v_mfma_f32_16x16x32_bf16 v[68:71], a[152:155], a[88:91], v[68:71]\n\t"
    "buffer_load_dwordx4 v12, s[20:23], s46 offen offset:0 nt lds\n\t"
    "v_mfma_f32_16x16x32_bf16 v[68:71], a[156:159], a[92:95], v[68:71]\n\t"
    "s_waitcnt lgkmcnt(7)\n\t"
    "v_exp_f32 v78, v78\n\t"
    "v_fma_f32 v79, v79, s34, v22\n\t"
    "v_mfma_f32_16x16x32_bf16 a[172:175], a[160:163], v[60:63], a[172:175]\n\t"
    "v_exp_f32 v79, v79\n\t"
    "v_mfma_f32_16x16x32_bf16 v[92:95], a[96:99], a[0:3], 0\n\t"
    "s_waitcnt lgkmcnt(6)\n\t"
    "ds_read_b128 a[128:131], v40 offset:8192\n\t"
    "v_cvt_pk_bf16_f32 v76, v76, v77\n\t"
    "v_mfma_f32_16x16x32_bf16 v[92:95], a[100:103], a[4:7], v[92:95]\n\t"
    "s_waitcnt lgkmcnt(6)\n\t"
    "v_cvt_pk_bf16_f32 v77, v78, v79\n\t"
    "v_fma_f32 v96, v96, s34, v23\n\t"
    "v_mfma_f32_16x16x32_bf16 v[92:95], a[104:107], a[8:11], v[92:95]\n\t"
    "s_waitcnt lgkmcnt(5)\n\t"
    "v_exp_f32 v96, v96\n\t"
    "v_mfma_f32_16x16x32_bf16 v[92:95], a[108:111], a[12:15], v[92:95]\n\t"
    "s_waitcnt lgkmcnt(4)\n\t"
    "ds_read_b128 a[132:135], v41 offset:8192\n\t"
    "v_fma_f32 v97, v97, s34, v23\n\t"
    "v_mfma_f32_16x16x32_bf16 v[92:95], a[112:115], a[16:19], v[92:95]\n\t"
    "s_waitcnt lgkmcnt(4)\n\t"
    "buffer_load_dwordx4 v13, s[20:23], s46 offen offset:1024 nt lds\n\t"
    "v_mfma_f32_16x16x32_bf16 v[92:95], a[116:119], a[20:23], v[92:95]\n\t"
    "s_waitcnt lgkmcnt(3)\n\t"
    "v_exp_f32 v97, v97\n\t"
    "v_mfma_f32_16x16x32_bf16 v[92:95], a[120:123], a[24:27], v[92:95]\n\t"
    "s_waitcnt lgkmcnt(2)\n\t"
    "ds_read_b128 a[136:139], v42 offset:8192\n\t"
    "v_fma_f32 v98, v98, s34, v23\n\t"
    "v_mfma_f32_16x16x32_bf16 v[92:95], a[124:127], a[28:31], v[92:95]\n\t"
    "v_exp_f32 v98, v98\n\t"
    "v_fma_f32 v99, v99, s34, v23\n\t"
    "v_mfma_f32_16x16x32_bf16 v[120:123], a[96:99], a[32:35], 0\n\t"
    "v_exp_f32 v99, v99\n\t"
    "v_mfma_f32_16x16x32_bf16 v[120:123], a[100:103], a[36:39], v[120:123]\n\t"
    "ds_read_b128 a[140:143], v43 offset:8192\n\t"
    "v_cvt_pk_bf16_f32 v96, v96, v97\n\t"
    "v_mfma_f32_16x16x32_bf16 v[120:123], a[104:107], a[40:43], v[120:123]\n\t"
    "buffer_load_dwordx4 v14, s[20:23], s46 offen offset:2048 nt lds\n\t"
    "v_mfma_f32_16x16x32_bf16 v[120:123], a[108:111], a[44:47], v[120:123]\n\t"
    "v_cvt_pk_bf16_f32 v97, v98, v99\n\t"
    "v_fma_f32 v100, v100, s34, v24\n\t"
    "v_mfma_f32_16x16x32_bf16 v[120:123], a[112:115], a[48:51], v[120:123]\n\t"
    "ds_read_b128 a[144:147], v40 offset:8448\n\t"
    "v_fma_f32 v101, v101, s34, v24\n\t"
    "v_mfma_f32_16x16x32_bf16 v[120:123], a[116:119], a[52:55], v[120:123]\n\t"
    "v_exp_f32 v100, v100\n\t"
    "v_mfma_f32_16x16x32_bf16 v[120:123], a[120:123], a[56:59], v[120:123]\n\t"
    "buffer_load_dwordx4 v15, s[20:23], s46 offen offset:3072 nt lds\n\t"
    "v_mfma_f32_16x16x32_bf16 v[120:123], a[124:127], a[60:63], v[120:123]\n\t"
    "ds_read_b128 a[148:151], v41 offset:8448\n\t"
    "v_exp_f32 v101, v101\n\t"
    "v_mfma_f32_16x16x32_bf16 v[124:127], a[96:99], a[64:67], 0\n\t"
    "v_fma_f32 v102, v102, s34, v24\n\t"
    "v_fma_f32 v103, v103, s34, v24\n\t"
    "v_mfma_f32_16x16x32_bf16 v[124:127], a[100:103], a[68:71], v[124:127]\n\t"
    "v_exp_f32 v102, v102\n\t"
    "v_mfma_f32_16x16x32_bf16 v[124:127], a[104:107], a[72:75], v[124:127]\n\t"
    "ds_read_b128 a[152:155], v42 offset:8448\n\t"
    "v_cvt_pk_bf16_f32 v100, v100, v101\n\t"
    "v_mfma_f32_16x16x32_bf16 v[124:127], a[108:111], a[76:79], v[124:127]\n\t"
    "s_add_u32 m0, s40, 69632\n\t"
    "v_exp_f32 v103, v103\n\t"
    "v_mfma_f32_16x16x32_bf16 v[124:127], a[112:115], a[80:83], v[124:127]\n\t"
    "s_add_u32 s47, s42, 16384\n\t"
    "v_cvt_pk_bf16_f32 v101, v102, v103\n\t"
    "v_fma_f32 v104, v104, s34, v22\n\t"
    "v_mfma_f32_16x16x32_bf16 v[124:127], a[116:119], a[84:87], v[124:127]\n\t"
    "ds_read_b128 a[156:159], v43 offset:8448\n\t"
    ".p2align 12\n\t"
    "Lstub3_%=:\n\t"
    "s_cbranch_execz Lret_%=\n\t"
    "v_fma_f32 v105, v105, s34, v22\n\t"
    "v_mfma_f32_16x16x32_bf16 v[124:127], a[120:123], a[88:91], v[124:127]\n\t"
    "buffer_load_dwordx4 v16, s[24:27], s47 offen offset:0 lds\n\t"
    "v_mfma_f32_16x16x32_bf16 v[124:127], a[124:127], a[92:95], v[124:127]\n\t"
    "s_waitcnt lgkmcnt(7)\n\t"
    "v_exp_f32 v104, v104\n\t"
    "v_fma_f32 v106, v106, s34, v22\n\t"
    "v_mfma_f32_16x16x32_bf16 v[128:131], a[128:131], a[0:3], 0\n\t"
    "s_waitcnt lgkmcnt(6)\n\t"
    "ds_read_b128 a[96:99], v40 offset:16384\n\t"
    "v_fma_f32 v107, v107, s34, v22\n\t"
    "v_mfma_f32_16x16x32_bf16 v[128:131], a[132:135], a[4:7], v[128:131]\n\t"
    "s_waitcnt lgkmcnt(6)\n\t"
    "v_exp_f32 v105, v105\n\t"
    "v_mfma_f32_16x16x32_bf16 v[128:131], a[136:139], a[8:11], v[128:131]\n\t"
    "s_waitcnt lgkmcnt(5)\n\t"
    "v_exp_f32 v106, v106\n\t"
    "v_mfma_f32_16x16x32_bf16 v[128:131], a[140:143], a[12:15], v[128:131]\n\t"
    "s_waitcnt lgkmcnt(4)\n\t"
    "ds_read_b128 a[100:103], v41 offset:16384\n\t"
    "v_cvt_pk_bf16_f32 v78, v104, v105\n\t"
    "v_mfma_f32_16x16x32_bf16 v[128:131], a[144:147], a[16:19], v[128:131]\n\t"
    "s_waitcnt lgkmcnt(4)\n\t"
    "v_exp_f32 v107, v107\n\t"
    "v_mfma_f32_16x16x32_bf16 v[128:131], a[148:151], a[20:23], v[128:131]\n\t"
    "s_waitcnt lgkmcnt(3)\n\t"
    "v_cvt_pk_bf16_f32 v79, v106, v107\n\t"
    "v_fma_f32 v44, v44, s34, v23\n\t"
    "v_mfma_f32_16x16x32_bf16 v[128:131], a[152:155], a[24:27], v[128:131]\n\t"
    "s_waitcnt lgkmcnt(2)\n\t"
    "ds_read_b128 a[104:107], v42 offset:16384\n\t"
    "v_fma_f32 v45, v45, s34, v23\n\t"
    "v_mfma_f32_16x16x32_bf16 v[128:131], a[156:159], a[28:31], v[128:131]\n\t"
    "v_exp_f32 v44, v44\n\t"
    "v_fma_f32 v46, v46, s34, v23\n\t"
    "v_mfma_f32_16x16x32_bf16 a[164:167], a[160:163], v[76:79], a[164:167]\n\t"
    "v_exp_f32 v45, v45\n\t"
    "v_mfma_f32_16x16x32_bf16 v[104:107], a[128:131], a[32:35], 0\n\t"
    "v_exp_f32 v46, v46\n\t"
    "v_mfma_f32_16x16x32_bf16 v[104:107], a[132:135], a[36:39], v[104:107]\n\t"
    "ds_read_b128 a[108:111], v43 offset:16384\n\t"
    "v_fma_f32 v47, v47, s34, v23\n\t"
    "v_mfma_f32_16x16x32_bf16 v[104:107], a[136:139], a[40:43], v[104:107]\n\t"
    "v_exp_f32 v47, v47\n\t"
    "v_mfma_f32_16x16x32_bf16 v[104:107], a[140:143], a[44:47], v[104:107]\n\t"
    "v_cvt_pk_bf16_f32 v98, v44, v45\n\t"
    "v_cvt_pk_bf16_f32 v99, v46, v47\n\t"
    "v_mfma_f32_16x16x32_bf16 v[104:107], a[144:147], a[48:51], v[104:107]\n\t"
    "ds_read_b128 a[112:115], v40 offset:16640\n\t"
    "v_fma_f32 v88, v88, s34, v24\n\t"
    "v_mfma_f32_16x16x32_bf16 v[104:107], a[148:151], a[52:55], v[104:107]\n\t"
    "v_exp_f32 v88, v88\n\t"
    "v_mfma_f32_16x16x32_bf16 v[104:107], a[152:155], a[56:59], v[104:107]\n\t"
    "buffer_load_dwordx4 v17, s[24:27], s47 offen offset:1024 lds\n\t"
    "v_mfma_f32_16x16x32_bf16 v[104:107], a[156:159], a[60:63], v[104:107]\n\t"
    "ds_read_b128 a[116:119], v41 offset:16640\n\t"
    "v_fma_f32 v89, v89, s34, v24\n\t"
    "v_fma_f32 v90, v90, s34, v24\n\t"
    "v_mfma_f32_16x16x32_bf16 a[168:171], a[160:163], v[96:99], a[168:171]\n\t"
    "v_exp_f32 v89, v89\n\t"
    "v_mfma_f32_16x16x32_bf16 v[44:47], a[128:131], a[64:67], 0\n\t"
    "v_exp_f32 v90, v90\n\t"
    "v_mfma_f32_16x16x32_bf16 v[44:47], a[132:135], a[68:71], v[44:47]\n\t"
    "v_fma_f32 v91, v91, s34, v24\n\t"
    "v_cvt_pk_bf16_f32 v102, v88, v89\n\t"
    "v_mfma_f32_16x16x32_bf16 v[44:47], a[136:139], a[72:75], v[44:47]\n\t"
    "ds_read_b128 a[120:123], v42 offset:16640\n\t"
    "v_fma_f32 v48, v48, s34, v22\n\t"
    "v_mfma_f32_16x16x32_bf16 v[44:47], a[140:143], a[76:79], v[44:47]\n\t"
    "v_exp_f32 v91, v91\n\t"
    "v_mfma_f32_16x16x32_bf16 v[44:47], a[144:147], a[80:83], v[44:47]\n\t"
    "v_exp_f32 v48, v48\n\t"
    "v_mfma_f32_16x16x32_bf16 v[44:47], a[148:151], a[84:87], v[44:47]\n\t"
    "ds_read_b128 a[124:127], v43 offset:16640\n\t"
    "v_cvt_pk_bf16_f32 v103, v90, v91\n\t"
    "v_mfma_f32_16x16x32_bf16 v[44:47], a[152:155], a[88:91], v[44:47]\n\t"
    "v_fma_f32 v49, v49, s34, v22\n\t"
    "v_fma_f32 v50, v50, s34, v22\n\t"
    "v_mfma_f32_16x16x32_bf16 v[44:47], a[156:159], a[92:95], v[44:47]\n\t"
    "s_waitcnt lgkmcnt(7)\n\t"
    "v_exp_f32 v49, v49\n\t"
    "v_fma_f32 v51, v51, s34, v22\n\t"
    "v_mfma_f32_16x16x32_bf16 a[172:175], a[160:163], v[100:103], a[172:175]\n\t"
    "v_exp_f32 v50, v50\n\t"
    "v_mfma_f32_16x16x32_bf16 v[88:91], a[96:99], a[0:3], 0\n\t"
    "s_waitcnt lgkmcnt(6)\n\t"
    "ds_read_b128 a[128:131], v40 offset:24576\n\t"
    "v_cvt_pk_bf16_f32 v48, v48, v49\n\t"
    "v_mfma_f32_16x16x32_bf16 v[88:91], a[100:103], a[4:7], v[88:91]\n\t"
    "s_waitcnt lgkmcnt(6)\n\t"
    "v_exp_f32 v51, v51\n\t"
    "v_mfma_f32_16x16x32_bf16 v[88:91], a[104:107], a[8:11], v[88:91]\n\t"
    "s_waitcnt lgkmcnt(5)\n\t"
    "v_cvt_pk_bf16_f32 v49, v50, v51\n\t"
    "v_fma_f32 v108, v108, s34, v23\n\t"
    "v_mfma_f32_16x16x32_bf16 v[88:91], a[108:111], a[12:15], v[88:91]\n\t"
    "s_waitcnt lgkmcnt(4)\n\t"
    "ds_read_b128 a[132:135], v41 offset:24576\n\t"
    "v_fma_f32 v109, v109, s34, v23\n\t"
    "v_mfma_f32_16x16x32_bf16 v[88:91], a[112:115], a[16:19], v[88:91]\n\t"
    "s_waitcnt lgkmcnt(4)\n\t"
    "v_exp_f32 v108, v108\n\t"
    "v_mfma_f32_16x16x32_bf16 v[88:91], a[116:119], a[20:23], v[88:91]\n\t"
    "s_waitcnt lgkmcnt(3)\n\t"
    "v_exp_f32 v109, v109\n\t"
    "v_mfma_f32_16x16x32_bf16 v[88:91], a[120:123], a[24:27], v[88:91]\n\t"
    "s_waitcnt lgkmcnt(2)\n\t"
    "ds_read_b128 a[136:139], v42 offset:24576\n\t"
    "v_fma_f32 v110, v110, s34, v23\n\t"
    "v_mfma_f32_16x16x32_bf16 v[88:91], a[124:127], a[28:31], v[88:91]\n\t"
    "buffer_load_dwordx4 v18, s[24:27], s47 offen offset:2048 lds\n\t"
    "v_mfma_f32_16x16x32_bf16 v[132:135], a[96:99], a[32:35], 0\n\t"
    "v_exp_f32 v110, v110\n\t"
    "v_mfma_f32_16x16x32_bf16 v[132:135], a[100:103], a[36:39], v[132:135]\n\t"
    "ds_read_b128 a[140:143], v43 offset:24576\n\t"
    "v_fma_f32 v111, v111, s34, v23\n\t"
    "v_mfma_f32_16x16x32_bf16 v[132:135], a[104:107], a[40:43], v[132:135]\n\t"
    "v_exp_f32 v111, v111\n\t"
    "v_mfma_f32_16x16x32_bf16 v[132:135], a[108:111], a[44:47], v[132:135]\n\t"
    "v_cvt_pk_bf16_f32 v108, v108, v109\n\t"
    "v_cvt_pk_bf16_f32 v109, v110, v111\n\t"
    "v_mfma_f32_16x16x32_bf16 v[132:135], a[112:115], a[48:51], v[132:135]\n\t"
    "ds_read_b128 a[144:147], v40 offset:24832\n\t"
    "v_fma_f32 v112, v112, s34, v24\n\t"
    "v_mfma_f32_16x16x32_bf16 v[132:135], a[116:119], a[52:55], v[132:135]\n\t"
    "v_exp_f32 v112, v112\n\t"
    "v_mfma_f32_16x16x32_bf16 v[132:135], a[120:123], a[56:59], v[132:135]\n\t"
    "v_fma_f32 v113, v113, s34, v24\n\t"
    "v_fma_f32 v114, v114, s34, v24\n\t"
    "v_mfma_f32_16x16x32_bf16 v[132:135], a[124:127], a[60:63], v[132:135]\n\t"
    "ds_read_b128 a[148:151], v41 offset:24832\n\t"
    "v_exp_f32 v113, v113\n\t"
    "v_mfma_f32_16x16x32_bf16 v[136:139], a[96:99], a[64:67], 0\n\t"
    "v_exp_f32 v114, v114\n\t"
    "v_mfma_f32_16x16x32_bf16 v[136:139], a[100:103], a[68:71], v[136:139]\n\t"
    "v_fma_f32 v115, v115, s34, v24\n\t"
    "v_cvt_pk_bf16_f32 v112, v112, v113\n\t"
    "v_mfma_f32_16x16x32_bf16 v[136:139], a[104:107], a[72:75], v[136:139]\n\t"
    "ds_read_b128 a[152:155], v42 offset:24832\n\t"
    "v_fma_f32 v116, v116, s34, v22\n\t"
    "v_mfma_f32_16x16x32_bf16 v[136:139], a[108:111], a[76:79], v[136:139]\n\t"
    "v_exp_f32 v115, v115\n\t"
    "v_mfma_f32_16x16x32_bf16 v[136:139], a[112:115], a[80:83], v[136:139]\n\t"
    "s_waitcnt vmcnt(15)\n\t"
    "s_barrier\n\t"
    "v_cvt_pk_bf16_f32 v113, v114, v115\n\t"
    "v_mfma_f32_16x16x32_bf16 v[136:139], a[116:119], a[84:87], v[136:139]\n\t"
    "ds_read_b128 a[156:159], v43 offset:24832\n\t"
    "v_fma_f32 v117, v117, s34, v22\n\t"
    "v_mfma_f32_16x16x32_bf16 v[136:139], a[120:123], a[88:91], v[136:139]\n\t"
    "buffer_load_dwordx4 v19, s[24:27], s47 offen offset:3072 lds\n\t"
    "v_mfma_f32_16x16x32_bf16 v[136:139], a[124:127], a[92:95], v[136:139]\n\t"
    "s_waitcnt lgkmcnt(7)\n\t"
    "v_exp_f32 v116, v116\n\t"
    "v_fma_f32 v118, v118, s34, v22\n\t"
    "v_mfma_f32_16x16x32_bf16 v[140:143], a[128:131], a[0:3], 0\n\t"
    "s_waitcnt lgkmcnt(6)\n\t"
    "ds_read_b128 a[96:99], v40 offset:32768\n\t"
    "v_fma_f32 v119, v119, s34, v22\n\t"
    "v_mfma_f32_16x16x32_bf16 v[140:143], a[132:135], a[4:7], v[140:143]\n\t"
    "s_waitcnt lgkmcnt(6)\n\t"
    "v_exp_f32 v117, v117\n\t"
    "v_mfma_f32_16x16x32_bf16 v[140:143], a[136:139], a[8:11], v[140:143]\n\t"
    "s_waitcnt lgkmcnt(5)\n\t"
    "v_exp_f32 v118, v118\n\t"
    "v_mfma_f32_16x16x32_bf16 v[140:143], a[140:143], a[12:15], v[140:143]\n\t"
    "s_waitcnt lgkmcnt(4)\n\t"
    "ds_read_b128 a[100:103], v41 offset:32768\n\t"
    "v_cvt_pk_bf16_f32 v50, v116, v117\n\t"
    "v_mfma_f32_16x16x32_bf16 v[140:143], a[144:147], a[16:19], v[140:143]\n\t"
    "s_waitcnt lgkmcnt(4)\n\t"
    "v_exp_f32 v119, v119\n\t"
    "v_mfma_f32_16x16x32_bf16 v[140:143], a[148:151], a[20:23], v[140:143]\n\t"
    "s_waitcnt lgkmcnt(3)\n\t"
    "v_cvt_pk_bf16_f32 v51, v118, v119\n\t"
    "v_fma_f32 v64, v64, s34, v23\n\t"
    "v_mfma_f32_16x16x32_bf16 v[140:143], a[152:155], a[24:27], v[140:143]\n\t"
    "s_waitcnt lgkmcnt(2)\n\t"
    "ds_read_b128 a[104:107], v42 offset:32768\n\t"
    "v_fma_f32 v65, v65, s34, v23\n\t"
    "v_mfma_f32_16x16x32_bf16 v[140:143], a[156:159], a[28:31], v[140:143]\n\t"
    "v_exp_f32 v64, v64\n\t"
    "v_fma_f32 v66, v66, s34, v23\n\t"
    "v_mfma_f32_16x16x32_bf16 a[164:167], a[160:163], v[48:51], a[164:167]\n\t"
    "v_exp_f32 v65, v65\n\t"
    "v_mfma_f32_16x16x32_bf16 v[116:119], a[128:131], a[32:35], 0\n\t"
    "v_exp_f32 v66, v66\n\t"
    "v_mfma_f32_16x16x32_bf16 v[116:119], a[132:135], a[36:39], v[116:119]\n\t"
    "ds_read_b128 a[108:111], v43 offset:32768\n\t"
    "v_fma_f32 v67, v67, s34, v23\n\t"
    "v_mfma_f32_16x16x32_bf16 v[116:119], a[136:139], a[40:43], v[116:119]\n\t"
    "v_exp_f32 v67, v67\n\t"
    "v_mfma_f32_16x16x32_bf16 v[116:119], a[140:143], a[44:47], v[116:119]\n\t"
    "v_cvt_pk_bf16_f32 v110, v64, v65\n\t"
    "v_cvt_pk_bf16_f32 v111, v66, v67\n\t"
    "v_mfma_f32_16x16x32_bf16 v[116:119], a[144:147], a[48:51], v[116:119]\n\t"
    "ds_read_b128 a[112:115], v40 offset:33024\n\t"
    "v_fma_f32 v68, v68, s34, v24\n\t"
    "v_mfma_f32_16x16x32_bf16 v[116:119], a[148:151], a[52:55], v[116:119]\n\t"
    "v_exp_f32 v68, v68\n\t"
    "v_mfma_f32_16x16x32_bf16 v[116:119], a[152:155], a[56:59], v[116:119]\n\t"
    "v_fma_f32 v69, v69, s34, v24\n\t"
    "v_fma_f32 v70, v70, s34, v24\n\t"
    "v_mfma_f32_16x16x32_bf16 v[116:119], a[156:159], a[60:63], v[116:119]\n\t"
    "s_add_u32 m0, s40, 131072\n\t"
    "ds_read_b128 a[116:119], v41 offset:33024\n\t"
    "v_exp_f32 v69, v69\n\t"
    "v_mfma_f32_16x16x32_bf16 a[168:171], a[160:163], v[108:111], a[168:171]\n\t"
    "v_exp_f32 v70, v70\n\t"
    "v_mfma_f32_16x16x32_bf16 v[64:67], a[128:131], a[64:67], 0\n\t"
    "s_add_u32 s46, s42, 131072\n\t"
    "v_fma_f32 v71, v71, s34, v24\n\t"
    "v_cvt_pk_bf16_f32 v114, v68, v69\n\t"
    "v_mfma_f32_16x16x32_bf16 v[64:67], a[132:135], a[68:71], v[64:67]\n\t"
    "v_exp_f32 v71, v71\n\t"
    "v_mfma_f32_16x16x32_bf16 v[64:67], a[136:139], a[72:75], v[64:67]\n\t"
    "ds_read_b128 a[120:123], v42 offset:33024\n\t"
    "v_cvt_pk_bf16_f32 v115, v70, v71\n\t"
    "v_mfma_f32_16x16x32_bf16 v[64:67], a[140:143], a[76:79], v[64:67]\n\t"
    "buffer_load_dwordx4 v16, s[24:27], s46 offen offset:0 lds\n\t"
    "v_mfma_f32_16x16x32_bf16 v[64:67], a[144:147], a[80:83], v[64:67]\n\t"
    "v_fma_f32 v92, v92, s34, v22\n\t"
    "v_fma_f32 v93, v93, s34, v22\n\t"
    "v_mfma_f32_16x16x32_bf16 v[64:67], a[148:151], a[84:87], v[64:67]\n\t"
    "ds_read_b128 a[124:127], v43 offset:33024\n\t"
    "v_fma_f32 v94, v94, s34, v22\n\t"
    "v_mfma_f32_16x16x32_bf16 v[64:67], a[152:155], a[88:91], v[64:67]\n\t"
    "v_exp_f32 v92, v92\n\t"
    "v_mfma_f32_16x16x32_bf16 v[64:67], a[156:159], a[92:95], v[64:67]\n\t"
    "s_waitcnt lgkmcnt(7)\n\t"
    "v_exp_f32 v93, v93\n\t"
    "v_fma_f32 v95, v95, s34, v22\n\t"
    "v_mfma_f32_16x16x32_bf16 a[172:175], a[160:163], v[112:115], a[172:175]\n\t"
    "v_exp_f32 v94, v94\n\t"
    "v_mfma_f32_16x16x32_bf16 v[68:71], a[96:99], a[0:3], 0\n\t"
    "s_waitcnt lgkmcnt(6)\n\t"
    "ds_read_b128 a[128:131], v40 offset:40960\n\t"
    "v_cvt_pk_bf16_f32 v92, v92, v93\n\t"
    "v_mfma_f32_16x16x32_bf16 v[68:71], a[100:103], a[4:7], v[68:71]\n\t"
    "s_waitcnt lgkmcnt(6)\n\t"
    "v_exp_f32 v95, v95\n\t"
    "v_mfma_f32_16x16x32_bf16 v[68:71], a[104:107], a[8:11], v[68:71]\n\t"
    "s_waitcnt lgkmcnt(5)\n\t"
    "v_cvt_pk_bf16_f32 v93, v94, v95\n\t"
    "v_fma_f32 v120, v120, s34, v23\n\t"
    "v_mfma_f32_16x16x32_bf16 v[68:71], a[108:111], a[12:15], v[68:71]\n\t"
    "s_waitcnt lgkmcnt(4)\n\t"
    "ds_read_b128 a[132:135], v41 offset:40960\n\t"
    "v_fma_f32 v121, v121, s34, v23\n\t"
    "v_mfma_f32_16x16x32_bf16 v[68:71], a[112:115], a[16:19], v[68:71]\n\t"
    "s_waitcnt lgkmcnt(4)\n\t"
    "v_exp_f32 v120, v120\n\t"
    "v_mfma_f32_16x16x32_bf16 v[68:71], a[116:119], a[20:23], v[68:71]\n\t"
    "s_waitcnt lgkmcnt(3)\n\t"
    "v_exp_f32 v121, v121\n\t"
    "v_mfma_f32_16x16x32_bf16 v[68:71], a[120:123], a[24:27], v[68:71]\n\t"
    "s_waitcnt lgkmcnt(2)\n\t"
    "ds_read_b128 a[136:139], v42 offset:40960\n\t"
    "v_fma_f32 v122, v122, s34, v23\n\t"
    "v_mfma_f32_16x16x32_bf16 v[68:71], a[124:127], a[28:31], v[68:71]\n\t"
    "v_exp_f32 v122, v122\n\t"
    "v_fma_f32 v123, v123, s34, v23\n\t"
    "v_mfma_f32_16x16x32_bf16 v[144:147], a[96:99], a[32:35], 0\n\t"
    "v_exp_f32 v123, v123\n\t"
    "v_mfma_f32_16x16x32_bf16 v[144:147], a[100:103], a[36:39], v[144:147]\n\t"
    "ds_read_b128 a[140:143], v43 offset:40960\n\t"
    "v_cvt_pk_bf16_f32 v120, v120, v121\n\t"
    "v_mfma_f32_16x16x32_bf16 v[144:147], a[104:107], a[40:43], v[144:147]\n\t"
    "v_cvt_pk_bf16_f32 v121, v122, v123\n\t"
    "v_fma_f32 v124, v124, s34, v24\n\t"
    "v_mfma_f32_16x16x32_bf16 v[144:147], a[108:111], a[44:47], v[144:147]\n\t"
    "v_exp_f32 v124, v124\n\t"
    "v_mfma_f32_16x16x32_bf16 v[144:147], a[112:115], a[48:51], v[144:147]\n\t"
    "ds_read_b128 a[144:147], v40 offset:41216\n\t"
    "v_fma_f32 v125, v125, s34, v24\n\t"
    "v_mfma_f32_16x16x32_bf16 v[144:147], a[116:119], a[52:55], v[144:147]\n\t"
    "buffer_load_dwordx4 v17, s[24:27], s46 offen offset:1024 lds\n\t"
    "v_mfma_f32_16x16x32_bf16 v[144:147], a[120:123], a[56:59], v[144:147]\n\t"
    "v_exp_f32 v125, v125\n\t"
    "v_mfma_f32_16x16x32_bf16 v[144:147], a[124:127], a[60:63], v[144:147]\n\t"
    "ds_read_b128 a[148:151], v41 offset:41216\n\t"
    "v_fma_f32 v126, v126, s34, v24\n\t"
    "v_fma_f32 v127, v127, s34, v24\n\t"
    "v_mfma_f32_16x16x32_bf16 v[148:151], a[96:99], a[64:67], 0\n\t"
    "v_exp_f32 v126, v126\n\t"
    "v_mfma_f32_16x16x32_bf16 v[148:151], a[100:103], a[68:71], v[148:151]\n\t"
    "v_exp_f32 v127, v127\n\t"
    "v_mfma_f32_16x16x32_bf16 v[148:151], a[104:107], a[72:75], v[148:151]\n\t"
    "ds_read_b128 a[152:155], v42 offset:41216\n\t"
    "v_cvt_pk_bf16_f32 v124, v124, v125\n\t"
    "v_mfma_f32_16x16x32_bf16 v[148:151], a[108:111], a[76:79], v[148:151]\n\t"
    "v_cvt_pk_bf16_f32 v125, v126, v127\n\t"
    "v_fma_f32 v128, v128, s34, v22\n\t"
    "v_mfma_f32_16x16x32_bf16 v[148:151], a[112:115], a[80:83], v[148:151]\n\t"
    "v_exp_f32 v128, v128\n\t"
    "v_mfma_f32_16x16x32_bf16 v[148:151], a[116:119], a[84:87], v[148:151]\n\t"
    "ds_read_b128 a[156:159], v43 offset:41216\n\t"
    "v_fma_f32 v129, v129, s34, v22\n\t"
    "v_mfma_f32_16x16x32_bf16 v[148:151], a[120:123], a[88:91], v[148:151]\n\t"
    "v_exp_f32 v129, v129\n\t"
    "v_mfma_f32_16x16x32_bf16 v[148:151], a[124:127], a[92:95], v[148:151]\n\t"
    "s_waitcnt lgkmcnt(7)\n\t"
    "v_fma_f32 v130, v130, s34, v22\n\t"
    "v_fma_f32 v131, v131, s34, v22\n\t"
    "v_cvt_pk_bf16_f32 v94, v128, v129\n\t"
    "v_mfma_f32_16x16x32_bf16 v[152:155], a[128:131], a[0:3], 0\n\t"
    "s_waitcnt lgkmcnt(6)\n\t"
    "ds_read_b128 a[96:99], v40 offset:49152\n\t"
    "v_fma_f32 v104, v104, s34, v23\n\t"
    "v_mfma_f32_16x16x32_bf16 v[152:155], a[132:135], a[4:7], v[152:155]\n\t"
    "s_waitcnt lgkmcnt(6)\n\t"
    "v_exp_f32 v130, v130\n\t"
    "v_mfma_f32_16x16x32_bf16 v[152:155], a[136:139], a[8:11], v[152:155]\n\t"
    "s_waitcnt lgkmcnt(5)\n\t"
    "v_exp_f32 v131, v131\n\t"
    "v_mfma_f32_16x16x32_bf16 v[152:155], a[140:143], a[12:15], v[152:155]\n\t"
    "s_waitcnt lgkmcnt(4)\n\t"
    "ds_read_b128 a[100:103], v41 offset:49152\n\t"
    "v_cvt_pk_bf16_f32 v95, v130, v131\n\t"
    "v_mfma_f32_16x16x32_bf16 v[152:155], a[144:147], a[16:19], v[152:155]\n\t"
    "s_waitcnt lgkmcnt(4)\n\t"
    "v_exp_f32 v104, v104\n\t"
    "v_mfma_f32_16x16x32_bf16 v[152:155], a[148:151], a[20:23], v[152:155]\n\t"
    "s_waitcnt lgkmcnt(3)\n\t"
    "v_fma_f32 v105, v105, s34, v23\n\t"
    "v_fma_f32 v106, v106, s34, v23\n\t"
    "v_mfma_f32_16x16x32_bf16 v[152:155], a[152:155], a[24:27], v[152:155]\n\t"
    "s_waitcnt lgkmcnt(2)\n\t"
    "ds_read_b128 a[104:107], v42 offset:49152\n\t"
    "v_fma_f32 v107, v107, s34, v23\n\t"
    "v_mfma_f32_16x16x32_bf16 v[152:155], a[156:159], a[28:31], v[152:155]\n\t"
    "v_exp_f32 v105, v105\n\t"
    "v_fma_f32 v44, v44, s34, v24\n\t"
    "v_mfma_f32_16x16x32_bf16 a[164:167], a[160:163], v[92:95], a[164:167]\n\t"
    "v_exp_f32 v106, v106\n\t"
    "v_mfma_f32_16x16x32_bf16 v[128:131], a[128:131], a[32:35], 0\n\t"
    "buffer_load_dwordx4 v18, s[24:27], s46 offen offset:2048 lds\n\t"
    "v_mfma_f32_16x16x32_bf16 v[128:131], a[132:135], a[36:39], v[128:131]\n\t"
    "ds_read_b128 a[108:111], v43 offset:49152\n\t"
    "v_cvt_pk_bf16_f32 v122, v104, v105\n\t"
    "v_mfma_f32_16x16x32_bf16 v[128:131], a[136:139], a[40:43], v[128:131]\n\t"
    "v_exp_f32 v107, v107\n\t"
    "v_mfma_f32_16x16x32_bf16 v[128:131], a[140:143], a[44:47], v[128:131]\n\t"
    "v_exp_f32 v44, v44\n\t"
    "v_mfma_f32_16x16x32_bf16 v[128:131], a[144:147], a[48:51], v[128:131]\n\t"
    "ds_read_b128 a[112:115], v40 offset:49408\n\t"
    "v_cvt_pk_bf16_f32 v123, v106, v107\n\t"
    "v_mfma_f32_16x16x32_bf16 v[128:131], a[148:151], a[52:55], v[128:131]\n\t"
    "v_fma_f32 v45, v45, s34, v24\n\t"
    "v_fma_f32 v46, v46, s34, v24\n\t"
    "v_mfma_f32_16x16x32_bf16 v[128:131], a[152:155], a[56:59], v[128:131]\n\t"
    "v_exp_f32 v45, v45\n\t"
    "v_mfma_f32_16x16x32_bf16 v[128:131], a[156:159], a[60:63], v[128:131]\n\t"
    "ds_read_b128 a[116:119], v41 offset:49408\n\t"
    "v_exp_f32 v46, v46\n\t"
    "v_mfma_f32_16x16x32_bf16 a[168:171], a[160:163], v[120:123], a[168:171]\n\t"
    "v_fma_f32 v47, v47, s34, v24\n\t"
    "v_cvt_pk_bf16_f32 v126, v44, v45\n\t"
    "v_mfma_f32_16x16x32_bf16 v[104:107], a[128:131], a[64:67], 0\n\t"
    "v_exp_f32 v47, v47\n\t"
    "v_mfma_f32_16x16x32_bf16 v[104:107], a[132:135], a[68:71], v[104:107]\n\t"
    "v_cvt_pk_bf16_f32 v127, v46, v47\n\t"
    "v_fma_f32 v88, v88, s34, v22\n\t"
    "v_mfma_f32_16x16x32_bf16 v[104:107], a[136:139], a[72:75], v[104:107]\n\t"
    "ds_read_b128 a[120:123], v42 offset:49408\n\t"
    "v_fma_f32 v89, v89, s34, v22\n\t"
    "v_mfma_f32_16x16x32_bf16 v[104:107], a[140:143], a[76:79], v[104:107]\n\t"
    "v_exp_f32 v88, v88\n\t"
    "v_mfma_f32_16x16x32_bf16 v[104:107], a[144:147], a[80:83], v[104:107]\n\t"
    "v_exp_f32 v89, v89\n\t"
    "v_mfma_f32_16x16x32_bf16 v[104:107], a[148:151], a[84:87], v[104:107]\n\t"
    "ds_read_b128 a[124:127], v43 offset:49408\n\t"
    "v_fma_f32 v90, v90, s34, v22\n\t"
    "v_mfma_f32_16x16x32_bf16 v[104:107], a[152:155], a[88:91], v[104:107]\n\t"
    "v_exp_f32 v90, v90\n\t"
    "v_mfma_f32_16x16x32_bf16 v[104:107], a[156:159], a[92:95], v[104:107]\n\t"
    "s_waitcnt lgkmcnt(7)\n\t"
    "v_fma_f32 v91, v91, s34, v22\n\t"
    "v_cvt_pk_bf16_f32 v88, v88, v89\n\t"
    "v_fma_f32 v132, v132, s34, v23\n\t"
    "v_mfma_f32_16x16x32_bf16 a[172:175], a[160:163], v[124:127], a[172:175]\n\t"
    "v_exp_f32 v91, v91\n\t"
    "v_mfma_f32_16x16x32_bf16 v[44:47], a[96:99], a[0:3], 0\n\t"
    "s_waitcnt lgkmcnt(6)\n\t"
    "ds_read_b128 a[128:131], v40 offset:57344\n\t"
    "v_cvt_pk_bf16_f32 v89, v90, v91\n\t"
    "v_mfma_f32_16x16x32_bf16 v[44:47], a[100:103], a[4:7], v[44:47]\n\t"
    "s_waitcnt lgkmcnt(6)\n\t"
    "v_exp_f32 v132, v132\n\t"
    "v_mfma_f32_16x16x32_bf16 v[44:47], a[104:107], a[8:11], v[44:47]\n\t"
    "s_waitcnt lgkmcnt(5)\n\t"
    "v_fma_f32 v133, v133, s34, v23\n\t"
    "v_fma_f32 v134, v134, s34, v23\n\t"
    "v_mfma_f32_16x16x32_bf16 v[44:47], a[108:111], a[12:15], v[44:47]\n\t"
    "s_waitcnt lgkmcnt(4)\n\t"
    "ds_read_b128 a[132:135], v41 offset:57344\n\t"
    "v_fma_f32 v135, v135, s34, v23\n\t"
    "v_mfma_f32_16x16x32_bf16 v[44:47], a[112:115], a[16:19], v[44:47]\n\t"
    "s_waitcnt lgkmcnt(4)\n\t"
    "buffer_load_dwordx4 v19, s[24:27], s46 offen offset:3072 lds\n\t"
    "v_mfma_f32_16x16x32_bf16 v[44:47], a[116:119], a[20:23], v[44:47]\n\t"
    "s_waitcnt lgkmcnt(3)\n\t"
    "v_exp_f32 v133, v133\n\t"
    "v_mfma_f32_16x16x32_bf16 v[44:47], a[120:123], a[24:27], v[44:47]\n\t"
    "s_waitcnt lgkmcnt(2)\n\t"
    "ds_read_b128 a[136:139], v42 offset:57344\n\t"
    "v_cvt_pk_bf16_f32 v132, v132, v133\n\t"
    "v_mfma_f32_16x16x32_bf16 v[44:47], a[124:127], a[28:31], v[44:47]\n\t"
    "v_exp_f32 v134, v134\n\t"
    "v_fma_f32 v136, v136, s34, v24\n\t"
    "v_mfma_f32_16x16x32_bf16 v[156:159], a[96:99], a[32:35], 0\n\t"
    "v_exp_f32 v135, v135\n\t"
    "v_mfma_f32_16x16x32_bf16 v[156:159], a[100:103], a[36:39], v[156:159]\n\t"
    "ds_read_b128 a[140:143], v43 offset:57344\n\t"
    "v_cvt_pk_bf16_f32 v133, v134, v135\n\t"
    "v_mfma_f32_16x16x32_bf16 v[156:159], a[104:107], a[40:43], v[156:159]\n\t"
    "v_exp_f32 v136, v136\n\t"
    "v_mfma_f32_16x16x32_bf16 v[156:159], a[108:111], a[44:47], v[156:159]\n\t"
    "v_fma_f32 v137, v137, s34, v24\n\t"
    "v_fma_f32 v138, v138, s34, v24\n\t"
    "v_mfma_f32_16x16x32_bf16 v[156:159], a[112:115], a[48:51], v[156:159]\n\t"
    "ds_read_b128 a[144:147], v40 offset:57600\n\t"
    "v_fma_f32 v139, v139, s34, v24\n\t"
    "v_mfma_f32_16x16x32_bf16 v[156:159], a[116:119], a[52:55], v[156:159]\n\t"
    "v_exp_f32 v137, v137\n\t"
    "v_mfma_f32_16x16x32_bf16 v[156:159], a[120:123], a[56:59], v[156:159]\n\t"
    "v_exp_f32 v138, v138\n\t"
    "v_mfma_f32_16x16x32_bf16 v[156:159], a[124:127], a[60:63], v[156:159]\n\t"
    "ds_read_b128 a[148:151], v41 offset:57600\n\t"
    "v_exp_f32 v139, v139\n\t"
    "v_mfma_f32_16x16x32_bf16 v[160:163], a[96:99], a[64:67], 0\n\t"
    "v_cvt_pk_bf16_f32 v136, v136, v137\n\t"
    "v_cvt_pk_bf16_f32 v137, v138, v139\n\t"
    "v_mfma_f32_16x16x32_bf16 v[160:163], a[100:103], a[68:71], v[160:163]\n\t"
    "v_fma_f32 v140, v140, s34, v22\n\t"
    "v_fma_f32 v141, v141, s34, v22\n\t"
    "v_mfma_f32_16x16x32_bf16 v[160:163], a[104:107], a[72:75], v[160:163]\n\t"
    "ds_read_b128 a[152:155], v42 offset:57600\n\t"
    "v_fma_f32 v142, v142, s34, v22\n\t"
    "v_mfma_f32_16x16x32_bf16 v[160:163], a[108:111], a[76:79], v[160:163]\n\t"
    "s_add_u32 m0, s40, 135168\n\t"
    "v_exp_f32 v140, v140\n\t"
    "v_mfma_f32_16x16x32_bf16 v[160:163], a[112:115], a[80:83], v[160:163]\n\t"
    "s_waitcnt vmcnt(8)\n\t"
    "s_barrier\n\t"
    "v_fma_f32 v143, v143, s34, v22\n\t"
    "v_mfma_f32_16x16x32_bf16 v[160:163], a[116:119], a[84:87], v[160:163]\n\t"
    "s_add_u32 s47, s42, 147456\n\t"
    "ds_read_b128 a[156:159], v43 offset:57600\n\t"
    "v_fma_f32 v116, v116, s34, v23\n\t"
    "v_mfma_f32_16x16x32_bf16 v[160:163], a[120:123], a[88:91], v[160:163]\n\t"
    "buffer_load_dwordx4 v16, s[24:27], s47 offen offset:0 lds\n\t"
    "v_mfma_f32_16x16x32_bf16 v[160:163], a[124:127], a[92:95], v[160:163]\n\t"
    "s_waitcnt lgkmcnt(7)\n\t"
    "v_exp_f32 v141, v141\n\t"
    "v_fma_f32 v117, v117, s34, v23\n\t"
    "v_mfma_f32_16x16x32_bf16 v[164:167], a[128:131], a[0:3], 0\n\t"
    "s_waitcnt lgkmcnt(6)\n\t"
    "ds_read_b128 a[96:99], v4 offset:0\n\t"
    "v_cvt_pk_bf16_f32 v90, v140, v141\n\t"
    "v_mfma_f32_16x16x32_bf16 v[164:167], a[132:135], a[4:7], v[164:167]\n\t"
    "s_waitcnt lgkmcnt(6)\n\t"
    "v_exp_f32 v142, v142\n\t"
    "v_mfma_f32_16x16x32_bf16 v[164:167], a[136:139], a[8:11], v[164:167]\n\t"
    "s_waitcnt lgkmcnt(5)\n\t"
    "v_exp_f32 v143, v143\n\t"
    "v_mfma_f32_16x16x32_bf16 v[164:167], a[140:143], a[12:15], v[164:167]\n\t"
    "s_waitcnt lgkmcnt(4)\n\t"
    "ds_read_b128 a[100:103], v5 offset:0\n\t"
    "v_cvt_pk_bf16_f32 v91, v142, v143\n\t"
    "v_mfma_f32_16x16x32_bf16 v[164:167], a[144:147], a[16:19], v[164:167]\n\t"
    "s_waitcnt lgkmcnt(4)\n\t"
    "v_exp_f32 v116, v116\n\t"
    "v_mfma_f32_16x16x32_bf16 v[164:167], a[148:151], a[20:23], v[164:167]\n\t"
    "s_waitcnt lgkmcnt(3)\n\t"
    "v_exp_f32 v117, v117\n\t"
    "v_mfma_f32_16x16x32_bf16 v[164:167], a[152:155], a[24:27], v[164:167]\n\t"
    "s_waitcnt lgkmcnt(2)\n\t"
    "ds_read_b128 a[104:107], v6 offset:0\n\t"
    "v_fma_f32 v118, v118, s34, v23\n\t"
    "v_mfma_f32_16x16x32_bf16 v[164:167], a[156:159], a[28:31], v[164:167]\n\t"
    "v_exp_f32 v118, v118\n\t"
    "v_fma_f32 v119, v119, s34, v23\n\t"
    "v_mfma_f32_16x16x32_bf16 a[164:167], a[160:163], v[88:91], a[164:167]\n\t"
    "v_exp_f32 v119, v119\n\t"
    "v_mfma_f32_16x16x32_bf16 v[140:143], a[128:131], a[32:35], 0\n\t"
    "v_cvt_pk_bf16_f32 v134, v116, v117\n\t"
    "v_cvt_pk_bf16_f32 v135, v118, v119\n\t"
    "v_mfma_f32_16x16x32_bf16 v[140:143], a[132:135], a[36:39], v[140:143]\n\t"
    "ds_read_b128 a[108:111], v7 offset:0\n\t"
    "v_fma_f32 v64, v64, s34, v24\n\t"
    "v_mfma_f32_16x16x32_bf16 v[140:143], a[136:139], a[40:43], v[140:143]\n\t"
    "v_exp_f32 v64, v64\n\t"
    "v_mfma_f32_16x16x32_bf16 v[140:143], a[140:143], a[44:47], v[140:143]\n\t"
    "v_fma_f32 v65, v65, s34, v24\n\t"
    "v_fma_f32 v66, v66, s34, v24\n\t"
    "v_mfma_f32_16x16x32_bf16 v[140:143], a[144:147], a[48:51], v[140:143]\n\t"
    "ds_read_b128 a[112:115], v4 offset:256\n\t"
    "v_fma_f32 v67, v67, s34, v24\n\t"
    "v_mfma_f32_16x16x32_bf16 v[140:143], a[148:151], a[52:55], v[140:143]\n\t"
    "v_exp_f32 v65, v65\n\t"
    "v_mfma_f32_16x16x32_bf16 v[140:143], a[152:155], a[56:59], v[140:143]\n\t"
    "v_exp_f32 v66, v66\n\t"
    "v_mfma_f32_16x16x32_bf16 v[140:143], a[156:159], a[60:63], v[140:143]\n\t"
    "ds_read_b128 a[116:119], v5 offset:256\n\t"
    "v_exp_f32 v67, v67\n\t"
    "v_mfma_f32_16x16x32_bf16 a[168:171], a[160:163], v[132:135], a[168:171]\n\t"
    "v_cvt_pk_bf16_f32 v138, v64, v65\n\t"
    "v_cvt_pk_bf16_f32 v139, v66, v67\n\t"
    "v_mfma_f32_16x16x32_bf16 v[116:119], a[128:131], a[64:67], 0\n\t"
    "v_fma_f32 v68, v68, s34, v22\n\t"
    "v_fma_f32 v69, v69, s34, v22\n\t"
    "v_mfma_f32_16x16x32_bf16 v[116:119], a[132:135], a[68:71], v[116:119]\n\t"
    "buffer_load_dwordx4 v17, s[24:27], s47 offen offset:1024 lds\n\t"
    "v_mfma_f32_16x16x32_bf16 v[116:119], a[136:139], a[72:75], v[116:119]\n\t"
    "ds_read_b128 a[120:123], v6 offset:256\n\t"
    "v_fma_f32 v70, v70, s34, v22\n\t"
    "v_mfma_f32_16x16x32_bf16 v[116:119], a[140:143], a[76:79], v[116:119]\n\t"
    "v_exp_f32 v68, v68\n\t"
    "v_mfma_f32_16x16x32_bf16 v[116:119], a[144:147], a[80:83], v[116:119]\n\t"
    "v_exp_f32 v69, v69\n\t"
    "v_mfma_f32_16x16x32_bf16 v[116:119], a[148:151], a[84:87], v[116:119]\n\t"
    "ds_read_b128 a[124:127], v7 offset:256\n\t"
    "v_fma_f32 v71, v71, s34, v22\n\t"
    "v_mfma_f32_16x16x32_bf16 v[116:119], a[152:155], a[88:91], v[116:119]\n\t"
    "v_exp_f32 v70, v70\n\t"
    "v_mfma_f32_16x16x32_bf16 v[116:119], a[156:159], a[92:95], v[116:119]\n\t"
    "s_waitcnt lgkmcnt(7)\n\t"
    "v_exp_f32 v71, v71\n\t"
    "v_cvt_pk_bf16_f32 v68, v68, v69\n\t"
    "v_mfma_f32_16x16x32_bf16 a[172:175], a[160:163], v[136:139], a[172:175]\n\t"
    "v_cvt_pk_bf16_f32 v69, v70, v71\n\t"
    "v_fma_f32 v144, v144, s34, v23\n\t"
    "v_mfma_f32_16x16x32_bf16 v[64:67], a[96:99], a[0:3], 0\n\t"
    "s_waitcnt lgkmcnt(6)\n\t"
    "ds_read_b128 a[128:131], v4 offset:8192\n\t"
    "v_fma_f32 v145, v145, s34, v23\n\t"
    "v_mfma_f32_16x16x32_bf16 v[64:67], a[100:103], a[4:7], v[64:67]\n\t"
    "s_waitcnt lgkmcnt(6)\n\t"
    "v_exp_f32 v144, v144\n\t"
    "v_mfma_f32_16x16x32_bf16 v[64:67], a[104:107], a[8:11], v[64:67]\n\t"
    "s_waitcnt lgkmcnt(5)\n\t"
    "v_exp_f32 v145, v145\n\t"
    "v_mfma_f32_16x16x32_bf16 v[64:67], a[108:111], a[12:15], v[64:67]\n\t"
    "s_waitcnt lgkmcnt(4)\n\t"
    "ds_read_b128 a[132:135], v5 offset:8192\n\t"
    "v_fma_f32 v146, v146, s34, v23\n\t"
    "v_mfma_f32_16x16x32_bf16 v[64:67], a[112:115], a[16:19], v[64:67]\n\t"
    "s_waitcnt lgkmcnt(4)\n\t"
    "v_exp_f32 v146, v146\n\t"
    "v_mfma_f32_16x16x32_bf16 v[64:67], a[116:119], a[20:23], v[64:67]\n\t"
    "s_waitcnt lgkmcnt(3)\n\t"
    "v_fma_f32 v147, v147, s34, v23\n\t"
    "v_cvt_pk_bf16_f32 v144, v144, v145\n\t"
    "v_mfma_f32_16x16x32_bf16 v[64:67], a[120:123], a[24:27], v[64:67]\n\t"
    "s_waitcnt lgkmcnt(2)\n\t"
    "ds_read_b128 a[136:139], v6 offset:8192\n\t"
    "v_fma_f32 v148, v148, s34, v24\n\t"
    "v_mfma_f32_16x16x32_bf16 v[64:67], a[124:127], a[28:31], v[64:67]\n\t"
    "v_exp_f32 v147, v147\n\t"
    "v_fma_f32 v149, v149, s34, v24\n\t"
    "v_mfma_f32_16x16x32_bf16 v[168:171], a[96:99], a[32:35], 0\n\t"
    "v_exp_f32 v148, v148\n\t"
    "v_mfma_f32_16x16x32_bf16 v[168:171], a[100:103], a[36:39], v[168:171]\n\t"
    "ds_read_b128 a[140:143], v7 offset:8192\n\t"
    "v_cvt_pk_bf16_f32 v145, v146, v147\n\t"
    "v_mfma_f32_16x16x32_bf16 v[168:171], a[104:107], a[40:43], v[168:171]\n\t"
    "v_exp_f32 v149, v149\n\t"
    "v_mfma_f32_16x16x32_bf16 v[168:171], a[108:111], a[44:47], v[168:171]\n\t"
    "v_fma_f32 v150, v150, s34, v24\n\t"
    "v_fma_f32 v151, v151, s34, v24\n\t"
    "v_mfma_f32_16x16x32_bf16 v[168:171], a[112:115], a[48:51], v[168:171]\n\t"
    "ds_read_b128 a[144:147], v4 offset:8448\n\t"
    "v_cvt_pk_bf16_f32 v148, v148, v149\n\t"
    "v_mfma_f32_16x16x32_bf16 v[168:171], a[116:119], a[52:55], v[168:171]\n\t"
    "buffer_load_dwordx4 v18, s[24:27], s47 offen offset:2048 lds\n\t"
    "v_mfma_f32_16x16x32_bf16 v[168:171], a[120:123], a[56:59], v[168:171]\n\t"
    "v_exp_f32 v150, v150\n\t"
    "v_mfma_f32_16x16x32_bf16 v[168:171], a[124:127], a[60:63], v[168:171]\n\t"
    "ds_read_b128 a[148:151], v5 offset:8448\n\t"
    "v_exp_f32 v151, v151\n\t"
    "v_mfma_f32_16x16x32_bf16 v[172:175], a[96:99], a[64:67], 0\n\t"
    "v_cvt_pk_bf16_f32 v149, v150, v151\n\t"
    "v_fma_f32 v152, v152, s34, v22\n\t"
    "v_mfma_f32_16x16x32_bf16 v[172:175], a[100:103], a[68:71], v[172:175]\n\t"
    "v_exp_f32 v152, v152\n\t"
    "v_mfma_f32_16x16x32_bf16 v[172:175], a[104:107], a[72:75], v[172:175]\n\t"
    "ds_read_b128 a[152:155], v6 offset:8448\n\t"
    "v_fma_f32 v153, v153, s34, v22\n\t"
    "v_mfma_f32_16x16x32_bf16 v[172:175], a[108:111], a[76:79], v[172:175]\n\t"
    "v_exp_f32 v153, v153\n\t"
    "v_mfma_f32_16x16x32_bf16 v[172:175], a[112:115], a[80:83], v[172:175]\n\t"
    "v_fma_f32 v154, v154, s34, v22\n\t"
    "v_fma_f32 v155, v155, s34, v22\n\t"
    ".p2align 12\n\t"
    "Lstub4_%=:\n\t"
    "s_cbranch_execz Lret_%=\n\t"
    "v_mfma_f32_16x16x32_bf16 v[172:175], a[116:119], a[84:87], v[172:175]\n\t"
    "ds_read_b128 a[156:159], v7 offset:8448\n\t"
    "v_cvt_pk_bf16_f32 v70, v152, v153\n\t"
    "v_mfma_f32_16x16x32_bf16 v[172:175], a[120:123], a[88:91], v[172:175]\n\t"
    "v_exp_f32 v154, v154\n\t"
    "v_mfma_f32_16x16x32_bf16 v[172:175], a[124:127], a[92:95], v[172:175]\n\t"
    "s_waitcnt lgkmcnt(7)\n\t"
    "v_exp_f32 v155, v155\n\t"
    "v_fma_f32 v128, v128, s34, v23\n\t"
    "v_mfma_f32_16x16x32_bf16 v[176:179], a[128:131], a[0:3], 0\n\t"
    "s_waitcnt lgkmcnt(6)\n\t"
    "ds_read_b128 a[96:99], v4 offset:16384\n\t"
    "v_cvt_pk_bf16_f32 v71, v154, v155\n\t"
    "v_mfma_f32_16x16x32_bf16 v[176:179], a[132:135], a[4:7], v[176:179]\n\t"
    "s_waitcnt lgkmcnt(6)\n\t"
    "v_exp_f32 v128, v128\n\t"
    "v_mfma_f32_16x16x32_bf16 v[176:179], a[136:139], a[8:11], v[176:179]\n\t"
    "s_waitcnt lgkmcnt(5)\n\t"
    "v_fma_f32 v129, v129, s34, v23\n\t"
    "v_fma_f32 v130, v130, s34, v23\n\t"
    "v_mfma_f32_16x16x32_bf16 v[176:179], a[140:143], a[12:15], v[176:179]\n\t"
    "s_waitcnt lgkmcnt(4)\n\t"
    "ds_read_b128 a[100:103], v5 offset:16384\n\t"
    "v_fma_f32 v131, v131, s34, v23\n\t"
    "v_mfma_f32_16x16x32_bf16 v[176:179], a[144:147], a[16:19], v[176:179]\n\t"
    "s_waitcnt lgkmcnt(4)\n\t"
    "v_exp_f32 v129, v129\n\t"
    "v_mfma_f32_16x16x32_bf16 v[176:179], a[148:151], a[20:23], v[176:179]\n\t"
    "s_waitcnt lgkmcnt(3)\n\t"
    "v_exp_f32 v130, v130\n\t"
    "v_mfma_f32_16x16x32_bf16 v[176:179], a[152:155], a[24:27], v[176:179]\n\t"
    "s_waitcnt lgkmcnt(2)\n\t"
    "ds_read_b128 a[104:107], v6 offset:16384\n\t"
    "v_cvt_pk_bf16_f32 v146, v128, v129\n\t"
    "v_mfma_f32_16x16x32_bf16 v[176:179], a[156:159], a[28:31], v[176:179]\n\t"
    "buffer_load_dwordx4 v19, s[24:27], s47 offen offset:3072 lds\n\t"
    "v_mfma_f32_16x16x32_bf16 a[164:167], a[160:163], v[68:71], a[164:167]\n\t"
    "v_exp_f32 v131, v131\n\t"
    "v_mfma_f32_16x16x32_bf16 v[152:155], a[128:131], a[32:35], 0\n\t"
    "v_cvt_pk_bf16_f32 v147, v130, v131\n\t"
    "v_fma_f32 v104, v104, s34, v24\n\t"
    "v_mfma_f32_16x16x32_bf16 v[152:155], a[132:135], a[36:39], v[152:155]\n\t"
    "ds_read_b128 a[108:111], v7 offset:16384\n\t"
    "v_fma_f32 v105, v105, s34, v24\n\t"
    "v_mfma_f32_16x16x32_bf16 v[152:155], a[136:139], a[40:43], v[152:155]\n\t"
    "v_exp_f32 v104, v104\n\t"
    "v_mfma_f32_16x16x32_bf16 v[152:155], a[140:143], a[44:47], v[152:155]\n\t"
    "v_exp_f32 v105, v105\n\t"
    "v_mfma_f32_16x16x32_bf16 v[152:155], a[144:147], a[48:51], v[152:155]\n\t"
    "ds_read_b128 a[112:115], v4 offset:16640\n\t"
    "v_fma_f32 v106, v106, s34, v24\n\t"
    "v_mfma_f32_16x16x32_bf16 v[152:155], a[148:151], a[52:55], v[152:155]\n\t"
    "v_exp_f32 v106, v106\n\t"
    "v_mfma_f32_16x16x32_bf16 v[152:155], a[152:155], a[56:59], v[152:155]\n\t"
    "v_fma_f32 v107, v107, s34, v24\n\t"
    "v_cvt_pk_bf16_f32 v150, v104, v105\n\t"
    "v_mfma_f32_16x16x32_bf16 v[152:155], a[156:159], a[60:63], v[152:155]\n\t"
    "ds_read_b128 a[116:119], v5 offset:16640\n\t"
    "v_exp_f32 v107, v107\n\t"
    "v_mfma_f32_16x16x32_bf16 a[168:171], a[160:163], v[144:147], a[168:171]\n\t"
    "v_cvt_pk_bf16_f32 v151, v106, v107\n\t"
    "v_fma_f32 v44, v44, s34, v22\n\t"
    "v_mfma_f32_16x16x32_bf16 v[128:131], a[128:131], a[64:67], 0\n\t"
    "v_exp_f32 v44, v44\n\t"
    "v_mfma_f32_16x16x32_bf16 v[128:131], a[132:135], a[68:71], v[128:131]\n\t"
    "v_fma_f32 v45, v45, s34, v22\n\t"
    "v_fma_f32 v46, v46, s34, v22\n\t"
    "v_mfma_f32_16x16x32_bf16 v[128:131], a[136:139], a[72:75], v[128:131]\n\t"
    "ds_read_b128 a[120:123], v6 offset:16640\n\t"
    "v_fma_f32 v47, v47, s34, v22\n\t"
    "v_mfma_f32_16x16x32_bf16 v[128:131], a[140:143], a[76:79], v[128:131]\n\t"
    "v_exp_f32 v45, v45\n\t"
    "v_mfma_f32_16x16x32_bf16 v[128:131], a[144:147], a[80:83], v[128:131]\n\t"
    "v_exp_f32 v46, v46\n\t"
    "v_mfma_f32_16x16x32_bf16 v[128:131], a[148:151], a[84:87], v[128:131]\n\t"
    "ds_read_b128 a[124:127], v7 offset:16640\n\t"
    "v_cvt_pk_bf16_f32 v44, v44, v45\n\t"
    "v_mfma_f32_16x16x32_bf16 v[128:131], a[152:155], a[88:91], v[128:131]\n\t"
    "v_exp_f32 v47, v47\n\t"
    "v_mfma_f32_16x16x32_bf16 v[128:131], a[156:159], a[92:95], v[128:131]\n\t"
    "s_waitcnt lgkmcnt(7)\n\t"
    "v_cvt_pk_bf16_f32 v45, v46, v47\n\t"
    "v_fma_f32 v156, v156, s34, v23\n\t"
    "v_fma_f32 v157, v157, s34, v23\n\t"
    "v_mfma_f32_16x16x32_bf16 a[172:175], a[160:163], v[148:151], a[172:175]\n\t"
    "v_exp_f32 v156, v156\n\t"
    "v_mfma_f32_16x16x32_bf16 v[104:107], a[96:99], a[0:3], 0\n\t"
    "s_waitcnt lgkmcnt(6)\n\t"
    "ds_read_b128 a[128:131], v4 offset:24576\n\t"
    "v_fma_f32 v158, v158, s34, v23\n\t"
    "v_mfma_f32_16x16x32_bf16 v[104:107], a[100:103], a[4:7], v[104:107]\n\t"
    "s_waitcnt lgkmcnt(6)\n\t"
    "v_exp_f32 v157, v157\n\t"
    "v_mfma_f32_16x16x32_bf16 v[104:107], a[104:107], a[8:11], v[104:107]\n\t"
    "s_waitcnt lgkmcnt(5)\n\t"
    "v_exp_f32 v158, v158\n\t"
    "v_mfma_f32_16x16x32_bf16 v[104:107], a[108:111], a[12:15], v[104:107]\n\t"
    "s_waitcnt lgkmcnt(4)\n\t"
    "ds_read_b128 a[132:135], v5 offset:24576\n\t"
    "v_fma_f32 v159, v159, s34, v23\n\t"
    "v_mfma_f32_16x16x32_bf16 v[104:107], a[112:115], a[16:19], v[104:107]\n\t"
    "s_waitcnt lgkmcnt(4)\n\t"
    "v_exp_f32 v159, v159\n\t"
    "v_mfma_f32_16x16x32_bf16 v[104:107], a[116:119], a[20:23], v[104:107]\n\t"
    "s_waitcnt lgkmcnt(3)\n\t"
    "v_cvt_pk_bf16_f32 v156, v156, v157\n\t"
    "v_cvt_pk_bf16_f32 v157, v158, v159\n\t"
    "v_mfma_f32_16x16x32_bf16 v[104:107], a[120:123], a[24:27], v[104:107]\n\t"
    "s_waitcnt lgkmcnt(2)\n\t"
    "ds_read_b128 a[136:139], v6 offset:24576\n\t"
    "v_fma_f32 v160, v160, s34, v24\n\t"
    "v_mfma_f32_16x16x32_bf16 v[104:107], a[124:127], a[28:31], v[104:107]\n\t"
    "s_add_u32 m0, s40, 0\n\t"
    "v_exp_f32 v160, v160\n\t"
    "v_fma_f32 v161, v161, s34, v24\n\t"
    "v_mfma_f32_16x16x32_bf16 v[180:183], a[96:99], a[32:35], 0\n\t"
    "s_add_u32 s46, s43, 0\n\t"
    "buffer_load_dwordx4 v16, s[24:27], s46 offen offset:0 lds\n\t"
    "v_mfma_f32_16x16x32_bf16 v[180:183], a[100:103], a[36:39], v[180:183]\n\t"
    "ds_read_b128 a[140:143], v7 offset:24576\n\t"
    "v_fma_f32 v162, v162, s34, v24\n\t"
    "v_mfma_f32_16x16x32_bf16 v[180:183], a[104:107], a[40:43], v[180:183]\n\t"
    "v_exp_f32 v161, v161\n\t"
    "v_mfma_f32_16x16x32_bf16 v[180:183], a[108:111], a[44:47], v[180:183]\n\t"
    "v_exp_f32 v162, v162\n\t"
    "v_mfma_f32_16x16x32_bf16 v[180:183], a[112:115], a[48:51], v[180:183]\n\t"
    "ds_read_b128 a[144:147], v4 offset:24832\n\t"
    "v_fma_f32 v163, v163, s34, v24\n\t"
    "v_mfma_f32_16x16x32_bf16 v[180:183], a[116:119], a[52:55], v[180:183]\n\t"
    "v_exp_f32 v163, v163\n\t"
    "v_mfma_f32_16x16x32_bf16 v[180:183], a[120:123], a[56:59], v[180:183]\n\t"
    "v_cvt_pk_bf16_f32 v160, v160, v161\n\t"
    "v_cvt_pk_bf16_f32 v161, v162, v163\n\t"
    "v_mfma_f32_16x16x32_bf16 v[180:183], a[124:127], a[60:63], v[180:183]\n\t"
    "ds_read_b128 a[148:151], v5 offset:24832\n\t"
    "v_fma_f32 v164, v164, s34, v22\n\t"
    "v_fma_f32 v165, v165, s34, v22\n\t"
    "v_mfma_f32_16x16x32_bf16 v[184:187], a[96:99], a[64:67], 0\n\t"
    "buffer_load_dwordx4 v17, s[24:27], s46 offen offset:1024 lds\n\t"
    "v_mfma_f32_16x16x32_bf16 v[184:187], a[100:103], a[68:71], v[184:187]\n\t"
    "v_exp_f32 v164, v164\n\t"
    "v_mfma_f32_16x16x32_bf16 v[184:187], a[104:107], a[72:75], v[184:187]\n\t"
    "ds_read_b128 a[152:155], v6 offset:24832\n\t"
    "v_fma_f32 v166, v166, s34, v22\n\t"
    "v_mfma_f32_16x16x32_bf16 v[184:187], a[108:111], a[76:79], v[184:187]\n\t"
    "v_exp_f32 v165, v165\n\t"
    "v_mfma_f32_16x16x32_bf16 v[184:187], a[112:115], a[80:83], v[184:187]\n\t"
    "s_waitcnt vmcnt(10)\n\t"
    "s_barrier\n\t"
    "v_fma_f32 v167, v167, s34, v22\n\t"
    "v_mfma_f32_16x16x32_bf16 v[184:187], a[116:119], a[84:87], v[184:187]\n\t"
    "ds_read_b128 a[156:159], v7 offset:24832\n\t"
    "v_cvt_pk_bf16_f32 v46, v164, v165\n\t"
    "v_mfma_f32_16x16x32_bf16 v[184:187], a[120:123], a[88:91], v[184:187]\n\t"
    "v_exp_f32 v166, v166\n\t"
    "v_mfma_f32_16x16x32_bf16 v[184:187], a[124:127], a[92:95], v[184:187]\n\t"
    "s_waitcnt lgkmcnt(7)\n\t"
    "v_exp_f32 v167, v167\n\t"
    "v_fma_f32 v140, v140, s34, v23\n\t"
    "v_mfma_f32_16x16x32_bf16 v[188:191], a[128:131], a[0:3], 0\n\t"
    "s_waitcnt lgkmcnt(6)\n\t"
    "ds_read_b64_tr_b16 a[96:97], v8 offset:0\n\t"
    "ds_read_b64_tr_b16 a[98:99], v8 offset:2048\n\t"
    "v_mfma_f32_16x16x32_bf16 v[188:191], a[132:135], a[4:7], v[188:191]\n\t"
    "s_waitcnt lgkmcnt(7)\n\t"
    "v_exp_f32 v140, v140\n\t"
    "v_mfma_f32_16x16x32_bf16 v[188:191], a[136:139], a[8:11], v[188:191]\n\t"
    "s_waitcnt lgkmcnt(6)\n\t"
    "buffer_load_dwordx4 v18, s[24:27], s46 offen offset:2048 lds\n\t"
    "v_mfma_f32_16x16x32_bf16 v[188:191], a[140:143], a[12:15], v[188:191]\n\t"
    "s_waitcnt lgkmcnt(5)\n\t"
    "ds_read_b64_tr_b16 a[100:101], v8 offset:4096\n\t"
    "ds_read_b64_tr_b16 a[102:103], v8 offset:6144\n\t"
    "v_mfma_f32_16x16x32_bf16 v[188:191], a[144:147], a[16:19], v[188:191]\n\t"
    "s_waitcnt lgkmcnt(6)\n\t"
    "v_cvt_pk_bf16_f32 v47, v166, v167\n\t"
    "v_fma_f32 v141, v141, s34, v23\n\t"
    "v_mfma_f32_16x16x32_bf16 v[188:191], a[148:151], a[20:23], v[188:191]\n\t"
    "s_waitcnt lgkmcnt(5)\n\t"
    "v_exp_f32 v141, v141\n\t"
    "v_mfma_f32_16x16x32_bf16 v[188:191], a[152:155], a[24:27], v[188:191]\n\t"
    "s_waitcnt lgkmcnt(4)\n\t"
    "ds_read_b64_tr_b16 a[104:105], v8 offset:8192\n\t"
    "ds_read_b64_tr_b16 a[106:107], v8 offset:10240\n\t"
    "v_mfma_f32_16x16x32_bf16 v[188:191], a[156:159], a[28:31], v[188:191]\n\t"
    "v_fma_f32 v142, v142, s34, v23\n\t"
    "v_fma_f32 v143, v143, s34, v23\n\t"
    "v_cvt_pk_bf16_f32 v158, v140, v141\n\t"
    "v_mfma_f32_16x16x32_bf16 a[164:167], a[160:163], v[44:47], a[164:167]\n\t"
    "v_exp_f32 v142, v142\n\t"
    "v_mfma_f32_16x16x32_bf16 v[40:43], a[128:131], a[32:35], 0\n\t"
    "v_exp_f32 v143, v143\n\t"
    "v_mfma_f32_16x16x32_bf16 v[40:43], a[132:135], a[36:39], v[40:43]\n\t"
    "ds_read_b64_tr_b16 a[108:109], v8 offset:12288\n\t"
    "ds_read_b64_tr_b16 a[110:111], v8 offset:14336\n\t"
    "v_mfma_f32_16x16x32_bf16 v[40:43], a[136:139], a[40:43], v[40:43]\n\t"
    "v_cvt_pk_bf16_f32 v159, v142, v143\n\t"
    "v_fma_f32 v116, v116, s34, v24\n\t"
    "v_mfma_f32_16x16x32_bf16 v[40:43], a[140:143], a[44:47], v[40:43]\n\t"
    "v_exp_f32 v116, v116\n\t"
    "v_mfma_f32_16x16x32_bf16 v[40:43], a[144:147], a[48:51], v[40:43]\n\t"
    "ds_read_b64_tr_b16 a[112:113], v8 offset:16384\n\t"
    "ds_read_b64_tr_b16 a[114:115], v8 offset:18432\n\t"
    "v_mfma_f32_16x16x32_bf16 v[40:43], a[148:151], a[52:55], v[40:43]\n\t"
    "buffer_load_dwordx4 v19, s[24:27], s46 offen offset:3072 lds\n\t"
    "v_mfma_f32_16x16x32_bf16 v[40:43], a[152:155], a[56:59], v[40:43]\n\t"
    "v_fma_f32 v117, v117, s34, v24\n\t"
    "v_fma_f32 v118, v118, s34, v24\n\t"
    "v_mfma_f32_16x16x32_bf16 v[40:43], a[156:159], a[60:63], v[40:43]\n\t"
    "ds_read_b64_tr_b16 a[116:117], v8 offset:20480\n\t"
    "ds_read_b64_tr_b16 a[118:119], v8 offset:22528\n\t"
    "v_fma_f32 v119, v119, s34, v24\n\t"
    "v_mfma_f32_16x16x32_bf16 a[168:171], a[160:163], v[156:159], a[168:171]\n\t"
    "v_exp_f32 v117, v117\n\t"
    "v_mfma_f32_16x16x32_bf16 v[140:143], a[128:131], a[64:67], 0\n\t"
    "v_exp_f32 v118, v118\n\t"
    "v_mfma_f32_16x16x32_bf16 v[140:143], a[132:135], a[68:71], v[140:143]\n\t"
    "v_exp_f32 v119, v119\n\t"
    "v_mfma_f32_16x16x32_bf16 v[140:143], a[136:139], a[72:75], v[140:143]\n\t"
    "ds_read_b64_tr_b16 a[120:121], v8 offset:24576\n\t"
    "ds_read_b64_tr_b16 a[122:123], v8 offset:26624\n\t"
    "v_mfma_f32_16x16x32_bf16 v[140:143], a[140:143], a[76:79], v[140:143]\n\t"
    "v_cvt_pk_bf16_f32 v162, v116, v117\n\t"
    "v_cvt_pk_bf16_f32 v163, v118, v119\n\t"
    "v_mfma_f32_16x16x32_bf16 v[140:143], a[144:147], a[80:83], v[140:143]\n\t"
    "s_add_u32 m0, s40, 4096\n\t"
    "v_fma_f32 v64, v64, s34, v22\n\t"
    "v_fma_f32 v65, v65, s34, v22\n\t"
    "v_mfma_f32_16x16x32_bf16 v[140:143], a[148:151], a[84:87], v[140:143]\n\t"
    "s_add_u32 s47, s43, 16384\n\t"
    "ds_read_b64_tr_b16 a[124:125], v8 offset:28672\n\t"
    "ds_read_b64_tr_b16 a[126:127], v8 offset:30720\n\t"
    "v_mfma_f32_16x16x32_bf16 v[140:143], a[152:155], a[88:91], v[140:143]\n\t"
    "v_exp_f32 v64, v64\n\t"
    "v_mfma_f32_16x16x32_bf16 v[140:143], a[156:159], a[92:95], v[140:143]\n\t"
    "s_waitcnt lgkmcnt(14)\n\t"
    "buffer_load_dwordx4 v16, s[24:27], s47 offen offset:0 lds\n\t"
    "v_mfma_f32_16x16x32_bf16 a[172:175], a[160:163], v[160:163], a[172:175]\n\t"
    "v_exp_f32 v65, v65\n\t"
    "v_mfma_f32_16x16x32_bf16 v[116:119], a[96:99], a[176:179], 0\n\t"
    "s_waitcnt lgkmcnt(12)\n\t"
    "ds_read_b64_tr_b16 a[128:129], v9 offset:0\n\t"
    "ds_read_b64_tr_b16 a[130:131], v9 offset:2048\n\t"
    "v_mfma_f32_16x16x32_bf16 v[116:119], a[100:103], a[188:191], v[116:119]\n\t"
    "s_waitcnt lgkmcnt(12)\n\t"
    "v_fma_f32 v66, v66, s34, v22\n\t"
    "v_fma_f32 v67, v67, s34, v22\n\t"
    "v_mfma_f32_16x16x32_bf16 v[116:119], a[104:107], a[200:203], v[116:119]\n\t"
    "s_waitcnt lgkmcnt(10)\n\t"
    "v_exp_f32 v66, v66\n\t"
    "v_mfma_f32_16x16x32_bf16 v[116:119], a[108:111], a[212:215], v[116:119]\n\t"
    "s_waitcnt lgkmcnt(8)\n\t"
    "ds_read_b64_tr_b16 a[132:133], v9 offset:4096\n\t"
    "ds_read_b64_tr_b16 a[134:135], v9 offset:6144\n\t"
    "v_mfma_f32_16x16x32_bf16 v[116:119], a[112:115], v[228:231], v[116:119]\n\t"
    "s_waitcnt lgkmcnt(8)\n\t"
    "v_exp_f32 v67, v67\n\t"
    "v_mfma_f32_16x16x32_bf16 v[116:119], a[116:119], v[252:255], v[116:119]\n\t"
    "s_waitcnt lgkmcnt(6)\n\t"
    "v_cvt_pk_bf16_f32 v64, v64, v65\n\t"
    "v_cvt_pk_bf16_f32 v65, v66, v67\n\t"
    "v_mfma_f32_16x16x32_bf16 v[116:119], a[120:123], v[80:83], v[116:119]\n\t"
    "s_waitcnt lgkmcnt(4)\n\t"
    "ds_read_b64_tr_b16 a[136:137], v9 offset:8192\n\t"
    "ds_read_b64_tr_b16 a[138:139], v9 offset:10240\n\t"
    "v_mfma_f32_16x16x32_bf16 v[116:119], a[124:127], v[52:55], v[116:119]\n\t"
    "v_fma_f32 v168, v168, s34, v23\n\t"
    "v_fma_f32 v169, v169, s34, v23\n\t"
    "v_mfma_f32_16x16x32_bf16 v[164:167], a[96:99], a[180:183], 0\n\t"
    "v_exp_f32 v168, v168\n\t"
    "v_mfma_f32_16x16x32_bf16 v[164:167], a[100:103], a[192:195], v[164:167]\n\t"
    "ds_read_b64_tr_b16 a[140:141], v9 offset:12288\n\t"
    "ds_read_b64_tr_b16 a[142:143], v9 offset:14336\n\t"
    "v_mfma_f32_16x16x32_bf16 v[164:167], a[104:107], a[204:207], v[164:167]\n\t"
    "buffer_load_dwordx4 v17, s[24:27], s47 offen offset:1024 lds\n\t"
    "v_mfma_f32_16x16x32_bf16 v[164:167], a[108:111], a[216:219], v[164:167]\n\t"
    "v_exp_f32 v169, v169\n\t"
    "v_mfma_f32_16x16x32_bf16 v[164:167], a[112:115], v[204:207], v[164:167]\n\t"
    "ds_read_b64_tr_b16 a[144:145], v9 offset:16384\n\t"
    "ds_read_b64_tr_b16 a[146:147], v9 offset:18432\n\t"
    "v_mfma_f32_16x16x32_bf16 v[164:167], a[116:119], v[208:211], v[164:167]\n\t"
    "v_fma_f32 v170, v170, s34, v23\n\t"
    "v_fma_f32 v171, v171, s34, v23\n\t"
    "v_mfma_f32_16x16x32_bf16 v[164:167], a[120:123], v[36:39], v[164:167]\n\t"
    "v_exp_f32 v170, v170\n\t"
    "v_mfma_f32_16x16x32_bf16 v[164:167], a[124:127], v[56:59], v[164:167]\n\t"
    "ds_read_b64_tr_b16 a[148:149], v9 offset:20480\n\t"
    "ds_read_b64_tr_b16 a[150:151], v9 offset:22528\n\t"
    "v_mfma_f32_16x16x32_bf16 v[192:195], a[96:99], a[184:187], 0\n\t"
    "v_exp_f32 v171, v171\n\t"
    "v_mfma_f32_16x16x32_bf16 v[192:195], a[100:103], a[196:199], v[192:195]\n\t"
    "v_cvt_pk_bf16_f32 v168, v168, v169\n\t"
    "v_cvt_pk_bf16_f32 v169, v170, v171\n\t"
    "v_mfma_f32_16x16x32_bf16 v[192:195], a[104:107], a[208:211], v[192:195]\n\t"
    "ds_read_b64_tr_b16 a[152:153], v9 offset:24576\n\t"
    "ds_read_b64_tr_b16 a[154:155], v9 offset:26624\n\t"
    "v_mfma_f32_16x16x32_bf16 v[192:195], a[108:111], a[220:223], v[192:195]\n\t"
    "buffer_load_dwordx4 v18, s[24:27], s47 offen offset:2048 lds\n\t"
    "v_mfma_f32_16x16x32_bf16 v[192:195], a[112:115], v[32:35], v[192:195]\n\t"
    "v_fma_f32 v172, v172, s34, v24\n\t"
    "v_fma_f32 v173, v173, s34, v24\n\t"
    "v_mfma_f32_16x16x32_bf16 v[192:195], a[116:119], v[72:75], v[192:195]\n\t"
    "ds_read_b64_tr_b16 a[156:157], v9 offset:28672\n\t"
    "ds_read_b64_tr_b16 a[158:159], v9 offset:30720\n\t"
    "v_mfma_f32_16x16x32_bf16 v[192:195], a[120:123], v[84:87], v[192:195]\n\t"
    "v_exp_f32 v172, v172\n\t"
    "v_mfma_f32_16x16x32_bf16 v[192:195], a[124:127], v[60:63], v[192:195]\n\t"
    "s_waitcnt lgkmcnt(14)\n\t"
    "v_exp_f32 v173, v173\n\t"
    "v_mfma_f32_16x16x32_bf16 v[196:199], a[128:131], a[176:179], 0\n\t"
    "s_waitcnt lgkmcnt(12)\n\t"
    "ds_read_b64_tr_b16 a[96:97], v10 offset:0\n\t"
    "ds_read_b64_tr_b16 a[98:99], v10 offset:2048\n\t"
    "v_mfma_f32_16x16x32_bf16 v[196:199], a[132:135], a[188:191], v[196:199]\n\t"
    "s_waitcnt lgkmcnt(12)\n\t"
    "v_fma_f32 v174, v174, s34, v24\n\t"
    "v_fma_f32 v175, v175, s34, v24\n\t"
    "v_mfma_f32_16x16x32_bf16 v[196:199], a[136:139], a[200:203], v[196:199]\n\t"
    "s_waitcnt lgkmcnt(10)\n\t"
    "v_exp_f32 v174, v174\n\t"
    "v_mfma_f32_16x16x32_bf16 v[196:199], a[140:143], a[212:215], v[196:199]\n\t"
    "s_waitcnt lgkmcnt(8)\n\t"
    "ds_read_b64_tr_b16 a[100:101], v10 offset:4096\n\t"
    "ds_read_b64_tr_b16 a[102:103], v10 offset:6144\n\t"
    "v_mfma_f32_16x16x32_bf16 v[196:199], a[144:147], v[228:231], v[196:199]\n\t"
    "s_waitcnt lgkmcnt(8)\n\t"
    "v_exp_f32 v175, v175\n\t"
    "v_mfma_f32_16x16x32_bf16 v[196:199], a[148:151], v[252:255], v[196:199]\n\t"
    "s_waitcnt lgkmcnt(6)\n\t"
    "buffer_load_dwordx4 v19, s[24:27], s47 offen offset:3072 lds\n\t"
    "v_mfma_f32_16x16x32_bf16 v[196:199], a[152:155], v[80:83], v[196:199]\n\t"
    "s_waitcnt lgkmcnt(4)\n\t"
    "ds_read_b64_tr_b16 a[104:105], v10 offset:8192\n\t"
    "ds_read_b64_tr_b16 a[106:107], v10 offset:10240\n\t"
    "v_mfma_f32_16x16x32_bf16 v[196:199], a[156:159], v[52:55], v[196:199]\n\t"
    "v_cvt_pk_bf16_f32 v172, v172, v173\n\t"
    "v_cvt_pk_bf16_f32 v173, v174, v175\n\t"
    "v_mfma_f32_16x16x32_bf16 v[200:203], a[128:131], a[180:183], 0\n\t"
    "v_fma_f32 v176, v176, s34, v22\n\t"
    "v_fma_f32 v177, v177, s34, v22\n\t"
    "v_mfma_f32_16x16x32_bf16 v[200:203], a[132:135], a[192:195], v[200:203]\n\t"
    "ds_read_b64_tr_b16 a[108:109], v10 offset:12288\n\t"
    "ds_read_b64_tr_b16 a[110:111], v10 offset:14336\n\t"
    "v_mfma_f32_16x16x32_bf16 v[200:203], a[136:139], a[204:207], v[200:203]\n\t"
    "v_exp_f32 v176, v176\n\t"
    "v_mfma_f32_16x16x32_bf16 v[200:203], a[140:143], a[216:219], v[200:203]\n\t"
    "v_exp_f32 v177, v177\n\t"
    "v_mfma_f32_16x16x32_bf16 v[200:203], a[144:147], v[204:207], v[200:203]\n\t"
    "ds_read_b64_tr_b16 a[112:113], v10 offset:16384\n\t"
    "ds_read_b64_tr_b16 a[114:115], v10 offset:18432\n\t"
    "v_mfma_f32_16x16x32_bf16 v[200:203], a[148:151], v[208:211], v[200:203]\n\t"
    "v_fma_f32 v178, v178, s34, v22\n\t"
    "v_fma_f32 v179, v179, s34, v22\n\t"
    "v_mfma_f32_16x16x32_bf16 v[200:203], a[152:155], v[36:39], v[200:203]\n\t"
    "v_exp_f32 v178, v178\n\t"
    "v_mfma_f32_16x16x32_bf16 v[200:203], a[156:159], v[56:59], v[200:203]\n\t"
    "s_add_u32 m0, s40, 32768\n\t"
    "ds_read_b64_tr_b16 a[116:117], v10 offset:20480\n\t"
    "ds_read_b64_tr_b16 a[118:119], v10 offset:22528\n\t"
    "v_mfma_f32_16x16x32_bf16 v[212:215], a[128:131], a[184:187], 0\n\t"
    "s_add_u32 s46, s43, 131072\n\t"
    "v_exp_f32 v179, v179\n\t"
    "v_mfma_f32_16x16x32_bf16 v[212:215], a[132:135], a[196:199], v[212:215]\n\t"
    "v_cvt_pk_bf16_f32 v66, v176, v177\n\t"
    "v_cvt_pk_bf16_f32 v67, v178, v179\n\t"
    "v_mfma_f32_16x16x32_bf16 v[212:215], a[136:139], a[208:211], v[212:215]\n\t"
    "ds_read_b64_tr_b16 a[120:121], v10 offset:24576\n\t"
    "ds_read_b64_tr_b16 a[122:123], v10 offset:26624\n\t"
    "v_mfma_f32_16x16x32_bf16 v[212:215], a[140:143], a[220:223], v[212:215]\n\t"
    "buffer_load_dwordx4 v16, s[24:27], s46 offen offset:0 lds\n\t"
    "v_mfma_f32_16x16x32_bf16 v[212:215], a[144:147], v[32:35], v[212:215]\n\t"
    "v_fma_f32 v152, v152, s34, v23\n\t"
    "v_fma_f32 v153, v153, s34, v23\n\t"
    "v_mfma_f32_16x16x32_bf16 v[212:215], a[148:151], v[72:75], v[212:215]\n\t"
    "ds_read_b64_tr_b16 a[124:125], v10 offset:28672\n\t"
    "ds_read_b64_tr_b16 a[126:127], v10 offset:30720\n\t"
    "v_mfma_f32_16x16x32_bf16 v[212:215], a[152:155], v[84:87], v[212:215]\n\t"
    "v_exp_f32 v152, v152\n\t"
    "v_mfma_f32_16x16x32_bf16 v[212:215], a[156:159], v[60:63], v[212:215]\n\t"
    "s_waitcnt lgkmcnt(14)\n\t"
    "v_exp_f32 v153, v153\n\t"
    "v_mfma_f32_16x16x32_bf16 a[164:167], a[160:163], v[64:67], a[164:167]\n\t"
    "v_fma_f32 v154, v154, s34, v23\n\t"
    "v_fma_f32 v155, v155, s34, v23\n\t"
    "v_mfma_f32_16x16x32_bf16 v[176:179], a[96:99], a[176:179], 0\n\t"
    "s_waitcnt lgkmcnt(12)\n\t"
    "ds_read_b64_tr_b16 a[128:129], v11 offset:0\n\t"
    "ds_read_b64_tr_b16 a[130:131], v11 offset:2048\n\t"
    "v_mfma_f32_16x16x32_bf16 v[176:179], a[100:103], a[188:191], v[176:179]\n\t"
    "s_waitcnt lgkmcnt(12)\n\t"
    "v_exp_f32 v154, v154\n\t"
    "v_mfma_f32_16x16x32_bf16 v[176:179], a[104:107], a[200:203], v[176:179]\n\t"
    "s_waitcnt lgkmcnt(10)\n\t"
    "buffer_load_dwordx4 v17, s[24:27], s46 offen offset:1024 lds\n\t"
    "v_mfma_f32_16x16x32_bf16 v[176:179], a[108:111], a[212:215], v[176:179]\n\t"
    "s_waitcnt lgkmcnt(8)\n\t"
    "ds_read_b64_tr_b16 a[132:133], v11 offset:4096\n\t"
    "ds_read_b64_tr_b16 a[134:135], v11 offset:6144\n\t"
    "v_mfma_f32_16x16x32_bf16 v[176:179], a[112:115], v[228:231], v[176:179]\n\t"
    "s_waitcnt lgkmcnt(8)\n\t"
    "v_exp_f32 v155, v155\n\t"
    "v_mfma_f32_16x16x32_bf16 v[176:179], a[116:119], v[252:255], v[176:179]\n\t"
    "s_waitcnt lgkmcnt(6)\n\t"
    "v_cvt_pk_bf16_f32 v170, v152, v153\n\t"
    "v_cvt_pk_bf16_f32 v171, v154, v155\n\t"
    "v_mfma_f32_16x16x32_bf16 v[176:179], a[120:123], v[80:83], v[176:179]\n\t"
    "s_waitcnt lgkmcnt(4)\n\t"
    "ds_read_b64_tr_b16 a[136:137], v11 offset:8192\n\t"
    "ds_read_b64_tr_b16 a[138:139], v11 offset:10240\n\t"
    "v_mfma_f32_16x16x32_bf16 v[176:179], a[124:127], v[52:55], v[176:179]\n\t"
    "v_fma_f32 v128, v128, s34, v24\n\t"
    "v_fma_f32 v129, v129, s34, v24\n\t"
    "v_mfma_f32_16x16x32_bf16 a[168:171], a[160:163], v[168:171], a[168:171]\n\t"
    "v_exp_f32 v128, v128\n\t"
    "v_mfma_f32_16x16x32_bf16 v[152:155], a[96:99], a[180:183], 0\n\t"
    "v_exp_f32 v129, v129\n\t"
    "v_mfma_f32_16x16x32_bf16 v[152:155], a[100:103], a[192:195], v[152:155]\n\t"
    "ds_read_b64_tr_b16 a[140:141], v11 offset:12288\n\t"
    "ds_read_b64_tr_b16 a[142:143], v11 offset:14336\n\t"
    "v_mfma_f32_16x16x32_bf16 v[152:155], a[104:107], a[204:207], v[152:155]\n\t"
    "v_fma_f32 v130, v130, s34, v24\n\t"
    "v_fma_f32 v131, v131, s34, v24\n\t"
    "v_mfma_f32_16x16x32_bf16 v[152:155], a[108:111], a[216:219], v[152:155]\n\t"
    "buffer_load_dwordx4 v18, s[24:27], s46 offen offset:2048 lds\n\t"
    "v_mfma_f32_16x16x32_bf16 v[152:155], a[112:115], v[204:207], v[152:155]\n\t"
    "ds_read_b64_tr_b16 a[144:145], v11 offset:16384\n\t"
    "ds_read_b64_tr_b16 a[146:147], v11 offset:18432\n\t"
    "v_mfma_f32_16x16x32_bf16 v[152:155], a[116:119], v[208:211], v[152:155]\n\t"
    "v_exp_f32 v130, v130\n\t"
    "v_mfma_f32_16x16x32_bf16 v[152:155], a[120:123], v[36:39], v[152:155]\n\t"
    "v_exp_f32 v131, v131\n\t"
    "v_mfma_f32_16x16x32_bf16 v[152:155], a[124:127], v[56:59], v[152:155]\n\t"
    "ds_read_b64_tr_b16 a[148:149], v11 offset:20480\n\t"
    "ds_read_b64_tr_b16 a[150:151], v11 offset:22528\n\t"
    "v_mfma_f32_16x16x32_bf16 v[216:219], a[96:99], a[184:187], 0\n\t"
    "v_cvt_pk_bf16_f32 v174, v128, v129\n\t"
    "v_cvt_pk_bf16_f32 v175, v130, v131\n\t"
    "v_mfma_f32_16x16x32_bf16 v[216:219], a[100:103], a[196:199], v[216:219]\n\t"
    "v_fma_f32 v104, v104, s34, v22\n\t"
    "v_fma_f32 v105, v105, s34, v22\n\t"
    "v_mfma_f32_16x16x32_bf16 v[216:219], a[104:107], a[208:211], v[216:219]\n\t"
    "ds_read_b64_tr_b16 a[152:153], v11 offset:24576\n\t"
    "ds_read_b64_tr_b16 a[154:155], v11 offset:26624\n\t"
    "v_mfma_f32_16x16x32_bf16 v[216:219], a[108:111], a[220:223], v[216:219]\n\t"
    "buffer_load_dwordx4 v19, s[24:27], s46 offen offset:3072 lds\n\t"
    "v_mfma_f32_16x16x32_bf16 v[216:219], a[112:115], v[32:35], v[216:219]\n\t"
    "s_waitcnt vmcnt(12)\n\t"
    "s_barrier\n\t"
    "v_fma_f32 v106, v106, s34, v22\n\t"
    "v_mfma_f32_16x16x32_bf16 v[216:219], a[116:119], v[72:75], v[216:219]\n\t"
    "ds_read_b64_tr_b16 a[156:157], v11 offset:28672\n\t"
    "ds_read_b64_tr_b16 a[158:159], v11 offset:30720\n\t"
    "v_mfma_f32_16x16x32_bf16 v[216:219], a[120:123], v[84:87], v[216:219]\n\t"
    "v_exp_f32 v104, v104\n\t"
    "v_mfma_f32_16x16x32_bf16 v[216:219], a[124:127], v[60:63], v[216:219]\n\t"
    "s_waitcnt lgkmcnt(14)\n\t"
    "v_exp_f32 v105, v105\n\t"
    "v_mfma_f32_16x16x32_bf16 a[172:175], a[160:163], v[172:175], a[172:175]\n\t"
    "v_exp_f32 v106, v106\n\t"
    "v_mfma_f32_16x16x32_bf16 v[128:131], a[128:131], a[176:179], 0\n\t"
    "s_waitcnt lgkmcnt(12)\n\t"
    "v_add_u32 v4, 0x8000, v8\n\t"
    "v_add_u32 v5, 0x8000, v9\n\t"
    "v_add_u32 v6, 0x8000, v10\n\t"
    "v_add_u32 v7, 0x8000, v11\n\t"
    "v_mfma_f32_16x16x32_bf16 v[128:131], a[132:135], a[188:191], v[128:131]\n\t"
    "s_waitcnt lgkmcnt(10)\n\t"
    "ds_read_b64_tr_b16 a[96:97], v4 offset:32768\n\t"
    "ds_read_b64_tr_b16 a[98:99], v4 offset:34816\n\t"
    "v_mfma_f32_16x16x32_bf16 v[128:131], a[136:139], a[200:203], v[128:131]\n\t"
    "s_waitcnt lgkmcnt(10)\n\t"
    "v_fma_f32 v107, v107, s34, v22\n\t"
    "v_cvt_pk_bf16_f32 v104, v104, v105\n\t"
    "v_mfma_f32_16x16x32_bf16 v[128:131], a[140:143], a[212:215], v[128:131]\n\t"
    "s_waitcnt lgkmcnt(8)\n\t"
    "ds_read_b64_tr_b16 a[100:101], v4 offset:36864\n\t"
    "ds_read_b64_tr_b16 a[102:103], v4 offset:38912\n\t"
    "v_mfma_f32_16x16x32_bf16 v[128:131], a[144:147], v[228:231], v[128:131]\n\t"
    "s_waitcnt lgkmcnt(8)\n\t"
    "v_exp_f32 v107, v107\n\t"
    "v_mfma_f32_16x16x32_bf16 v[128:131], a[148:151], v[252:255], v[128:131]\n\t"
    "s_waitcnt lgkmcnt(6)\n\t"
    "v_cvt_pk_bf16_f32 v105, v106, v107\n\t"
    "v_fma_f32 v180, v180, s34, v23\n\t"
    "v_mfma_f32_16x16x32_bf16 v[128:131], a[152:155], v[80:83], v[128:131]\n\t"
    "s_waitcnt lgkmcnt(4)\n\t"
    "ds_read_b64_tr_b16 a[104:105], v4 offset:40960\n\t"
    "ds_read_b64_tr_b16 a[106:107], v4 offset:43008\n\t"
    "v_mfma_f32_16x16x32_bf16 v[128:131], a[156:159], v[52:55], v[128:131]\n\t"
    "s_add_u32 m0, s40, 36864\n\t"
    "s_add_u32 s47, s43, 147456\n\t"
    "buffer_load_dwordx4 v16, s[24:27], s47 offen offset:0 lds\n\t"
    "v_mfma_f32_16x16x32_bf16 v[220:223], a[128:131], a[180:183], 0\n\t"
    "v_exp_f32 v180, v180\n\t"
    "v_mfma_f32_16x16x32_bf16 v[220:223], a[132:135], a[192:195], v[220:223]\n\t"
    "ds_read_b64_tr_b16 a[108:109], v4 offset:45056\n\t"
    "ds_read_b64_tr_b16 a[110:111], v4 offset:47104\n\t"
    "v_mfma_f32_16x16x32_bf16 v[220:223], a[136:139], a[204:207], v[220:223]\n\t"
    "v_fma_f32 v181, v181, s34, v23\n\t"
    "v_fma_f32 v182, v182, s34, v23\n\t"
    "v_mfma_f32_16x16x32_bf16 v[220:223], a[140:143], a[216:219], v[220:223]\n\t"
    "v_exp_f32 v181, v181\n\t"
    "v_mfma_f32_16x16x32_bf16 v[220:223], a[144:147], v[204:207], v[220:223]\n\t"
    "ds_read_b64_tr_b16 a[112:113], v4 offset:49152\n\t"
    "ds_read_b64_tr_b16 a[114:115], v4 offset:51200\n\t"
    "v_mfma_f32_16x16x32_bf16 v[220:223], a[148:151], v[208:211], v[220:223]\n\t"
    "buffer_load_dwordx4 v17, s[24:27], s47 offen offset:1024 lds\n\t"
    "v_mfma_f32_16x16x32_bf16 v[220:223], a[152:155], v[36:39], v[220:223]\n\t"
    "v_exp_f32 v182, v182\n\t"
    "v_mfma_f32_16x16x32_bf16 v[220:223], a[156:159], v[56:59], v[220:223]\n\t"
    "ds_read_b64_tr_b16 a[116:117], v4 offset:53248\n\t"
    "ds_read_b64_tr_b16 a[118:119], v4 offset:55296\n\t"
    "v_mfma_f32_16x16x32_bf16 v[224:227], a[128:131], a[184:187], 0\n\t"
    "v_fma_f32 v183, v183, s34, v23\n\t"
    "v_cvt_pk_bf16_f32 v180, v180, v181\n\t"
    "v_mfma_f32_16x16x32_bf16 v[224:227], a[132:135], a[196:199], v[224:227]\n\t"
    "v_exp_f32 v183, v183\n\t"
    "v_mfma_f32_16x16x32_bf16 v[224:227], a[136:139], a[208:211], v[224:227]\n\t"
    "ds_read_b64_tr_b16 a[120:121], v4 offset:57344\n\t"
    "ds_read_b64_tr_b16 a[122:123], v4 offset:59392\n\t"
    "v_mfma_f32_16x16x32_bf16 v[224:227], a[140:143], a[220:223], v[224:227]\n\t"
    "v_cvt_pk_bf16_f32 v181, v182, v183\n\t"
    "v_fma_f32 v184, v184, s34, v24\n\t"
    "v_mfma_f32_16x16x32_bf16 v[224:227], a[144:147], v[32:35], v[224:227]\n\t"
    "v_exp_f32 v184, v184\n\t"
    "v_mfma_f32_16x16x32_bf16 v[224:227], a[148:151], v[72:75], v[224:227]\n\t"
    "ds_read_b64_tr_b16 a[124:125], v4 offset:61440\n\t"
    "ds_read_b64_tr_b16 a[126:127], v4 offset:63488\n\t"
    "v_mfma_f32_16x16x32_bf16 v[224:227], a[152:155], v[84:87], v[224:227]\n\t"
    "buffer_load_dwordx4 v18, s[24:27], s47 offen offset:2048 lds\n\t"
    "v_mfma_f32_16x16x32_bf16 v[224:227], a[156:159], v[60:63], v[224:227]\n\t"
    "s_waitcnt lgkmcnt(14)\n\t"
    "v_fma_f32 v185, v185, s34, v24\n\t"
    "v_fma_f32 v186, v186, s34, v24\n\t"
    "v_mfma_f32_16x16x32_bf16 v[116:119], a[96:99], v[76:79], v[116:119]\n\t"
    "s_waitcnt lgkmcnt(12)\n\t"
    "ds_read_b64_tr_b16 a[128:129], v5 offset:32768\n\t"
    "v_fma_f32 v187, v187, s34, v24\n\t"
    "v_mfma_f32_16x16x32_bf16 v[116:119], a[100:103], v[48:51], v[116:119]\n\t"
    "s_waitcnt lgkmcnt(11)\n\t"
    "ds_read_b64_tr_b16 a[130:131], v5 offset:34816\n\t"
    "v_fma_f32 v188, v188, s34, v22\n\t"
    "v_mfma_f32_16x16x32_bf16 v[116:119], a[104:107], v[92:95], v[116:119]\n\t"
    "s_waitcnt lgkmcnt(10)\n\t"
    "v_exp_f32 v185, v185\n\t"
    "v_mfma_f32_16x16x32_bf16 v[116:119], a[108:111], v[88:91], v[116:119]\n\t"
    "s_waitcnt lgkmcnt(8)\n\t"
    "ds_read_b64_tr_b16 a[132:133], v5 offset:36864\n\t"
    "v_cvt_pk_bf16_f32 v184, v184, v185\n\t"
    "v_mfma_f32_16x16x32_bf16 v[116:119], a[112:115], v[68:71], v[116:119]\n\t"
    "s_waitcnt lgkmcnt(7)\n\t"
    "ds_read_b64_tr_b16 a[134:135], v5 offset:38912\n\t"
    "v_fma_f32 v189, v189, s34, v22\n\t"
    "v_mfma_f32_16x16x32_bf16 v[116:119], a[116:119], v[44:47], v[116:119]\n\t"
    "s_waitcnt lgkmcnt(6)\n\t"
    "buffer_load_dwordx4 v19, s[24:27], s47 offen offset:3072 lds\n\t"
    "v_mfma_f32_16x16x32_bf16 v[116:119], a[120:123], v[64:67], v[116:119]\n\t"
    "s_waitcnt lgkmcnt(4)\n\t"
    "ds_read_b64_tr_b16 a[136:137], v5 offset:40960\n\t"
    "v_fma_f32 v190, v190, s34, v22\n\t"
    "v_exp_f32 v186, v186\n\t"
    "v_exp_f32 v187, v187\n\t"
    "v_exp_f32 v188, v188\n\t"
    "v_exp_f32 v189, v189\n\t"
    "v_exp_f32 v190, v190\n\t"
    "v_cvt_pk_bf16_f32 v185, v186, v187\n\t"
    "v_fma_f32 v191, v191, s34, v22\n\t"
    "v_cvt_pk_bf16_f32 v106, v188, v189\n\t"
    "v_fma_f32 v40, v40, s34, v23\n\t"
    "v_exp_f32 v191, v191\n\t"
    "v_exp_f32 v40, v40\n\t"
    "v_cvt_pk_bf16_f32 v107, v190, v191\n\t"
    "v_fma_f32 v41, v41, s34, v23\n\t"
    "v_fma_f32 v42, v42, s34, v23\n\t"
    "v_fma_f32 v43, v43, s34, v23\n\t"
    "v_mfma_f32_16x16x32_bf16 v[116:119], a[124:127], v[104:107], v[116:119]\n\t"
    "ds_read_b64_tr_b16 a[138:139], v5 offset:43008\n\t"
    "v_fma_f32 v140, v140, s34, v24\n\t"
    "v_mfma_f32_16x16x32_bf16 a[164:167], a[160:163], v[104:107], a[164:167]\n\t"
    "v_exp_f32 v41, v41\n\t"
    "v_mfma_f32_16x16x32_bf16 v[164:167], a[96:99], v[96:99], v[164:167]\n\t"
    "v_exp_f32 v42, v42\n\t"
    "v_mfma_f32_16x16x32_bf16 v[164:167], a[100:103], v[108:111], v[164:167]\n\t"
    "ds_read_b64_tr_b16 a[140:141], v5 offset:45056\n\t"
    "v_cvt_pk_bf16_f32 v182, v40, v41\n\t"
    ".p2align 12\n\t"
    "Lstub5_%=:\n\t"
    "s_cbranch_execz Lret_%=\n\t"
    "v_mfma_f32_16x16x32_bf16 v[164:167], a[104:107], v[120:123], v[164:167]\n\t"
    "ds_read_b64_tr_b16 a[142:143], v5 offset:47104\n\t"
    "v_accvgpr_read_b32 v25, a164\n\t"
    "v_mfma_f32_16x16x32_bf16 v[164:167], a[108:111], v[132:135], v[164:167]\n\t"
    "v_exp_f32 v43, v43\n\t"
    "v_mfma_f32_16x16x32_bf16 v[164:167], a[112:115], v[144:147], v[164:167]\n\t"
    "ds_read_b64_tr_b16 a[144:145], v5 offset:49152\n\t"
    "v_cmp_nle_f32 s[28:29], v25, v31\n\t"
    "v_mfma_f32_16x16x32_bf16 v[164:167], a[116:119], v[156:159], v[164:167]\n\t"
    "ds_read_b64_tr_b16 a[146:147], v5 offset:51200\n\t"
    "v_cvt_pk_bf16_f32 v183, v42, v43\n\t"
    "v_mfma_f32_16x16x32_bf16 v[164:167], a[120:123], v[168:171], v[164:167]\n\t"
    "v_rcp_f32 v25, v25\n\t"
    "v_mfma_f32_16x16x32_bf16 v[164:167], a[124:127], v[180:183], v[164:167]\n\t"
    "ds_read_b64_tr_b16 a[148:149], v5 offset:53248\n\t"
    "v_fma_f32 v141, v141, s34, v24\n\t"
    "v_mfma_f32_16x16x32_bf16 a[168:171], a[160:163], v[180:183], a[168:171]\n\t"
    "v_exp_f32 v140, v140\n\t"
    "v_mfma_f32_16x16x32_bf16 v[192:195], a[96:99], v[100:103], v[192:195]\n\t"
    "ds_read_b64_tr_b16 a[150:151], v5 offset:55296\n\t"
    "v_fma_f32 v142, v142, s34, v24\n\t"
    "v_mfma_f32_16x16x32_bf16 v[192:195], a[100:103], v[112:115], v[192:195]\n\t"
    "v_exp_f32 v141, v141\n\t"
    "v_mfma_f32_16x16x32_bf16 v[192:195], a[104:107], v[124:127], v[192:195]\n\t"
    "ds_read_b64_tr_b16 a[152:153], v5 offset:57344\n\t"
    "v_accvgpr_read_b32 v26, a168\n\t"
    "v_mfma_f32_16x16x32_bf16 v[192:195], a[108:111], v[136:139], v[192:195]\n\t"
    "ds_read_b64_tr_b16 a[154:155], v5 offset:59392\n\t"
    "v_cmp_nle_f32 s[30:31], v26, v31\n\t"
    "v_mfma_f32_16x16x32_bf16 v[192:195], a[112:115], v[148:151], v[192:195]\n\t"
    "v_rcp_f32 v26, v26\n\t"
    "v_mfma_f32_16x16x32_bf16 v[192:195], a[116:119], v[160:163], v[192:195]\n\t"
    "ds_read_b64_tr_b16 a[156:157], v5 offset:61440\n\t"
    "v_fma_f32 v143, v143, s34, v24\n\t"
    "v_mfma_f32_16x16x32_bf16 v[192:195], a[120:123], v[172:175], v[192:195]\n\t"
    "ds_read_b64_tr_b16 a[158:159], v5 offset:63488\n\t"
    "v_cvt_pk_bf16_f32 v186, v140, v141\n\t"
    "v_exp_f32 v142, v142\n\t"
    "v_exp_f32 v143, v143\n\t"
    "v_mul_f32 v116, v25, v116\n\t"
    "v_cvt_pk_bf16_f32 v187, v142, v143\n\t"
    "v_mul_f32 v117, v25, v117\n\t"
    "v_mul_f32 v118, v25, v118\n\t"
    "v_mfma_f32_16x16x32_bf16 v[192:195], a[124:127], v[184:187], v[192:195]\n\t"
    "s_waitcnt lgkmcnt(14)\n\t"
    "v_mul_f32 v119, v25, v119\n\t"
    "v_cvt_pk_bf16_f32 v116, v116, v117\n\t"
    "v_mfma_f32_16x16x32_bf16 a[172:175], a[160:163], v[184:187], a[172:175]\n\t"
    "v_cvt_pk_bf16_f32 v117, v118, v119\n\t"
    "v_mul_f32 v164, v26, v164\n\t"
    "v_mfma_f32_16x16x32_bf16 v[196:199], a[128:131], v[76:79], v[196:199]\n\t"
    "s_waitcnt lgkmcnt(12)\n\t"
    "ds_read_b64_tr_b16 a[96:97], v6 offset:32768\n\t"
    "v_mul_f32 v165, v26, v165\n\t"
    "v_mfma_f32_16x16x32_bf16 v[196:199], a[132:135], v[48:51], v[196:199]\n\t"
    "s_waitcnt lgkmcnt(11)\n\t"
    "ds_read_b64_tr_b16 a[98:99], v6 offset:34816\n\t"
    "v_accvgpr_read_b32 v27, a172\n\t"
    "v_mfma_f32_16x16x32_bf16 v[196:199], a[136:139], v[92:95], v[196:199]\n\t"
    "s_waitcnt lgkmcnt(10)\n\t"
    "v_cmp_nle_f32 s[58:59], v27, v31\n\t"
    "v_mul_f32 v166, v26, v166\n\t"
    "v_mfma_f32_16x16x32_bf16 v[196:199], a[140:143], v[88:91], v[196:199]\n\t"
    "s_waitcnt lgkmcnt(8)\n\t"
    "ds_read_b64_tr_b16 a[100:101], v6 offset:36864\n\t"
    "v_mul_f32 v167, v26, v167\n\t"
    "v_mfma_f32_16x16x32_bf16 v[196:199], a[144:147], v[68:71], v[196:199]\n\t"
    "s_waitcnt lgkmcnt(7)\n\t"
    "ds_read_b64_tr_b16 a[102:103], v6 offset:38912\n\t"
    "v_cvt_pk_bf16_f32 v164, v164, v165\n\t"
    "v_mfma_f32_16x16x32_bf16 v[196:199], a[148:151], v[44:47], v[196:199]\n\t"
    "s_waitcnt lgkmcnt(6)\n\t"
    "v_rcp_f32 v27, v27\n\t"
    "v_mfma_f32_16x16x32_bf16 v[196:199], a[152:155], v[64:67], v[196:199]\n\t"
    "s_waitcnt lgkmcnt(4)\n\t"
    "ds_read_b64_tr_b16 a[104:105], v6 offset:40960\n\t"
    "v_cvt_pk_bf16_f32 v165, v166, v167\n\t"
    "v_mfma_f32_16x16x32_bf16 v[196:199], a[156:159], v[104:107], v[196:199]\n\t"
    "ds_read_b64_tr_b16 a[106:107], v6 offset:43008\n\t"
    "v_mul_f32 v192, v27, v192\n\t"
    "v_mfma_f32_16x16x32_bf16 v[200:203], a[128:131], v[96:99], v[200:203]\n\t"
    "v_mul_f32 v193, v27, v193\n\t"
    "v_mul_f32 v194, v27, v194\n\t"
    "v_mfma_f32_16x16x32_bf16 v[200:203], a[132:135], v[108:111], v[200:203]\n\t"
    "ds_read_b64_tr_b16 a[108:109], v6 offset:45056\n\t"
    "v_mul_f32 v195, v27, v195\n\t"
    "v_mfma_f32_16x16x32_bf16 v[200:203], a[136:139], v[120:123], v[200:203]\n\t"
    "ds_read_b64_tr_b16 a[110:111], v6 offset:47104\n\t"
    "v_cvt_pk_bf16_f32 v192, v192, v193\n\t"
    "v_mfma_f32_16x16x32_bf16 v[200:203], a[140:143], v[132:135], v[200:203]\n\t"
    "v_cvt_pk_bf16_f32 v193, v194, v195\n\t"
    "v_mul_f32 v196, v25, v196\n\t"
    "v_mfma_f32_16x16x32_bf16 v[200:203], a[144:147], v[144:147], v[200:203]\n\t"
    "ds_read_b64_tr_b16 a[112:113], v6 offset:49152\n\t"
    "v_mul_f32 v197, v25, v197\n\t"
    "v_mfma_f32_16x16x32_bf16 v[200:203], a[148:151], v[156:159], v[200:203]\n\t"
    "ds_read_b64_tr_b16 a[114:115], v6 offset:51200\n\t"
    "v_mul_f32 v198, v25, v198\n\t"
    "v_mfma_f32_16x16x32_bf16 v[200:203], a[152:155], v[168:171], v[200:203]\n\t"
    "v_mul_f32 v199, v25, v199\n\t"
    "v_cvt_pk_bf16_f32 v118, v196, v197\n\t"
    "v_mfma_f32_16x16x32_bf16 v[200:203], a[156:159], v[180:183], v[200:203]\n\t"
    "ds_read_b64_tr_b16 a[116:117], v6 offset:53248\n\t"
    "v_cvt_pk_bf16_f32 v119, v198, v199\n\t"
    "v_mfma_f32_16x16x32_bf16 v[212:215], a[128:131], v[100:103], v[212:215]\n\t"
    "ds_read_b64_tr_b16 a[118:119], v6 offset:55296\n\t"
    "v_mfma_f32_16x16x32_bf16 v[212:215], a[132:135], v[112:115], v[212:215]\n\t"
    "v_permlane16_swap_b32 v116, v118\n\t"
    "v_mfma_f32_16x16x32_bf16 v[212:215], a[136:139], v[124:127], v[212:215]\n\t"
    "ds_read_b64_tr_b16 a[120:121], v6 offset:57344\n\t"
    "v_mul_f32 v200, v26, v200\n\t"
    "v_mfma_f32_16x16x32_bf16 v[212:215], a[140:143], v[136:139], v[212:215]\n\t"
    "ds_read_b64_tr_b16 a[122:123], v6 offset:59392\n\t"
    "v_mul_f32 v201, v26, v201\n\t"
    "v_mfma_f32_16x16x32_bf16 v[212:215], a[144:147], v[148:151], v[212:215]\n\t"
    "v_permlane16_swap_b32 v117, v119\n\t"
    "v_mfma_f32_16x16x32_bf16 v[212:215], a[148:151], v[160:163], v[212:215]\n\t"
    "ds_read_b64_tr_b16 a[124:125], v6 offset:61440\n\t"
    "v_mul_f32 v202, v26, v202\n\t"
    "v_mfma_f32_16x16x32_bf16 v[212:215], a[152:155], v[172:175], v[212:215]\n\t"
    "ds_read_b64_tr_b16 a[126:127], v6 offset:63488\n\t"
    "v_mul_f32 v203, v26, v203\n\t"
    "v_mfma_f32_16x16x32_bf16 v[212:215], a[156:159], v[184:187], v[212:215]\n\t"
    "s_waitcnt lgkmcnt(14)\n\t"
    "buffer_store_dwordx4 v[116:119], v21, s[36:39], s64 offen offset:0 sc1\n\t"
    "v_mfma_f32_16x16x32_bf16 v[176:179], a[96:99], v[76:79], v[176:179]\n\t"
    "s_waitcnt lgkmcnt(12)\n\t"
    "ds_read_b64_tr_b16 a[128:129], v7 offset:32768\n\t"
    "v_cvt_pk_bf16_f32 v166, v200, v201\n\t"
    "v_mfma_f32_16x16x32_bf16 v[176:179], a[100:103], v[48:51], v[176:179]\n\t"
    "s_waitcnt lgkmcnt(11)\n\t"
    "ds_read_b64_tr_b16 a[130:131], v7 offset:34816\n\t"
    "v_cvt_pk_bf16_f32 v167, v202, v203\n\t"
    "v_mfma_f32_16x16x32_bf16 v[176:179], a[104:107], v[92:95], v[176:179]\n\t"
    "s_waitcnt lgkmcnt(10)\n\t"
    "v_permlane16_swap_b32 v164, v166\n\t"
    "v_mfma_f32_16x16x32_bf16 v[176:179], a[108:111], v[88:91], v[176:179]\n\t"
    "s_waitcnt lgkmcnt(8)\n\t"
    "ds_read_b64_tr_b16 a[132:133], v7 offset:36864\n\t"
    "v_mul_f32 v212, v27, v212\n\t"
    "v_mfma_f32_16x16x32_bf16 v[176:179], a[112:115], v[68:71], v[176:179]\n\t"
    "s_waitcnt lgkmcnt(7)\n\t"
    "ds_read_b64_tr_b16 a[134:135], v7 offset:38912\n\t"
    "v_mul_f32 v213, v27, v213\n\t"
    "v_mfma_f32_16x16x32_bf16 v[176:179], a[116:119], v[44:47], v[176:179]\n\t"
    "s_waitcnt lgkmcnt(6)\n\t"
    "v_permlane16_swap_b32 v165, v167\n\t"
    "v_mfma_f32_16x16x32_bf16 v[176:179], a[120:123], v[64:67], v[176:179]\n\t"
    "s_waitcnt lgkmcnt(4)\n\t"
    "ds_read_b64_tr_b16 a[136:137], v7 offset:40960\n\t"
    "v_mul_f32 v214, v27, v214\n\t"
    "v_mfma_f32_16x16x32_bf16 v[176:179], a[124:127], v[104:107], v[176:179]\n\t"
    "ds_read_b64_tr_b16 a[138:139], v7 offset:43008\n\t"
    "v_mul_f32 v215, v27, v215\n\t"
    "v_mfma_f32_16x16x32_bf16 v[152:155], a[96:99], v[96:99], v[152:155]\n\t"
    "buffer_store_dwordx4 v[164:167], v21, s[36:39], s65 offen offset:0 sc1\n\t"
    "v_mfma_f32_16x16x32_bf16 v[152:155], a[100:103], v[108:111], v[152:155]\n\t"
    "ds_read_b64_tr_b16 a[140:141], v7 offset:45056\n\t"
    "v_cvt_pk_bf16_f32 v194, v212, v213\n\t"
    "v_mfma_f32_16x16x32_bf16 v[152:155], a[104:107], v[120:123], v[152:155]\n\t"
    "ds_read_b64_tr_b16 a[142:143], v7 offset:47104\n\t"
    "v_cvt_pk_bf16_f32 v195, v214, v215\n\t"
    "v_mfma_f32_16x16x32_bf16 v[152:155], a[108:111], v[132:135], v[152:155]\n\t"
    "v_permlane16_swap_b32 v192, v194\n\t"
    "v_mfma_f32_16x16x32_bf16 v[152:155], a[112:115], v[144:147], v[152:155]\n\t"
    "ds_read_b64_tr_b16 a[144:145], v7 offset:49152\n\t"
    "v_mul_f32 v176, v25, v176\n\t"
    "v_mfma_f32_16x16x32_bf16 v[152:155], a[116:119], v[156:159], v[152:155]\n\t"
    "ds_read_b64_tr_b16 a[146:147], v7 offset:51200\n\t"
    "v_mul_f32 v177, v25, v177\n\t"
    "v_mfma_f32_16x16x32_bf16 v[152:155], a[120:123], v[168:171], v[152:155]\n\t"
    "v_permlane16_swap_b32 v193, v195\n\t"
    "v_mfma_f32_16x16x32_bf16 v[152:155], a[124:127], v[180:183], v[152:155]\n\t"
    "ds_read_b64_tr_b16 a[148:149], v7 offset:53248\n\t"
    "v_mul_f32 v178, v25, v178\n\t"
    "v_mfma_f32_16x16x32_bf16 v[216:219], a[96:99], v[100:103], v[216:219]\n\t"
    "ds_read_b64_tr_b16 a[150:151], v7 offset:55296\n\t"
    "v_mul_f32 v179, v25, v179\n\t"
    "v_mfma_f32_16x16x32_bf16 v[216:219], a[100:103], v[112:115], v[216:219]\n\t"
    "buffer_store_dwordx4 v[192:195], v21, s[36:39], s66 offen offset:0 sc1\n\t"
    "v_mfma_f32_16x16x32_bf16 v[216:219], a[104:107], v[124:127], v[216:219]\n\t"
    "ds_read_b64_tr_b16 a[152:153], v7 offset:57344\n\t"
    "v_cvt_pk_bf16_f32 v176, v176, v177\n\t"
    "v_mfma_f32_16x16x32_bf16 v[216:219], a[108:111], v[136:139], v[216:219]\n\t"
    "ds_read_b64_tr_b16 a[154:155], v7 offset:59392\n\t"
    "v_cvt_pk_bf16_f32 v177, v178, v179\n\t"
    "v_mfma_f32_16x16x32_bf16 v[216:219], a[112:115], v[148:151], v[216:219]\n\t"
    "s_waitcnt vmcnt(3)\n\t"
    "s_barrier\n\t"
    "v_mul_f32 v152, v26, v152\n\t"
    "v_mfma_f32_16x16x32_bf16 v[216:219], a[116:119], v[160:163], v[216:219]\n\t"
    "ds_read_b64_tr_b16 a[156:157], v7 offset:61440\n\t"
    "v_mul_f32 v153, v26, v153\n\t"
    "v_mfma_f32_16x16x32_bf16 v[216:219], a[120:123], v[172:175], v[216:219]\n\t"
    "ds_read_b64_tr_b16 a[158:159], v7 offset:63488\n\t"
    "v_mul_f32 v154, v26, v154\n\t"
    "v_mfma_f32_16x16x32_bf16 v[216:219], a[124:127], v[184:187], v[216:219]\n\t"
    "s_waitcnt lgkmcnt(14)\n\t"
    "v_mul_f32 v155, v26, v155\n\t"
    "v_cvt_pk_bf16_f32 v152, v152, v153\n\t"
    "v_mfma_f32_16x16x32_bf16 v[128:131], a[128:131], v[76:79], v[128:131]\n\t"
    "s_waitcnt lgkmcnt(12)\n\t"
    "v_add_u32 v8, 0xffff0000, v8\n\t"
    "v_add_u32 v9, 0xffff0000, v9\n\t"
    "v_add_u32 v10, 0xffff0000, v10\n\t"
    "v_add_u32 v11, 0xffff0000, v11\n\t"
    "v_mfma_f32_16x16x32_bf16 v[128:131], a[132:135], v[48:51], v[128:131]\n\t"
    "s_waitcnt lgkmcnt(10)\n\t"
    "ds_read_b64_tr_b16 a[96:97], v8 offset:0\n\t"
    "v_cvt_pk_bf16_f32 v153, v154, v155\n\t"
    "v_mfma_f32_16x16x32_bf16 v[128:131], a[136:139], v[92:95], v[128:131]\n\t"
    "s_waitcnt lgkmcnt(9)\n\t"
    "ds_read_b64_tr_b16 a[98:99], v8 offset:2048\n\t"
    "v_mul_f32 v216, v27, v216\n\t"
    "v_mfma_f32_16x16x32_bf16 v[128:131], a[140:143], v[88:91], v[128:131]\n\t"
    "s_waitcnt lgkmcnt(8)\n\t"
    "ds_read_b64_tr_b16 a[100:101], v8 offset:4096\n\t"
    "v_mul_f32 v217, v27, v217\n\t"
    "v_mfma_f32_16x16x32_bf16 v[128:131], a[144:147], v[68:71], v[128:131]\n\t"
    "s_waitcnt lgkmcnt(7)\n\t"
    "ds_read_b64_tr_b16 a[102:103], v8 offset:6144\n\t"
    "v_mul_f32 v218, v27, v218\n\t"
    "v_mfma_f32_16x16x32_bf16 v[128:131], a[148:151], v[44:47], v[128:131]\n\t"
    "s_waitcnt lgkmcnt(6)\n\t"
    "v_mul_f32 v219, v27, v219\n\t"
    "v_cvt_pk_bf16_f32 v216, v216, v217\n\t"
    "v_mfma_f32_16x16x32_bf16 v[128:131], a[152:155], v[64:67], v[128:131]\n\t"
    "s_waitcnt lgkmcnt(4)\n\t"
    "ds_read_b64_tr_b16 a[104:105], v8 offset:8192\n\t"
    "v_cvt_pk_bf16_f32 v217, v218, v219\n\t"
    "v_mfma_f32_16x16x32_bf16 v[128:131], a[156:159], v[104:107], v[128:131]\n\t"
    "ds_read_b64_tr_b16 a[106:107], v8 offset:10240\n\t"
    "v_mfma_f32_16x16x32_bf16 v[220:223], a[128:131], v[96:99], v[220:223]\n\t"
    "v_mfma_f32_16x16x32_bf16 v[220:223], a[132:135], v[108:111], v[220:223]\n\t"
    "ds_read_b64_tr_b16 a[108:109], v8 offset:12288\n\t"
    "v_mfma_f32_16x16x32_bf16 v[220:223], a[136:139], v[120:123], v[220:223]\n\t"
    "ds_read_b64_tr_b16 a[110:111], v8 offset:14336\n\t"
    "v_mfma_f32_16x16x32_bf16 v[220:223], a[140:143], v[132:135], v[220:223]\n\t"
    "v_mfma_f32_16x16x32_bf16 v[220:223], a[144:147], v[144:147], v[220:223]\n\t"
    "ds_read_b64_tr_b16 a[112:113], v8 offset:16384\n\t"
    "v_mul_f32 v128, v25, v128\n\t"
    "v_mfma_f32_16x16x32_bf16 v[220:223], a[148:151], v[156:159], v[220:223]\n\t"
    "ds_read_b64_tr_b16 a[114:115], v8 offset:18432\n\t"
    "v_mul_f32 v129, v25, v129\n\t"
    "v_mfma_f32_16x16x32_bf16 v[220:223], a[152:155], v[168:171], v[220:223]\n\t"
    "v_mul_f32 v130, v25, v130\n\t"
    "v_mul_f32 v131, v25, v131\n\t"
    "v_mfma_f32_16x16x32_bf16 v[220:223], a[156:159], v[180:183], v[220:223]\n\t"
    "ds_read_b64_tr_b16 a[116:117], v8 offset:20480\n\t"
    "v_cvt_pk_bf16_f32 v178, v128, v129\n\t"
    "v_mfma_f32_16x16x32_bf16 v[224:227], a[128:131], v[100:103], v[224:227]\n\t"
    "ds_read_b64_tr_b16 a[118:119], v8 offset:22528\n\t"
    "v_cvt_pk_bf16_f32 v179, v130, v131\n\t"
    "v_mfma_f32_16x16x32_bf16 v[224:227], a[132:135], v[112:115], v[224:227]\n\t"
    "v_permlane16_swap_b32 v176, v178\n\t"
    "v_mfma_f32_16x16x32_bf16 v[224:227], a[136:139], v[124:127], v[224:227]\n\t"
    "ds_read_b64_tr_b16 a[120:121], v8 offset:24576\n\t"
    "v_mul_f32 v220, v26, v220\n\t"
    "v_mfma_f32_16x16x32_bf16 v[224:227], a[140:143], v[136:139], v[224:227]\n\t"
    "ds_read_b64_tr_b16 a[122:123], v8 offset:26624\n\t"
    "v_mul_f32 v221, v26, v221\n\t"
    "v_mfma_f32_16x16x32_bf16 v[224:227], a[144:147], v[148:151], v[224:227]\n\t"
    "v_permlane16_swap_b32 v177, v179\n\t"
    "v_mfma_f32_16x16x32_bf16 v[224:227], a[148:151], v[160:163], v[224:227]\n\t"
    "ds_read_b64_tr_b16 a[124:125], v8 offset:28672\n\t"
    "v_mul_f32 v222, v26, v222\n\t"
    "v_mfma_f32_16x16x32_bf16 v[224:227], a[152:155], v[172:175], v[224:227]\n\t"
    "ds_read_b64_tr_b16 a[126:127], v8 offset:30720\n\t"
    "v_mul_f32 v223, v26, v223\n\t"
    "v_mfma_f32_16x16x32_bf16 v[224:227], a[156:159], v[184:187], v[224:227]\n\t"
    "s_waitcnt lgkmcnt(14)\n\t"
    "buffer_store_dwordx4 v[176:179], v21, s[36:39], s64 offen offset:64 sc1\n\t"
    "v_mfma_f32_16x16x32_bf16 v[40:43], a[96:99], a[176:179], 0\n\t"
    "s_waitcnt lgkmcnt(12)\n\t"
    "ds_read_b64_tr_b16 a[128:129], v9 offset:0\n\t"
    "v_cvt_pk_bf16_f32 v154, v220, v221\n\t"
    "v_mfma_f32_16x16x32_bf16 v[40:43], a[100:103], a[188:191], v[40:43]\n\t"
    "s_waitcnt lgkmcnt(11)\n\t"
    "ds_read_b64_tr_b16 a[130:131], v9 offset:2048\n\t"
    "v_cvt_pk_bf16_f32 v155, v222, v223\n\t"
    "v_mfma_f32_16x16x32_bf16 v[40:43], a[104:107], a[200:203], v[40:43]\n\t"
    "s_waitcnt lgkmcnt(10)\n\t"
    "v_permlane16_swap_b32 v152, v154\n\t"
    "v_mfma_f32_16x16x32_bf16 v[40:43], a[108:111], a[212:215], v[40:43]\n\t"
    "s_waitcnt lgkmcnt(8)\n\t"
    "ds_read_b64_tr_b16 a[132:133], v9 offset:4096\n\t"
    "v_mul_f32 v224, v27, v224\n\t"
    "v_mfma_f32_16x16x32_bf16 v[40:43], a[112:115], v[228:231], v[40:43]\n\t"
    "s_waitcnt lgkmcnt(7)\n\t"
    "ds_read_b64_tr_b16 a[134:135], v9 offset:6144\n\t"
    "v_mul_f32 v225, v27, v225\n\t"
    "v_mfma_f32_16x16x32_bf16 v[40:43], a[116:119], v[252:255], v[40:43]\n\t"
    "s_waitcnt lgkmcnt(6)\n\t"
    "v_permlane16_swap_b32 v153, v155\n\t"
    "v_mfma_f32_16x16x32_bf16 v[40:43], a[120:123], v[80:83], v[40:43]\n\t"
    "s_waitcnt lgkmcnt(4)\n\t"
    "ds_read_b64_tr_b16 a[136:137], v9 offset:8192\n\t"
    "v_mul_f32 v226, v27, v226\n\t"
    "v_mfma_f32_16x16x32_bf16 v[40:43], a[124:127], v[52:55], v[40:43]\n\t"
    "ds_read_b64_tr_b16 a[138:139], v9 offset:10240\n\t"
    "v_mul_f32 v227, v27, v227\n\t"
    "v_mfma_f32_16x16x32_bf16 v[116:119], a[96:99], a[180:183], 0\n\t"
    "buffer_store_dwordx4 v[152:155], v21, s[36:39], s65 offen offset:64 sc1\n\t"
    "v_mfma_f32_16x16x32_bf16 v[116:119], a[100:103], a[192:195], v[116:119]\n\t"
    "ds_read_b64_tr_b16 a[140:141], v9 offset:12288\n\t"
    "v_cvt_pk_bf16_f32 v218, v224, v225\n\t"
    "v_mfma_f32_16x16x32_bf16 v[116:119], a[104:107], a[204:207], v[116:119]\n\t"
    "ds_read_b64_tr_b16 a[142:143], v9 offset:14336\n\t"
    "v_cvt_pk_bf16_f32 v219, v226, v227\n\t"
    "v_mfma_f32_16x16x32_bf16 v[116:119], a[108:111], a[216:219], v[116:119]\n\t"
    "v_permlane16_swap_b32 v216, v218\n\t"
    "v_mfma_f32_16x16x32_bf16 v[116:119], a[112:115], v[204:207], v[116:119]\n\t"
    "ds_read_b64_tr_b16 a[144:145], v9 offset:16384\n\t"
    "v_mfma_f32_16x16x32_bf16 v[116:119], a[116:119], v[208:211], v[116:119]\n\t"
    "ds_read_b64_tr_b16 a[146:147], v9 offset:18432\n\t"
    "v_mfma_f32_16x16x32_bf16 v[116:119], a[120:123], v[36:39], v[116:119]\n\t"
    "v_permlane16_swap_b32 v217, v219\n\t"
    "v_mfma_f32_16x16x32_bf16 v[116:119], a[124:127], v[56:59], v[116:119]\n\t"
    "ds_read_b64_tr_b16 a[148:149], v9 offset:20480\n\t"
    "v_mfma_f32_16x16x32_bf16 v[128:131], a[96:99], a[184:187], 0\n\t"
    "ds_read_b64_tr_b16 a[150:151], v9 offset:22528\n\t"
    "v_mfma_f32_16x16x32_bf16 v[128:131], a[100:103], a[196:199], v[128:131]\n\t"
    "buffer_store_dwordx4 v[216:219], v21, s[36:39], s66 offen offset:64 sc1\n\t"
    "v_mfma_f32_16x16x32_bf16 v[128:131], a[104:107], a[208:211], v[128:131]\n\t"
    "ds_read_b64_tr_b16 a[152:153], v9 offset:24576\n\t"
    "v_mfma_f32_16x16x32_bf16 v[128:131], a[108:111], a[220:223], v[128:131]\n\t"
    "ds_read_b64_tr_b16 a[154:155], v9 offset:26624\n\t"
    "v_mfma_f32_16x16x32_bf16 v[128:131], a[112:115], v[32:35], v[128:131]\n\t"
    "v_mfma_f32_16x16x32_bf16 v[128:131], a[116:119], v[72:75], v[128:131]\n\t"
    "ds_read_b64_tr_b16 a[156:157], v9 offset:28672\n\t"
    "v_mfma_f32_16x16x32_bf16 v[128:131], a[120:123], v[84:87], v[128:131]\n\t"
    "ds_read_b64_tr_b16 a[158:159], v9 offset:30720\n\t"
    "v_mfma_f32_16x16x32_bf16 v[128:131], a[124:127], v[60:63], v[128:131]\n\t"
    "s_waitcnt lgkmcnt(14)\n\t"
    "v_mfma_f32_16x16x32_bf16 v[140:143], a[128:131], a[176:179], 0\n\t"
    "s_waitcnt lgkmcnt(12)\n\t"
    "ds_read_b64_tr_b16 a[96:97], v10 offset:0\n\t"
    "v_mfma_f32_16x16x32_bf16 v[140:143], a[132:135], a[188:191], v[140:143]\n\t"
    "s_waitcnt lgkmcnt(11)\n\t"
    "ds_read_b64_tr_b16 a[98:99], v10 offset:2048\n\t"
    "v_mfma_f32_16x16x32_bf16 v[140:143], a[136:139], a[200:203], v[140:143]\n\t"
    "s_waitcnt lgkmcnt(10)\n\t"
    "v_mfma_f32_16x16x32_bf16 v[140:143], a[140:143], a[212:215], v[140:143]\n\t"
    "s_waitcnt lgkmcnt(8)\n\t"
    "ds_read_b64_tr_b16 a[100:101], v10 offset:4096\n\t"
    "v_mfma_f32_16x16x32_bf16 v[140:143], a[144:147], v[228:231], v[140:143]\n\t"
    "s_waitcnt lgkmcnt(7)\n\t"
    "ds_read_b64_tr_b16 a[102:103], v10 offset:6144\n\t"
    "v_mfma_f32_16x16x32_bf16 v[140:143], a[148:151], v[252:255], v[140:143]\n\t"
    "s_waitcnt lgkmcnt(6)\n\t"
    "v_mfma_f32_16x16x32_bf16 v[140:143], a[152:155], v[80:83], v[140:143]\n\t"
    "s_waitcnt lgkmcnt(4)\n\t"
    "ds_read_b64_tr_b16 a[104:105], v10 offset:8192\n\t"
    "v_mfma_f32_16x16x32_bf16 v[140:143], a[156:159], v[52:55], v[140:143]\n\t"
    "ds_read_b64_tr_b16 a[106:107], v10 offset:10240\n\t"
    "v_mfma_f32_16x16x32_bf16 v[152:155], a[128:131], a[180:183], 0\n\t"
    "v_mfma_f32_16x16x32_bf16 v[152:155], a[132:135], a[192:195], v[152:155]\n\t"
    "ds_read_b64_tr_b16 a[108:109], v10 offset:12288\n\t"
    "v_mfma_f32_16x16x32_bf16 v[152:155], a[136:139], a[204:207], v[152:155]\n\t"
    "ds_read_b64_tr_b16 a[110:111], v10 offset:14336\n\t"
    "v_mfma_f32_16x16x32_bf16 v[152:155], a[140:143], a[216:219], v[152:155]\n\t"
    "v_mfma_f32_16x16x32_bf16 v[152:155], a[144:147], v[204:207], v[152:155]\n\t"
    "ds_read_b64_tr_b16 a[112:113], v10 offset:16384\n\t"
    "v_mfma_f32_16x16x32_bf16 v[152:155], a[148:151], v[208:211], v[152:155]\n\t"
    "s_add_u32 m0, s40, 98304\n\t"
    "ds_read_b64_tr_b16 a[114:115], v10 offset:18432\n\t"
    "v_mfma_f32_16x16x32_bf16 v[152:155], a[152:155], v[36:39], v[152:155]\n\t"
    "s_add_u32 s46, s44, 0\n\t"
    "v_mfma_f32_16x16x32_bf16 v[152:155], a[156:159], v[56:59], v[152:155]\n\t"
    "ds_read_b64_tr_b16 a[116:117], v10 offset:20480\n\t"
    "v_mfma_f32_16x16x32_bf16 v[164:167], a[128:131], a[184:187], 0\n\t"
    "ds_read_b64_tr_b16 a[118:119], v10 offset:22528\n\t"
    "v_mfma_f32_16x16x32_bf16 v[164:167], a[132:135], a[196:199], v[164:167]\n\t"
    "buffer_load_dwordx4 v16, s[24:27], s46 offen offset:0 lds\n\t"
    "v_mfma_f32_16x16x32_bf16 v[164:167], a[136:139], a[208:211], v[164:167]\n\t"
    "ds_read_b64_tr_b16 a[120:121], v10 offset:24576\n\t"
    "v_mfma_f32_16x16x32_bf16 v[164:167], a[140:143], a[220:223], v[164:167]\n\t"
    "ds_read_b64_tr_b16 a[122:123], v10 offset:26624\n\t"
    "v_mfma_f32_16x16x32_bf16 v[164:167], a[144:147], v[32:35], v[164:167]\n\t"
    "v_mfma_f32_16x16x32_bf16 v[164:167], a[148:151], v[72:75], v[164:167]\n\t"
    "ds_read_b64_tr_b16 a[124:125], v10 offset:28672\n\t"
    "v_mfma_f32_16x16x32_bf16 v[164:167], a[152:155], v[84:87], v[164:167]\n\t"
    "ds_read_b64_tr_b16 a[126:127], v10 offset:30720\n\t"
    "v_mfma_f32_16x16x32_bf16 v[164:167], a[156:159], v[60:63], v[164:167]\n\t"
    "s_waitcnt lgkmcnt(14)\n\t"
    "buffer_load_dwordx4 v17, s[24:27], s46 offen offset:1024 lds\n\t"
    "v_mfma_f32_16x16x32_bf16 v[176:179], a[96:99], a[176:179], 0\n\t"
    "s_waitcnt lgkmcnt(12)\n\t"
    "ds_read_b64_tr_b16 a[128:129], v11 offset:0\n\t"
    "v_mfma_f32_16x16x32_bf16 v[176:179], a[100:103], a[188:191], v[176:179]\n\t"
    "s_waitcnt lgkmcnt(11)\n\t"
    "ds_read_b64_tr_b16 a[130:131], v11 offset:2048\n\t"
    "v_mfma_f32_16x16x32_bf16 v[176:179], a[104:107], a[200:203], v[176:179]\n\t"
    "s_waitcnt lgkmcnt(10)\n\t"
    "buffer_load_dwordx4 v18, s[24:27], s46 offen offset:2048 lds\n\t"
    "v_mfma_f32_16x16x32_bf16 v[176:179], a[108:111], a[212:215], v[176:179]\n\t"
    "s_waitcnt lgkmcnt(8)\n\t"
    "ds_read_b64_tr_b16 a[132:133], v11 offset:4096\n\t"
    "v_mfma_f32_16x16x32_bf16 v[176:179], a[112:115], v[228:231], v[176:179]\n\t"
    "s_waitcnt lgkmcnt(7)\n\t"
    "ds_read_b64_tr_b16 a[134:135], v11 offset:6144\n\t"
    "v_mfma_f32_16x16x32_bf16 v[176:179], a[116:119], v[252:255], v[176:179]\n\t"
    "s_waitcnt lgkmcnt(6)\n\t"
    "v_mfma_f32_16x16x32_bf16 v[176:179], a[120:123], v[80:83], v[176:179]\n\t"
    "s_waitcnt lgkmcnt(4)\n\t"
    "ds_read_b64_tr_b16 a[136:137], v11 offset:8192\n\t"
    "v_mfma_f32_16x16x32_bf16 v[176:179], a[124:127], v[52:55], v[176:179]\n\t"
    "ds_read_b64_tr_b16 a[138:139], v11 offset:10240\n\t"
    "v_mfma_f32_16x16x32_bf16 v[188:191], a[96:99], a[180:183], 0\n\t"
    "buffer_load_dwordx4 v19, s[24:27], s46 offen offset:3072 lds\n\t"
    "v_mfma_f32_16x16x32_bf16 v[188:191], a[100:103], a[192:195], v[188:191]\n\t"
    "ds_read_b64_tr_b16 a[140:141], v11 offset:12288\n\t"
    "v_mfma_f32_16x16x32_bf16 v[188:191], a[104:107], a[204:207], v[188:191]\n\t"
    "s_add_u32 m0, s40, 102400\n\t"
    "ds_read_b64_tr_b16 a[142:143], v11 offset:14336\n\t"
    "v_mfma_f32_16x16x32_bf16 v[188:191], a[108:111], a[216:219], v[188:191]\n\t"
    "s_add_u32 s47, s44, 16384\n\t"
    "v_mfma_f32_16x16x32_bf16 v[188:191], a[112:115], v[204:207], v[188:191]\n\t"
    "ds_read_b64_tr_b16 a[144:145], v11 offset:16384\n\t"
    "v_mfma_f32_16x16x32_bf16 v[188:191], a[116:119], v[208:211], v[188:191]\n\t"
    "ds_read_b64_tr_b16 a[146:147], v11 offset:18432\n\t"
    "v_mfma_f32_16x16x32_bf16 v[188:191], a[120:123], v[36:39], v[188:191]\n\t"
    "buffer_load_dwordx4 v16, s[24:27], s47 offen offset:0 lds\n\t"
    "v_mfma_f32_16x16x32_bf16 v[188:191], a[124:127], v[56:59], v[188:191]\n\t"
    "ds_read_b64_tr_b16 a[148:149], v11 offset:20480\n\t"
    "v_mfma_f32_16x16x32_bf16 v[192:195], a[96:99], a[184:187], 0\n\t"
    "ds_read_b64_tr_b16 a[150:151], v11 offset:22528\n\t"
    "v_mfma_f32_16x16x32_bf16 v[192:195], a[100:103], a[196:199], v[192:195]\n\t"
    "v_mfma_f32_16x16x32_bf16 v[192:195], a[104:107], a[208:211], v[192:195]\n\t"
    "ds_read_b64_tr_b16 a[152:153], v11 offset:24576\n\t"
    "v_mfma_f32_16x16x32_bf16 v[192:195], a[108:111], a[220:223], v[192:195]\n\t"
    "ds_read_b64_tr_b16 a[154:155], v11 offset:26624\n\t"
    "v_mfma_f32_16x16x32_bf16 v[192:195], a[112:115], v[32:35], v[192:195]\n\t"
    "buffer_load_dwordx4 v17, s[24:27], s47 offen offset:1024 lds\n\t"
    "v_mfma_f32_16x16x32_bf16 v[192:195], a[116:119], v[72:75], v[192:195]\n\t"
    "ds_read_b64_tr_b16 a[156:157], v11 offset:28672\n\t"
    "v_mfma_f32_16x16x32_bf16 v[192:195], a[120:123], v[84:87], v[192:195]\n\t"
    "ds_read_b64_tr_b16 a[158:159], v11 offset:30720\n\t"
    "v_mfma_f32_16x16x32_bf16 v[192:195], a[124:127], v[60:63], v[192:195]\n\t"
    "s_waitcnt lgkmcnt(14)\n\t"
    "v_mfma_f32_16x16x32_bf16 v[196:199], a[128:131], a[176:179], 0\n\t"
    "s_waitcnt lgkmcnt(12)\n\t"
    "ds_read_b64_tr_b16 a[96:97], v8 offset:32768\n\t"
    "v_mfma_f32_16x16x32_bf16 v[196:199], a[132:135], a[188:191], v[196:199]\n\t"
    "s_waitcnt lgkmcnt(11)\n\t"
    "ds_read_b64_tr_b16 a[98:99], v8 offset:34816\n\t"
    "v_mfma_f32_16x16x32_bf16 v[196:199], a[136:139], a[200:203], v[196:199]\n\t"
    "s_waitcnt lgkmcnt(10)\n\t"
    "buffer_load_dwordx4 v18, s[24:27], s47 offen offset:2048 lds\n\t"
    "v_mfma_f32_16x16x32_bf16 v[196:199], a[140:143], a[212:215], v[196:199]\n\t"
    "s_waitcnt lgkmcnt(8)\n\t"
    "ds_read_b64_tr_b16 a[100:101], v8 offset:36864\n\t"
    "v_mfma_f32_16x16x32_bf16 v[196:199], a[144:147], v[228:231], v[196:199]\n\t"
    "s_waitcnt lgkmcnt(7)\n\t"
    "ds_read_b64_tr_b16 a[102:103], v8 offset:38912\n\t"
    "v_mfma_f32_16x16x32_bf16 v[196:199], a[148:151], v[252:255], v[196:199]\n\t"
    "s_waitcnt lgkmcnt(6)\n\t"
    "buffer_load_dwordx4 v19, s[24:27], s47 offen offset:3072 lds\n\t"
    "v_mfma_f32_16x16x32_bf16 v[196:199], a[152:155], v[80:83], v[196:199]\n\t"
    "s_waitcnt lgkmcnt(4)\n\t"
    "ds_read_b64_tr_b16 a[104:105], v8 offset:40960\n\t"
    "v_mfma_f32_16x16x32_bf16 v[196:199], a[156:159], v[52:55], v[196:199]\n\t"
    "ds_read_b64_tr_b16 a[106:107], v8 offset:43008\n\t"
    "v_mfma_f32_16x16x32_bf16 v[200:203], a[128:131], a[180:183], 0\n\t"
    "v_mfma_f32_16x16x32_bf16 v[200:203], a[132:135], a[192:195], v[200:203]\n\t"
    "ds_read_b64_tr_b16 a[108:109], v8 offset:45056\n\t"
    "v_mfma_f32_16x16x32_bf16 v[200:203], a[136:139], a[204:207], v[200:203]\n\t"
    "s_add_u32 m0, s40, 65536\n\t"
    "ds_read_b64_tr_b16 a[110:111], v8 offset:47104\n\t"
    "v_mfma_f32_16x16x32_bf16 v[200:203], a[140:143], a[216:219], v[200:203]\n\t"
    "s_add_u32 s46, s44, 131072\n\t"
    "v_mfma_f32_16x16x32_bf16 v[200:203], a[144:147], v[204:207], v[200:203]\n\t"
    "ds_read_b64_tr_b16 a[112:113], v8 offset:49152\n\t"
    "v_mfma_f32_16x16x32_bf16 v[200:203], a[148:151], v[208:211], v[200:203]\n\t"
    "ds_read_b64_tr_b16 a[114:115], v8 offset:51200\n\t"
    "v_mfma_f32_16x16x32_bf16 v[200:203], a[152:155], v[36:39], v[200:203]\n\t"
    "buffer_load_dwordx4 v16, s[24:27], s46 offen offset:0 lds\n\t"
    "v_mfma_f32_16x16x32_bf16 v[200:203], a[156:159], v[56:59], v[200:203]\n\t"
    "ds_read_b64_tr_b16 a[116:117], v8 offset:53248\n\t"
    "v_mfma_f32_16x16x32_bf16 v[212:215], a[128:131], a[184:187], 0\n\t"
    "ds_read_b64_tr_b16 a[118:119], v8 offset:55296\n\t"
    "v_mfma_f32_16x16x32_bf16 v[212:215], a[132:135], a[196:199], v[212:215]\n\t"
    "v_mfma_f32_16x16x32_bf16 v[212:215], a[136:139], a[208:211], v[212:215]\n\t"
    "ds_read_b64_tr_b16 a[120:121], v8 offset:57344\n\t"
    "v_mfma_f32_16x16x32_bf16 v[212:215], a[140:143], a[220:223], v[212:215]\n\t"
    "ds_read_b64_tr_b16 a[122:123], v8 offset:59392\n\t"
    "v_mfma_f32_16x16x32_bf16 v[212:215], a[144:147], v[32:35], v[212:215]\n\t"
    "buffer_load_dwordx4 v17, s[24:27], s46 offen offset:1024 lds\n\t"
    "v_mfma_f32_16x16x32_bf16 v[212:215], a[148:151], v[72:75], v[212:215]\n\t"
    "ds_read_b64_tr_b16 a[124:125], v8 offset:61440\n\t"
    "v_mfma_f32_16x16x32_bf16 v[212:215], a[152:155], v[84:87], v[212:215]\n\t"
    "ds_read_b64_tr_b16 a[126:127], v8 offset:63488\n\t"
    "v_mfma_f32_16x16x32_bf16 v[212:215], a[156:159], v[60:63], v[212:215]\n\t"
    "s_waitcnt lgkmcnt(14)\n\t"
    "buffer_load_dwordx4 v18, s[24:27], s46 offen offset:2048 lds\n\t"
    "v_mfma_f32_16x16x32_bf16 v[40:43], a[96:99], v[76:79], v[40:43]\n\t"
    "s_waitcnt lgkmcnt(12)\n\t"
    "ds_read_b64_tr_b16 a[128:129], v9 offset:32768\n\t"
    "v_mfma_f32_16x16x32_bf16 v[40:43], a[100:103], v[48:51], v[40:43]\n\t"
    "s_waitcnt lgkmcnt(11)\n\t"
    "ds_read_b64_tr_b16 a[130:131], v9 offset:34816\n\t"
    "v_mfma_f32_16x16x32_bf16 v[40:43], a[104:107], v[92:95], v[40:43]\n\t"
    "s_waitcnt lgkmcnt(10)\n\t"
    "v_mfma_f32_16x16x32_bf16 v[40:43], a[108:111], v[88:91], v[40:43]\n\t"
    "s_waitcnt lgkmcnt(8)\n\t"
    "ds_read_b64_tr_b16 a[132:133], v9 offset:36864\n\t"
    "v_mfma_f32_16x16x32_bf16 v[40:43], a[112:115], v[68:71], v[40:43]\n\t"
    "s_waitcnt lgkmcnt(7)\n\t"
    "ds_read_b64_tr_b16 a[134:135], v9 offset:38912\n\t"
    "v_mfma_f32_16x16x32_bf16 v[40:43], a[116:119], v[44:47], v[40:43]\n\t"
    "s_waitcnt lgkmcnt(6)\n\t"
    "buffer_load_dwordx4 v19, s[24:27], s46 offen offset:3072 lds\n\t"
    "v_mfma_f32_16x16x32_bf16 v[40:43], a[120:123], v[64:67], v[40:43]\n\t"
    "s_waitcnt lgkmcnt(4)\n\t"
    "ds_read_b64_tr_b16 a[136:137], v9 offset:40960\n\t"
    "v_mfma_f32_16x16x32_bf16 v[40:43], a[124:127], v[104:107], v[40:43]\n\t"
    "s_add_u32 m0, s40, 69632\n\t"
    "ds_read_b64_tr_b16 a[138:139], v9 offset:43008\n\t"
    "v_mfma_f32_16x16x32_bf16 v[116:119], a[96:99], v[96:99], v[116:119]\n\t"
    "s_add_u32 s47, s44, 147456\n\t"
    "v_mfma_f32_16x16x32_bf16 v[116:119], a[100:103], v[108:111], v[116:119]\n\t"
    "ds_read_b64_tr_b16 a[140:141], v9 offset:45056\n\t"
    "v_mfma_f32_16x16x32_bf16 v[116:119], a[104:107], v[120:123], v[116:119]\n\t"
    "ds_read_b64_tr_b16 a[142:143], v9 offset:47104\n\t"
    "v_mul_f32 v40, v25, v40\n\t"
    "v_mfma_f32_16x16x32_bf16 v[116:119], a[108:111], v[132:135], v[116:119]\n\t"
    "buffer_load_dwordx4 v16, s[24:27], s47 offen offset:0 lds\n\t"
    "v_mfma_f32_16x16x32_bf16 v[116:119], a[112:115], v[144:147], v[116:119]\n\t"
    "ds_read_b64_tr_b16 a[144:145], v9 offset:49152\n\t"
    "v_mul_f32 v41, v25, v41\n\t"
    "v_mfma_f32_16x16x32_bf16 v[116:119], a[116:119], v[156:159], v[116:119]\n\t"
    "ds_read_b64_tr_b16 a[146:147], v9 offset:51200\n\t"
    "v_mul_f32 v42, v25, v42\n\t"
    "v_mfma_f32_16x16x32_bf16 v[116:119], a[120:123], v[168:171], v[116:119]\n\t"
    "v_mul_f32 v43, v25, v43\n\t"
    "v_cvt_pk_bf16_f32 v40, v40, v41\n\t"
    "v_mfma_f32_16x16x32_bf16 v[116:119], a[124:127], v[180:183], v[116:119]\n\t"
    "ds_read_b64_tr_b16 a[148:149], v9 offset:53248\n\t"
    "v_cvt_pk_bf16_f32 v41, v42, v43\n\t"
    "v_mfma_f32_16x16x32_bf16 v[128:131], a[96:99], v[100:103], v[128:131]\n\t"
    "ds_read_b64_tr_b16 a[150:151], v9 offset:55296\n\t"
    "v_mfma_f32_16x16x32_bf16 v[128:131], a[100:103], v[112:115], v[128:131]\n\t"
    "buffer_load_dwordx4 v17, s[24:27], s47 offen offset:1024 lds\n\t"
    "v_mfma_f32_16x16x32_bf16 v[128:131], a[104:107], v[124:127], v[128:131]\n\t"
    "ds_read_b64_tr_b16 a[152:153], v9 offset:57344\n\t"
    "v_mul_f32 v116, v26, v116\n\t"
    "v_mfma_f32_16x16x32_bf16 v[128:131], a[108:111], v[136:139], v[128:131]\n\t"
    "ds_read_b64_tr_b16 a[154:155], v9 offset:59392\n\t"
    "v_mul_f32 v117, v26, v117\n\t"
    "v_mfma_f32_16x16x32_bf16 v[128:131], a[112:115], v[148:151], v[128:131]\n\t"
    "v_mul_f32 v118, v26, v118\n\t"
    "v_mul_f32 v119, v26, v119\n\t"
    "v_mfma_f32_16x16x32_bf16 v[128:131], a[116:119], v[160:163], v[128:131]\n\t"
    "ds_read_b64_tr_b16 a[156:157], v9 offset:61440\n\t"
    "v_cvt_pk_bf16_f32 v116, v116, v117\n\t"
    "v_mfma_f32_16x16x32_bf16 v[128:131], a[120:123], v[172:175], v[128:131]\n\t"
    "ds_read_b64_tr_b16 a[158:159], v9 offset:63488\n\t"
    "v_cvt_pk_bf16_f32 v117, v118, v119\n\t"
    "v_mfma_f32_16x16x32_bf16 v[128:131], a[124:127], v[184:187], v[128:131]\n\t"
    "s_waitcnt lgkmcnt(14)\n\t"
    "buffer_load_dwordx4 v18, s[24:27], s47 offen offset:2048 lds\n\t"
    "v_mfma_f32_16x16x32_bf16 v[140:143], a[128:131], v[76:79], v[140:143]\n\t"
    "s_waitcnt lgkmcnt(12)\n\t"
    "ds_read_b64_tr_b16 a[96:97], v10 offset:32768\n\t"
    "v_mfma_f32_16x16x32_bf16 v[140:143], a[132:135], v[48:51], v[140:143]\n\t"
    "s_waitcnt lgkmcnt(11)\n\t"
    "ds_read_b64_tr_b16 a[98:99], v10 offset:34816\n\t"
    "v_mul_f32 v128, v27, v128\n\t"
    "v_mfma_f32_16x16x32_bf16 v[140:143], a[136:139], v[92:95], v[140:143]\n\t"
    "s_waitcnt lgkmcnt(10)\n\t"
    "buffer_load_dwordx4 v19, s[24:27], s47 offen offset:3072 lds\n\t"
    "v_mfma_f32_16x16x32_bf16 v[140:143], a[140:143], v[88:91], v[140:143]\n\t"
    "s_waitcnt lgkmcnt(8)\n\t"
    "ds_read_b64_tr_b16 a[100:101], v10 offset:36864\n\t"
    "v_mul_f32 v129, v27, v129\n\t"
    "v_mfma_f32_16x16x32_bf16 v[140:143], a[144:147], v[68:71], v[140:143]\n\t"
    "s_waitcnt lgkmcnt(7)\n\t"
    "ds_read_b64_tr_b16 a[102:103], v10 offset:38912\n\t"
    "v_mul_f32 v130, v27, v130\n\t"
    "v_mfma_f32_16x16x32_bf16 v[140:143], a[148:151], v[44:47], v[140:143]\n\t"
    "s_waitcnt lgkmcnt(6)\n\t"
    "v_mul_f32 v131, v27, v131\n\t"
    "v_cvt_pk_bf16_f32 v128, v128, v129\n\t"
    "v_mfma_f32_16x16x32_bf16 v[140:143], a[152:155], v[64:67], v[140:143]\n\t"
    "s_waitcnt lgkmcnt(4)\n\t"
    "ds_read_b64_tr_b16 a[104:105], v10 offset:40960\n\t"
    "v_cvt_pk_bf16_f32 v129, v130, v131\n\t"
    "v_mfma_f32_16x16x32_bf16 v[140:143], a[156:159], v[104:107], v[140:143]\n\t"
    "ds_read_b64_tr_b16 a[106:107], v10 offset:43008\n\t"
    ".p2align 12\n\t"
    "Lstub6_%=:\n\t"
    "s_cbranch_execz Lret_%=\n\t"
    "v_mfma_f32_16x16x32_bf16 v[152:155], a[128:131], v[96:99], v[152:155]\n\t"
    "v_mfma_f32_16x16x32_bf16 v[152:155], a[132:135], v[108:111], v[152:155]\n\t"
    "ds_read_b64_tr_b16 a[108:109], v10 offset:45056\n\t"
    "v_mfma_f32_16x16x32_bf16 v[152:155], a[136:139], v[120:123], v[152:155]\n\t"
    "ds_read_b64_tr_b16 a[110:111], v10 offset:47104\n\t"
    "v_mfma_f32_16x16x32_bf16 v[152:155], a[140:143], v[132:135], v[152:155]\n\t"
    "v_mfma_f32_16x16x32_bf16 v[152:155], a[144:147], v[144:147], v[152:155]\n\t"
    "ds_read_b64_tr_b16 a[112:113], v10 offset:49152\n\t"
    "v_mul_f32 v140, v25, v140\n\t"
    "v_mfma_f32_16x16x32_bf16 v[152:155], a[148:151], v[156:159], v[152:155]\n\t"
    "ds_read_b64_tr_b16 a[114:115], v10 offset:51200\n\t"
    "v_mul_f32 v141, v25, v141\n\t"
    "v_mfma_f32_16x16x32_bf16 v[152:155], a[152:155], v[168:171], v[152:155]\n\t"
    "v_mul_f32 v142, v25, v142\n\t"
    "v_mul_f32 v143, v25, v143\n\t"
    "v_mfma_f32_16x16x32_bf16 v[152:155], a[156:159], v[180:183], v[152:155]\n\t"
    "ds_read_b64_tr_b16 a[116:117], v10 offset:53248\n\t"
    "v_cvt_pk_bf16_f32 v42, v140, v141\n\t"
    "v_mfma_f32_16x16x32_bf16 v[164:167], a[128:131], v[100:103], v[164:167]\n\t"
    "ds_read_b64_tr_b16 a[118:119], v10 offset:55296\n\t"
    "v_cvt_pk_bf16_f32 v43, v142, v143\n\t"
    "v_mfma_f32_16x16x32_bf16 v[164:167], a[132:135], v[112:115], v[164:167]\n\t"
    "v_permlane16_swap_b32 v40, v42\n\t"
    "v_mfma_f32_16x16x32_bf16 v[164:167], a[136:139], v[124:127], v[164:167]\n\t"
    "ds_read_b64_tr_b16 a[120:121], v10 offset:57344\n\t"
    "v_mul_f32 v152, v26, v152\n\t"
    "v_mfma_f32_16x16x32_bf16 v[164:167], a[140:143], v[136:139], v[164:167]\n\t"
    "ds_read_b64_tr_b16 a[122:123], v10 offset:59392\n\t"
    "v_mul_f32 v153, v26, v153\n\t"
    "v_mfma_f32_16x16x32_bf16 v[164:167], a[144:147], v[148:151], v[164:167]\n\t"
    "v_permlane16_swap_b32 v41, v43\n\t"
    "v_mfma_f32_16x16x32_bf16 v[164:167], a[148:151], v[160:163], v[164:167]\n\t"
    "ds_read_b64_tr_b16 a[124:125], v10 offset:61440\n\t"
    "v_mul_f32 v154, v26, v154\n\t"
    "v_mfma_f32_16x16x32_bf16 v[164:167], a[152:155], v[172:175], v[164:167]\n\t"
    "ds_read_b64_tr_b16 a[126:127], v10 offset:63488\n\t"
    "v_mul_f32 v155, v26, v155\n\t"
    "v_mfma_f32_16x16x32_bf16 v[164:167], a[156:159], v[184:187], v[164:167]\n\t"
    "s_waitcnt lgkmcnt(14)\n\t"
    "buffer_store_dwordx4 v[40:43], v21, s[36:39], s67 offen offset:0 sc1\n\t"
    "v_mfma_f32_16x16x32_bf16 v[176:179], a[96:99], v[76:79], v[176:179]\n\t"
    "s_waitcnt lgkmcnt(12)\n\t"
    "ds_read_b64_tr_b16 a[128:129], v11 offset:32768\n\t"
    "v_cvt_pk_bf16_f32 v118, v152, v153\n\t"
    "v_mfma_f32_16x16x32_bf16 v[176:179], a[100:103], v[48:51], v[176:179]\n\t"
    "s_waitcnt lgkmcnt(11)\n\t"
    "ds_read_b64_tr_b16 a[130:131], v11 offset:34816\n\t"
    "v_cvt_pk_bf16_f32 v119, v154, v155\n\t"
    "v_mfma_f32_16x16x32_bf16 v[176:179], a[104:107], v[92:95], v[176:179]\n\t"
    "s_waitcnt lgkmcnt(10)\n\t"
    "v_permlane16_swap_b32 v116, v118\n\t"
    "v_mfma_f32_16x16x32_bf16 v[176:179], a[108:111], v[88:91], v[176:179]\n\t"
    "s_waitcnt lgkmcnt(8)\n\t"
    "ds_read_b64_tr_b16 a[132:133], v11 offset:36864\n\t"
    "v_mul_f32 v164, v27, v164\n\t"
    "v_mfma_f32_16x16x32_bf16 v[176:179], a[112:115], v[68:71], v[176:179]\n\t"
    "s_waitcnt lgkmcnt(7)\n\t"
    "ds_read_b64_tr_b16 a[134:135], v11 offset:38912\n\t"
    "v_mul_f32 v165, v27, v165\n\t"
    "v_mfma_f32_16x16x32_bf16 v[176:179], a[116:119], v[44:47], v[176:179]\n\t"
    "s_waitcnt lgkmcnt(6)\n\t"
    "v_permlane16_swap_b32 v117, v119\n\t"
    "v_mfma_f32_16x16x32_bf16 v[176:179], a[120:123], v[64:67], v[176:179]\n\t"
    "s_waitcnt lgkmcnt(4)\n\t"
    "ds_read_b64_tr_b16 a[136:137], v11 offset:40960\n\t"
    "v_mul_f32 v166, v27, v166\n\t"
    "v_mfma_f32_16x16x32_bf16 v[176:179], a[124:127], v[104:107], v[176:179]\n\t"
    "ds_read_b64_tr_b16 a[138:139], v11 offset:43008\n\t"
    "v_mul_f32 v167, v27, v167\n\t"
    "v_mfma_f32_16x16x32_bf16 v[188:191], a[96:99], v[96:99], v[188:191]\n\t"
    "buffer_store_dwordx4 v[116:119], v21, s[36:39], s68 offen offset:0 sc1\n\t"
    "v_mfma_f32_16x16x32_bf16 v[188:191], a[100:103], v[108:111], v[188:191]\n\t"
    "ds_read_b64_tr_b16 a[140:141], v11 offset:45056\n\t"
    "v_cvt_pk_bf16_f32 v130, v164, v165\n\t"
    "v_mfma_f32_16x16x32_bf16 v[188:191], a[104:107], v[120:123], v[188:191]\n\t"
    "ds_read_b64_tr_b16 a[142:143], v11 offset:47104\n\t"
    "v_cvt_pk_bf16_f32 v131, v166, v167\n\t"
    "v_mfma_f32_16x16x32_bf16 v[188:191], a[108:111], v[132:135], v[188:191]\n\t"
    "v_permlane16_swap_b32 v128, v130\n\t"
    "v_mfma_f32_16x16x32_bf16 v[188:191], a[112:115], v[144:147], v[188:191]\n\t"
    "ds_read_b64_tr_b16 a[144:145], v11 offset:49152\n\t"
    "v_mul_f32 v176, v25, v176\n\t"
    "v_mfma_f32_16x16x32_bf16 v[188:191], a[116:119], v[156:159], v[188:191]\n\t"
    "ds_read_b64_tr_b16 a[146:147], v11 offset:51200\n\t"
    "v_mul_f32 v177, v25, v177\n\t"
    "v_mfma_f32_16x16x32_bf16 v[188:191], a[120:123], v[168:171], v[188:191]\n\t"
    "v_permlane16_swap_b32 v129, v131\n\t"
    "v_mfma_f32_16x16x32_bf16 v[188:191], a[124:127], v[180:183], v[188:191]\n\t"
    "ds_read_b64_tr_b16 a[148:149], v11 offset:53248\n\t"
    "v_mul_f32 v178, v25, v178\n\t"
    "v_mfma_f32_16x16x32_bf16 v[192:195], a[96:99], v[100:103], v[192:195]\n\t"
    "ds_read_b64_tr_b16 a[150:151], v11 offset:55296\n\t"
    "v_mul_f32 v179, v25, v179\n\t"
    "v_mfma_f32_16x16x32_bf16 v[192:195], a[100:103], v[112:115], v[192:195]\n\t"
    "buffer_store_dwordx4 v[128:131], v21, s[36:39], s69 offen offset:0 sc1\n\t"
    "v_mfma_f32_16x16x32_bf16 v[192:195], a[104:107], v[124:127], v[192:195]\n\t"
    "ds_read_b64_tr_b16 a[152:153], v11 offset:57344\n\t"
    "v_cvt_pk_bf16_f32 v176, v176, v177\n\t"
    "v_mfma_f32_16x16x32_bf16 v[192:195], a[108:111], v[136:139], v[192:195]\n\t"
    "ds_read_b64_tr_b16 a[154:155], v11 offset:59392\n\t"
    "v_cvt_pk_bf16_f32 v177, v178, v179\n\t"
    "v_mfma_f32_16x16x32_bf16 v[192:195], a[112:115], v[148:151], v[192:195]\n\t"
    "s_waitcnt vmcnt(3)\n\t"
    "s_barrier\n\t"
    "v_mul_f32 v188, v26, v188\n\t"
    "v_mfma_f32_16x16x32_bf16 v[192:195], a[116:119], v[160:163], v[192:195]\n\t"
    "s_or_b64 s[28:29], s[28:29], s[30:31]\n\t"
    "ds_read_b64_tr_b16 a[156:157], v11 offset:61440\n\t"
    "v_mul_f32 v189, v26, v189\n\t"
    "v_mfma_f32_16x16x32_bf16 v[192:195], a[120:123], v[172:175], v[192:195]\n\t"
    "s_or_b64 s[28:29], s[28:29], s[58:59]\n\t"
    "ds_read_b64_tr_b16 a[158:159], v11 offset:63488\n\t"
    "v_mul_f32 v190, v26, v190\n\t"
    "v_mfma_f32_16x16x32_bf16 v[192:195], a[124:127], v[184:187], v[192:195]\n\t"
    "s_waitcnt lgkmcnt(14)\n\t"
    "v_mul_f32 v191, v26, v191\n\t"
    "v_cvt_pk_bf16_f32 v188, v188, v189\n\t"
    "v_mfma_f32_16x16x32_bf16 v[196:199], a[128:131], v[76:79], v[196:199]\n\t"
    "s_waitcnt lgkmcnt(12)\n\t"
    "ds_read_b64_tr_b16 a[96:97], v4 offset:0\n\t"
    "v_cvt_pk_bf16_f32 v189, v190, v191\n\t"
    "v_mfma_f32_16x16x32_bf16 v[196:199], a[132:135], v[48:51], v[196:199]\n\t"
    "s_waitcnt lgkmcnt(11)\n\t"
    "ds_read_b64_tr_b16 a[98:99], v4 offset:2048\n\t"
    "v_mul_f32 v192, v27, v192\n\t"
    "v_mfma_f32_16x16x32_bf16 v[196:199], a[136:139], v[92:95], v[196:199]\n\t"
    "s_waitcnt lgkmcnt(10)\n\t"
    "v_mul_f32 v193, v27, v193\n\t"
    "v_mul_f32 v194, v27, v194\n\t"
    "v_mfma_f32_16x16x32_bf16 v[196:199], a[140:143], v[88:91], v[196:199]\n\t"
    "s_waitcnt lgkmcnt(8)\n\t"
    "ds_read_b64_tr_b16 a[100:101], v4 offset:4096\n\t"
    "v_mul_f32 v195, v27, v195\n\t"
    "v_mfma_f32_16x16x32_bf16 v[196:199], a[144:147], v[68:71], v[196:199]\n\t"
    "s_waitcnt lgkmcnt(7)\n\t"
    "ds_read_b64_tr_b16 a[102:103], v4 offset:6144\n\t"
    "v_cvt_pk_bf16_f32 v192, v192, v193\n\t"
    "v_mfma_f32_16x16x32_bf16 v[196:199], a[148:151], v[44:47], v[196:199]\n\t"
    "s_waitcnt lgkmcnt(6)\n\t"
    "v_cvt_pk_bf16_f32 v193, v194, v195\n\t"
    "v_mfma_f32_16x16x32_bf16 v[196:199], a[152:155], v[64:67], v[196:199]\n\t"
    "s_waitcnt lgkmcnt(4)\n\t"
    "ds_read_b64_tr_b16 a[104:105], v4 offset:8192\n\t"
    "v_mfma_f32_16x16x32_bf16 v[196:199], a[156:159], v[104:107], v[196:199]\n\t"
    "s_or_b32 s28, s28, s29\n\t"
    "ds_read_b64_tr_b16 a[106:107], v4 offset:10240\n\t"
    "v_mov_b32 v28, s28\n\t"
    "v_mfma_f32_16x16x32_bf16 v[200:203], a[128:131], v[96:99], v[200:203]\n\t"
    "s_lshl2_add_u32 s56, s7, s57\n\t"
    "v_mov_b32 v30, s57\n\t"
    "v_mov_b32 v29, s56\n\t"
    "v_mfma_f32_16x16x32_bf16 v[200:203], a[132:135], v[108:111], v[200:203]\n\t"
    "ds_read_b64_tr_b16 a[108:109], v4 offset:12288\n\t"
    "v_mul_f32 v196, v25, v196\n\t"
    "v_mfma_f32_16x16x32_bf16 v[200:203], a[136:139], v[120:123], v[200:203]\n\t"
    "ds_read_b64_tr_b16 a[110:111], v4 offset:14336\n\t"
    "v_mul_f32 v197, v25, v197\n\t"
    "v_mfma_f32_16x16x32_bf16 v[200:203], a[140:143], v[132:135], v[200:203]\n\t"
    "ds_write_b32 v29, v28 offset:32768\n\t"
    "v_mfma_f32_16x16x32_bf16 v[200:203], a[144:147], v[144:147], v[200:203]\n\t"
    "ds_read_b64_tr_b16 a[112:113], v4 offset:16384\n\t"
    "v_mul_f32 v198, v25, v198\n\t"
    "v_mfma_f32_16x16x32_bf16 v[200:203], a[148:151], v[156:159], v[200:203]\n\t"
    "ds_read_b64_tr_b16 a[114:115], v4 offset:18432\n\t"
    "v_mul_f32 v199, v25, v199\n\t"
    "v_mfma_f32_16x16x32_bf16 v[200:203], a[152:155], v[168:171], v[200:203]\n\t"
    "v_cvt_pk_bf16_f32 v178, v196, v197\n\t"
    "v_cvt_pk_bf16_f32 v179, v198, v199\n\t"
    "v_mfma_f32_16x16x32_bf16 v[200:203], a[156:159], v[180:183], v[200:203]\n\t"
    "ds_read_b64_tr_b16 a[116:117], v4 offset:20480\n\t"
    "v_mfma_f32_16x16x32_bf16 v[212:215], a[128:131], v[100:103], v[212:215]\n\t"
    "ds_read_b64_tr_b16 a[118:119], v4 offset:22528\n\t"
    "v_mfma_f32_16x16x32_bf16 v[212:215], a[132:135], v[112:115], v[212:215]\n\t"
    "v_permlane16_swap_b32 v176, v178\n\t"
    "v_mfma_f32_16x16x32_bf16 v[212:215], a[136:139], v[124:127], v[212:215]\n\t"
    "ds_read_b64_tr_b16 a[120:121], v4 offset:24576\n\t"
    "v_mfma_f32_16x16x32_bf16 v[212:215], a[140:143], v[136:139], v[212:215]\n\t"
    "ds_read_b64_tr_b16 a[122:123], v4 offset:26624\n\t"
    "v_mul_f32 v200, v26, v200\n\t"
    "v_mfma_f32_16x16x32_bf16 v[212:215], a[144:147], v[148:151], v[212:215]\n\t"
    "v_permlane16_swap_b32 v177, v179\n\t"
    "v_mfma_f32_16x16x32_bf16 v[212:215], a[148:151], v[160:163], v[212:215]\n\t"
    "ds_read_b64_tr_b16 a[124:125], v4 offset:28672\n\t"
    "v_mul_f32 v201, v26, v201\n\t"
    "v_mfma_f32_16x16x32_bf16 v[212:215], a[152:155], v[172:175], v[212:215]\n\t"
    "ds_read_b64_tr_b16 a[126:127], v4 offset:30720\n\t"
    "v_mul_f32 v202, v26, v202\n\t"
    "v_mfma_f32_16x16x32_bf16 v[212:215], a[156:159], v[184:187], v[212:215]\n\t"
    "s_waitcnt lgkmcnt(15)\n\t"
    "buffer_store_dwordx4 v[176:179], v21, s[36:39], s67 offen offset:64 sc1\n\t"
    "v_mfma_f32_16x16x32_bf16 v[40:43], a[96:99], a[176:179], 0\n\t"
    "s_waitcnt lgkmcnt(13)\n\t"
    "ds_read_b64_tr_b16 a[128:129], v5 offset:0\n\t"
    "v_mul_f32 v203, v26, v203\n\t"
    "v_mfma_f32_16x16x32_bf16 v[40:43], a[100:103], a[188:191], v[40:43]\n\t"
    "s_waitcnt lgkmcnt(12)\n\t"
    "ds_read_b64_tr_b16 a[130:131], v5 offset:2048\n\t"
    "v_cvt_pk_bf16_f32 v190, v200, v201\n\t"
    "v_mfma_f32_16x16x32_bf16 v[40:43], a[104:107], a[200:203], v[40:43]\n\t"
    "s_waitcnt lgkmcnt(11)\n\t"
    "v_permlane16_swap_b32 v188, v190\n\t"
    "v_mfma_f32_16x16x32_bf16 v[40:43], a[108:111], a[212:215], v[40:43]\n\t"
    "s_waitcnt lgkmcnt(8)\n\t"
    "ds_read_b64_tr_b16 a[132:133], v5 offset:4096\n\t"
    "v_cvt_pk_bf16_f32 v191, v202, v203\n\t"
    "v_mfma_f32_16x16x32_bf16 v[40:43], a[112:115], v[228:231], v[40:43]\n\t"
    "s_waitcnt lgkmcnt(7)\n\t"
    "ds_read_b64_tr_b16 a[134:135], v5 offset:6144\n\t"
    "v_mul_f32 v212, v27, v212\n\t"
    "v_mfma_f32_16x16x32_bf16 v[40:43], a[116:119], v[252:255], v[40:43]\n\t"
    "s_waitcnt lgkmcnt(6)\n\t"
    "v_permlane16_swap_b32 v189, v191\n\t"
    "v_mfma_f32_16x16x32_bf16 v[40:43], a[120:123], v[80:83], v[40:43]\n\t"
    "s_waitcnt lgkmcnt(4)\n\t"
    "ds_read_b64_tr_b16 a[136:137], v5 offset:8192\n\t"
    "v_mul_f32 v213, v27, v213\n\t"
    "v_mfma_f32_16x16x32_bf16 v[40:43], a[124:127], v[52:55], v[40:43]\n\t"
    "ds_read_b64_tr_b16 a[138:139], v5 offset:10240\n\t"
    "v_mul_f32 v214, v27, v214\n\t"
    "v_mfma_f32_16x16x32_bf16 v[116:119], a[96:99], a[180:183], 0\n\t"
    "buffer_store_dwordx4 v[188:191], v21, s[36:39], s68 offen offset:64 sc1\n\t"
    "v_mfma_f32_16x16x32_bf16 v[116:119], a[100:103], a[192:195], v[116:119]\n\t"
    "ds_read_b64_tr_b16 a[140:141], v5 offset:12288\n\t"
    "v_mul_f32 v215, v27, v215\n\t"
    "v_mfma_f32_16x16x32_bf16 v[116:119], a[104:107], a[204:207], v[116:119]\n\t"
    "ds_read_b64_tr_b16 a[142:143], v5 offset:14336\n\t"
    "v_cvt_pk_bf16_f32 v194, v212, v213\n\t"
    "v_mfma_f32_16x16x32_bf16 v[116:119], a[108:111], a[216:219], v[116:119]\n\t"
    "v_cvt_pk_bf16_f32 v195, v214, v215\n\t"
    "v_mfma_f32_16x16x32_bf16 v[116:119], a[112:115], v[204:207], v[116:119]\n\t"
    "ds_read_b64_tr_b16 a[144:145], v5 offset:16384\n\t"
    "v_mfma_f32_16x16x32_bf16 v[116:119], a[116:119], v[208:211], v[116:119]\n\t"
    "ds_read_b64_tr_b16 a[146:147], v5 offset:18432\n\t"
    "v_mfma_f32_16x16x32_bf16 v[116:119], a[120:123], v[36:39], v[116:119]\n\t"
    "v_permlane16_swap_b32 v192, v194\n\t"
    "v_mfma_f32_16x16x32_bf16 v[116:119], a[124:127], v[56:59], v[116:119]\n\t"
    "ds_read_b64_tr_b16 a[148:149], v5 offset:20480\n\t"
    "v_mfma_f32_16x16x32_bf16 v[128:131], a[96:99], a[184:187], 0\n\t"
    "ds_read_b64_tr_b16 a[150:151], v5 offset:22528\n\t"
    "v_mfma_f32_16x16x32_bf16 v[128:131], a[100:103], a[196:199], v[128:131]\n\t"
    "v_permlane16_swap_b32 v193, v195\n\t"
    "v_mfma_f32_16x16x32_bf16 v[128:131], a[104:107], a[208:211], v[128:131]\n\t"
    "ds_read_b64_tr_b16 a[152:153], v5 offset:24576\n\t"
    "v_mfma_f32_16x16x32_bf16 v[128:131], a[108:111], a[220:223], v[128:131]\n\t"
    "ds_read_b64_tr_b16 a[154:155], v5 offset:26624\n\t"
    "v_mfma_f32_16x16x32_bf16 v[128:131], a[112:115], v[32:35], v[128:131]\n\t"
    "buffer_store_dwordx4 v[192:195], v21, s[36:39], s69 offen offset:64 sc1\n\t"
    "v_mfma_f32_16x16x32_bf16 v[128:131], a[116:119], v[72:75], v[128:131]\n\t"
    "ds_read_b64_tr_b16 a[156:157], v5 offset:28672\n\t"
    "v_mfma_f32_16x16x32_bf16 v[128:131], a[120:123], v[84:87], v[128:131]\n\t"
    "ds_read_b64_tr_b16 a[158:159], v5 offset:30720\n\t"
    "v_mfma_f32_16x16x32_bf16 v[128:131], a[124:127], v[60:63], v[128:131]\n\t"
    "s_waitcnt lgkmcnt(14)\n\t"
    "v_mfma_f32_16x16x32_bf16 v[140:143], a[128:131], a[176:179], 0\n\t"
    "s_waitcnt lgkmcnt(12)\n\t"
    "ds_read_b64_tr_b16 a[96:97], v6 offset:0\n\t"
    "v_mfma_f32_16x16x32_bf16 v[140:143], a[132:135], a[188:191], v[140:143]\n\t"
    "s_waitcnt lgkmcnt(11)\n\t"
    "ds_read_b64_tr_b16 a[98:99], v6 offset:2048\n\t"
    "v_mfma_f32_16x16x32_bf16 v[140:143], a[136:139], a[200:203], v[140:143]\n\t"
    "s_waitcnt lgkmcnt(10)\n\t"
    "v_mfma_f32_16x16x32_bf16 v[140:143], a[140:143], a[212:215], v[140:143]\n\t"
    "s_waitcnt lgkmcnt(8)\n\t"
    "ds_read_b64_tr_b16 a[100:101], v6 offset:4096\n\t"
    "v_mfma_f32_16x16x32_bf16 v[140:143], a[144:147], v[228:231], v[140:143]\n\t"
    "s_waitcnt lgkmcnt(7)\n\t"
    "ds_read_b64_tr_b16 a[102:103], v6 offset:6144\n\t"
    "v_mfma_f32_16x16x32_bf16 v[140:143], a[148:151], v[252:255], v[140:143]\n\t"
    "s_waitcnt lgkmcnt(6)\n\t"
    "v_mfma_f32_16x16x32_bf16 v[140:143], a[152:155], v[80:83], v[140:143]\n\t"
    "s_waitcnt lgkmcnt(4)\n\t"
    "ds_read_b64_tr_b16 a[104:105], v6 offset:8192\n\t"
    "v_mfma_f32_16x16x32_bf16 v[140:143], a[156:159], v[52:55], v[140:143]\n\t"
    "ds_read_b64_tr_b16 a[106:107], v6 offset:10240\n\t"
    "v_mfma_f32_16x16x32_bf16 v[152:155], a[128:131], a[180:183], 0\n\t"
    "v_mfma_f32_16x16x32_bf16 v[152:155], a[132:135], a[192:195], v[152:155]\n\t"
    "ds_read_b64_tr_b16 a[108:109], v6 offset:12288\n\t"
    "v_mfma_f32_16x16x32_bf16 v[152:155], a[136:139], a[204:207], v[152:155]\n\t"
    "ds_read_b64_tr_b16 a[110:111], v6 offset:14336\n\t"
    "v_mfma_f32_16x16x32_bf16 v[152:155], a[140:143], a[216:219], v[152:155]\n\t"
    "v_mfma_f32_16x16x32_bf16 v[152:155], a[144:147], v[204:207], v[152:155]\n\t"
    "ds_read_b64_tr_b16 a[112:113], v6 offset:16384\n\t"
    "v_mfma_f32_16x16x32_bf16 v[152:155], a[148:151], v[208:211], v[152:155]\n\t"
    "s_add_u32 m0, s40, 131072\n\t"
    "ds_read_b64_tr_b16 a[114:115], v6 offset:18432\n\t"
    "v_mfma_f32_16x16x32_bf16 v[152:155], a[152:155], v[36:39], v[152:155]\n\t"
    "s_add_u32 s46, s45, 0\n\t"
    "v_mfma_f32_16x16x32_bf16 v[152:155], a[156:159], v[56:59], v[152:155]\n\t"
    "ds_read_b64_tr_b16 a[116:117], v6 offset:20480\n\t"
    "v_mfma_f32_16x16x32_bf16 v[164:167], a[128:131], a[184:187], 0\n\t"
    "ds_read_b64_tr_b16 a[118:119], v6 offset:22528\n\t"
    "v_mfma_f32_16x16x32_bf16 v[164:167], a[132:135], a[196:199], v[164:167]\n\t"
    "buffer_load_dwordx4 v16, s[24:27], s46 offen offset:0 lds\n\t"
    "v_mfma_f32_16x16x32_bf16 v[164:167], a[136:139], a[208:211], v[164:167]\n\t"
    "ds_read_b64_tr_b16 a[120:121], v6 offset:24576\n\t"
    "v_mfma_f32_16x16x32_bf16 v[164:167], a[140:143], a[220:223], v[164:167]\n\t"
    "ds_read_b64_tr_b16 a[122:123], v6 offset:26624\n\t"
    "v_mfma_f32_16x16x32_bf16 v[164:167], a[144:147], v[32:35], v[164:167]\n\t"
    "v_mfma_f32_16x16x32_bf16 v[164:167], a[148:151], v[72:75], v[164:167]\n\t"
    "ds_read_b64_tr_b16 a[124:125], v6 offset:28672\n\t"
    "v_mfma_f32_16x16x32_bf16 v[164:167], a[152:155], v[84:87], v[164:167]\n\t"
    "ds_read_b64_tr_b16 a[126:127], v6 offset:30720\n\t"
    "v_mfma_f32_16x16x32_bf16 v[164:167], a[156:159], v[60:63], v[164:167]\n\t"
    "s_waitcnt lgkmcnt(14)\n\t"
    "buffer_load_dwordx4 v17, s[24:27], s46 offen offset:1024 lds\n\t"
    "v_mfma_f32_16x16x32_bf16 v[176:179], a[96:99], a[176:179], 0\n\t"
    "s_waitcnt lgkmcnt(12)\n\t"
    "ds_read_b64_tr_b16 a[128:129], v7 offset:0\n\t"
    "v_mfma_f32_16x16x32_bf16 v[176:179], a[100:103], a[188:191], v[176:179]\n\t"
    "s_waitcnt lgkmcnt(11)\n\t"
    "ds_read_b64_tr_b16 a[130:131], v7 offset:2048\n\t"
    "v_mfma_f32_16x16x32_bf16 v[176:179], a[104:107], a[200:203], v[176:179]\n\t"
    "s_waitcnt lgkmcnt(10)\n\t"
    "buffer_load_dwordx4 v18, s[24:27], s46 offen offset:2048 lds\n\t"
    "v_mfma_f32_16x16x32_bf16 v[176:179], a[108:111], a[212:215], v[176:179]\n\t"
    "s_waitcnt lgkmcnt(8)\n\t"
    "ds_read_b64_tr_b16 a[132:133], v7 offset:4096\n\t"
    "v_mfma_f32_16x16x32_bf16 v[176:179], a[112:115], v[228:231], v[176:179]\n\t"
    "s_waitcnt lgkmcnt(7)\n\t"
    "ds_read_b64_tr_b16 a[134:135], v7 offset:6144\n\t"
    "v_mfma_f32_16x16x32_bf16 v[176:179], a[116:119], v[252:255], v[176:179]\n\t"
    "s_waitcnt lgkmcnt(6)\n\t"
    "v_mfma_f32_16x16x32_bf16 v[176:179], a[120:123], v[80:83], v[176:179]\n\t"
    "s_waitcnt lgkmcnt(4)\n\t"
    "ds_read_b64_tr_b16 a[136:137], v7 offset:8192\n\t"
    "v_mfma_f32_16x16x32_bf16 v[176:179], a[124:127], v[52:55], v[176:179]\n\t"
    "ds_read_b64_tr_b16 a[138:139], v7 offset:10240\n\t"
    "v_mfma_f32_16x16x32_bf16 v[188:191], a[96:99], a[180:183], 0\n\t"
    "buffer_load_dwordx4 v19, s[24:27], s46 offen offset:3072 lds\n\t"
    "v_mfma_f32_16x16x32_bf16 v[188:191], a[100:103], a[192:195], v[188:191]\n\t"
    "ds_read_b64_tr_b16 a[140:141], v7 offset:12288\n\t"
    "v_mfma_f32_16x16x32_bf16 v[188:191], a[104:107], a[204:207], v[188:191]\n\t"
    "s_add_u32 m0, s40, 135168\n\t"
    "ds_read_b64_tr_b16 a[142:143], v7 offset:14336\n\t"
    "v_mfma_f32_16x16x32_bf16 v[188:191], a[108:111], a[216:219], v[188:191]\n\t"
    "s_add_u32 s47, s45, 16384\n\t"
    "v_mfma_f32_16x16x32_bf16 v[188:191], a[112:115], v[204:207], v[188:191]\n\t"
    "ds_read_b64_tr_b16 a[144:145], v7 offset:16384\n\t"
    "v_mfma_f32_16x16x32_bf16 v[188:191], a[116:119], v[208:211], v[188:191]\n\t"
    "ds_read_b64_tr_b16 a[146:147], v7 offset:18432\n\t"
    "v_mfma_f32_16x16x32_bf16 v[188:191], a[120:123], v[36:39], v[188:191]\n\t"
    "buffer_load_dwordx4 v16, s[24:27], s47 offen offset:0 lds\n\t"
    "v_mfma_f32_16x16x32_bf16 v[188:191], a[124:127], v[56:59], v[188:191]\n\t"
    "ds_read_b64_tr_b16 a[148:149], v7 offset:20480\n\t"
    "v_mfma_f32_16x16x32_bf16 v[192:195], a[96:99], a[184:187], 0\n\t"
    "ds_read_b64_tr_b16 a[150:151], v7 offset:22528\n\t"
    "v_mfma_f32_16x16x32_bf16 v[192:195], a[100:103], a[196:199], v[192:195]\n\t"
    "v_mfma_f32_16x16x32_bf16 v[192:195], a[104:107], a[208:211], v[192:195]\n\t"
    "ds_read_b64_tr_b16 a[152:153], v7 offset:24576\n\t"
    "v_mfma_f32_16x16x32_bf16 v[192:195], a[108:111], a[220:223], v[192:195]\n\t"
    "ds_read_b64_tr_b16 a[154:155], v7 offset:26624\n\t"
    "v_mfma_f32_16x16x32_bf16 v[192:195], a[112:115], v[32:35], v[192:195]\n\t"
    "buffer_load_dwordx4 v17, s[24:27], s47 offen offset:1024 lds\n\t"
    "v_mfma_f32_16x16x32_bf16 v[192:195], a[116:119], v[72:75], v[192:195]\n\t"
    "ds_read_b64_tr_b16 a[156:157], v7 offset:28672\n\t"
    "v_mfma_f32_16x16x32_bf16 v[192:195], a[120:123], v[84:87], v[192:195]\n\t"
    "ds_read_b64_tr_b16 a[158:159], v7 offset:30720\n\t"
    "v_mfma_f32_16x16x32_bf16 v[192:195], a[124:127], v[60:63], v[192:195]\n\t"
    "s_waitcnt lgkmcnt(14)\n\t"
    "v_mfma_f32_16x16x32_bf16 v[196:199], a[128:131], a[176:179], 0\n\t"
    "s_waitcnt lgkmcnt(12)\n\t"
    "v_add_u32 v8, 0x10000, v8\n\t"
    "v_add_u32 v9, 0x10000, v9\n\t"
    "v_add_u32 v10, 0x10000, v10\n\t"
    "v_add_u32 v11, 0x10000, v11\n\t"
    "v_mfma_f32_16x16x32_bf16 v[196:199], a[132:135], a[188:191], v[196:199]\n\t"
    "s_waitcnt lgkmcnt(10)\n\t"
    "ds_read_b64_tr_b16 a[96:97], v8 offset:0\n\t"
    "v_mfma_f32_16x16x32_bf16 v[196:199], a[136:139], a[200:203], v[196:199]\n\t"
    "s_waitcnt lgkmcnt(9)\n\t"
    "ds_read_b64_tr_b16 a[98:99], v8 offset:2048\n\t"
    "buffer_load_dwordx4 v18, s[24:27], s47 offen offset:2048 lds\n\t"
    "v_mfma_f32_16x16x32_bf16 v[196:199], a[140:143], a[212:215], v[196:199]\n\t"
    "s_waitcnt lgkmcnt(8)\n\t"
    "ds_read_b64_tr_b16 a[100:101], v8 offset:4096\n\t"
    "v_mfma_f32_16x16x32_bf16 v[196:199], a[144:147], v[228:231], v[196:199]\n\t"
    "s_waitcnt lgkmcnt(7)\n\t"
    "ds_read_b64_tr_b16 a[102:103], v8 offset:6144\n\t"
    "v_mfma_f32_16x16x32_bf16 v[196:199], a[148:151], v[252:255], v[196:199]\n\t"
    "s_waitcnt lgkmcnt(6)\n\t"
    "buffer_load_dwordx4 v19, s[24:27], s47 offen offset:3072 lds\n\t"
    "v_mfma_f32_16x16x32_bf16 v[196:199], a[152:155], v[80:83], v[196:199]\n\t"
    "s_waitcnt lgkmcnt(4)\n\t"
    "ds_read_b64_tr_b16 a[104:105], v8 offset:8192\n\t"
    "v_mfma_f32_16x16x32_bf16 v[196:199], a[156:159], v[52:55], v[196:199]\n\t"
    "ds_read_b64_tr_b16 a[106:107], v8 offset:10240\n\t"
    "v_mfma_f32_16x16x32_bf16 v[200:203], a[128:131], a[180:183], 0\n\t"
    "v_mfma_f32_16x16x32_bf16 v[200:203], a[132:135], a[192:195], v[200:203]\n\t"
    "ds_read_b64_tr_b16 a[108:109], v8 offset:12288\n\t"
    "v_mfma_f32_16x16x32_bf16 v[200:203], a[136:139], a[204:207], v[200:203]\n\t"
    "s_add_u32 m0, s40, 0\n\t"
    "ds_read_b64_tr_b16 a[110:111], v8 offset:14336\n\t"
    "v_mfma_f32_16x16x32_bf16 v[200:203], a[140:143], a[216:219], v[200:203]\n\t"
    "s_add_u32 s46, s45, 131072\n\t"
    "v_mfma_f32_16x16x32_bf16 v[200:203], a[144:147], v[204:207], v[200:203]\n\t"
    "ds_read_b64_tr_b16 a[112:113], v8 offset:16384\n\t"
    "v_mfma_f32_16x16x32_bf16 v[200:203], a[148:151], v[208:211], v[200:203]\n\t"
    "ds_read_b64_tr_b16 a[114:115], v8 offset:18432\n\t"
    "v_mfma_f32_16x16x32_bf16 v[200:203], a[152:155], v[36:39], v[200:203]\n\t"
    "buffer_load_dwordx4 v16, s[24:27], s46 offen offset:0 lds\n\t"
    "v_mfma_f32_16x16x32_bf16 v[200:203], a[156:159], v[56:59], v[200:203]\n\t"
    "ds_read_b64_tr_b16 a[116:117], v8 offset:20480\n\t"
    "v_mfma_f32_16x16x32_bf16 v[212:215], a[128:131], a[184:187], 0\n\t"
    "ds_read_b64_tr_b16 a[118:119], v8 offset:22528\n\t"
    "v_mfma_f32_16x16x32_bf16 v[212:215], a[132:135], a[196:199], v[212:215]\n\t"
    "v_mfma_f32_16x16x32_bf16 v[212:215], a[136:139], a[208:211], v[212:215]\n\t"
    "ds_read_b64_tr_b16 a[120:121], v8 offset:24576\n\t"
    "v_mfma_f32_16x16x32_bf16 v[212:215], a[140:143], a[220:223], v[212:215]\n\t"
    "ds_read_b64_tr_b16 a[122:123], v8 offset:26624\n\t"
    "v_mfma_f32_16x16x32_bf16 v[212:215], a[144:147], v[32:35], v[212:215]\n\t"
    "buffer_load_dwordx4 v17, s[24:27], s46 offen offset:1024 lds\n\t"
    "v_mfma_f32_16x16x32_bf16 v[212:215], a[148:151], v[72:75], v[212:215]\n\t"
    "ds_read_b64_tr_b16 a[124:125], v8 offset:28672\n\t"
    "v_mfma_f32_16x16x32_bf16 v[212:215], a[152:155], v[84:87], v[212:215]\n\t"
    "ds_read_b64_tr_b16 a[126:127], v8 offset:30720\n\t"
    "v_mfma_f32_16x16x32_bf16 v[212:215], a[156:159], v[60:63], v[212:215]\n\t"
    "s_waitcnt lgkmcnt(14)\n\t"
    "buffer_load_dwordx4 v18, s[24:27], s46 offen offset:2048 lds\n\t"
    "v_mfma_f32_16x16x32_bf16 v[40:43], a[96:99], v[76:79], v[40:43]\n\t"
    "s_waitcnt lgkmcnt(12)\n\t"
    "ds_read_b64_tr_b16 a[128:129], v9 offset:0\n\t"
    "v_mfma_f32_16x16x32_bf16 v[40:43], a[100:103], v[48:51], v[40:43]\n\t"
    "s_waitcnt lgkmcnt(11)\n\t"
    "ds_read_b64_tr_b16 a[130:131], v9 offset:2048\n\t"
    "v_mfma_f32_16x16x32_bf16 v[40:43], a[104:107], v[92:95], v[40:43]\n\t"
    "s_waitcnt lgkmcnt(10)\n\t"
    "v_mfma_f32_16x16x32_bf16 v[40:43], a[108:111], v[88:91], v[40:43]\n\t"
    "s_waitcnt lgkmcnt(8)\n\t"
    "ds_read_b64_tr_b16 a[132:133], v9 offset:4096\n\t"
    "v_mfma_f32_16x16x32_bf16 v[40:43], a[112:115], v[68:71], v[40:43]\n\t"
    "s_waitcnt lgkmcnt(7)\n\t"
    "ds_read_b64_tr_b16 a[134:135], v9 offset:6144\n\t"
    "v_mfma_f32_16x16x32_bf16 v[40:43], a[116:119], v[44:47], v[40:43]\n\t"
    "s_waitcnt lgkmcnt(6)\n\t"
    "buffer_load_dwordx4 v19, s[24:27], s46 offen offset:3072 lds\n\t"
    "v_mfma_f32_16x16x32_bf16 v[40:43], a[120:123], v[64:67], v[40:43]\n\t"
    "s_waitcnt lgkmcnt(4)\n\t"
    "ds_read_b64_tr_b16 a[136:137], v9 offset:8192\n\t"
    "v_mfma_f32_16x16x32_bf16 v[40:43], a[124:127], v[104:107], v[40:43]\n\t"
    "s_add_u32 m0, s40, 4096\n\t"
    "ds_read_b64_tr_b16 a[138:139], v9 offset:10240\n\t"
    "v_mfma_f32_16x16x32_bf16 v[116:119], a[96:99], v[96:99], v[116:119]\n\t"
    "s_add_u32 s47, s45, 147456\n\t"
    "v_mfma_f32_16x16x32_bf16 v[116:119], a[100:103], v[108:111], v[116:119]\n\t"
    "ds_read_b64_tr_b16 a[140:141], v9 offset:12288\n\t"
    "v_mfma_f32_16x16x32_bf16 v[116:119], a[104:107], v[120:123], v[116:119]\n\t"
    "ds_read_b64_tr_b16 a[142:143], v9 offset:14336\n\t"
    "v_mul_f32 v40, v25, v40\n\t"
    "v_mfma_f32_16x16x32_bf16 v[116:119], a[108:111], v[132:135], v[116:119]\n\t"
    "buffer_load_dwordx4 v16, s[24:27], s47 offen offset:0 lds\n\t"
    "v_mfma_f32_16x16x32_bf16 v[116:119], a[112:115], v[144:147], v[116:119]\n\t"
    "ds_read_b64_tr_b16 a[144:145], v9 offset:16384\n\t"
    "v_mul_f32 v41, v25, v41\n\t"
    "v_mfma_f32_16x16x32_bf16 v[116:119], a[116:119], v[156:159], v[116:119]\n\t"
    "ds_read_b64_tr_b16 a[146:147], v9 offset:18432\n\t"
    "v_mul_f32 v42, v25, v42\n\t"
    "v_mfma_f32_16x16x32_bf16 v[116:119], a[120:123], v[168:171], v[116:119]\n\t"
    "v_mul_f32 v43, v25, v43\n\t"
    "v_cvt_pk_bf16_f32 v40, v40, v41\n\t"
    "v_mfma_f32_16x16x32_bf16 v[116:119], a[124:127], v[180:183], v[116:119]\n\t"
    "ds_read_b64_tr_b16 a[148:149], v9 offset:20480\n\t"
    "v_cvt_pk_bf16_f32 v41, v42, v43\n\t"
    "v_mfma_f32_16x16x32_bf16 v[128:131], a[96:99], v[100:103], v[128:131]\n\t"
    "ds_read_b64_tr_b16 a[150:151], v9 offset:22528\n\t"
    "v_mfma_f32_16x16x32_bf16 v[128:131], a[100:103], v[112:115], v[128:131]\n\t"
    "buffer_load_dwordx4 v17, s[24:27], s47 offen offset:1024 lds\n\t"
    "v_mfma_f32_16x16x32_bf16 v[128:131], a[104:107], v[124:127], v[128:131]\n\t"
    "ds_read_b64_tr_b16 a[152:153], v9 offset:24576\n\t"
    "v_mul_f32 v116, v26, v116\n\t"
    "v_mfma_f32_16x16x32_bf16 v[128:131], a[108:111], v[136:139], v[128:131]\n\t"
    "ds_read_b64_tr_b16 a[154:155], v9 offset:26624\n\t"
    "v_mul_f32 v117, v26, v117\n\t"
    "v_mfma_f32_16x16x32_bf16 v[128:131], a[112:115], v[148:151], v[128:131]\n\t"
    "v_mul_f32 v118, v26, v118\n\t"
    "v_mul_f32 v119, v26, v119\n\t"
    "v_mfma_f32_16x16x32_bf16 v[128:131], a[116:119], v[160:163], v[128:131]\n\t"
    "ds_read_b64_tr_b16 a[156:157], v9 offset:28672\n\t"
    "v_cvt_pk_bf16_f32 v116, v116, v117\n\t"
    "v_mfma_f32_16x16x32_bf16 v[128:131], a[120:123], v[172:175], v[128:131]\n\t"
    "ds_read_b64_tr_b16 a[158:159], v9 offset:30720\n\t"
    "v_cvt_pk_bf16_f32 v117, v118, v119\n\t"
    "v_mfma_f32_16x16x32_bf16 v[128:131], a[124:127], v[184:187], v[128:131]\n\t"
    "s_waitcnt lgkmcnt(14)\n\t"
    "buffer_load_dwordx4 v18, s[24:27], s47 offen offset:2048 lds\n\t"
    "v_mfma_f32_16x16x32_bf16 v[140:143], a[128:131], v[76:79], v[140:143]\n\t"
    "s_waitcnt lgkmcnt(12)\n\t"
    "ds_read_b64_tr_b16 a[96:97], v10 offset:0\n\t"
    "v_mfma_f32_16x16x32_bf16 v[140:143], a[132:135], v[48:51], v[140:143]\n\t"
    "s_waitcnt lgkmcnt(11)\n\t"
    "ds_read_b64_tr_b16 a[98:99], v10 offset:2048\n\t"
    "v_mul_f32 v128, v27, v128\n\t"
    "v_mfma_f32_16x16x32_bf16 v[140:143], a[136:139], v[92:95], v[140:143]\n\t"
    "s_waitcnt lgkmcnt(10)\n\t"
    "buffer_load_dwordx4 v19, s[24:27], s47 offen offset:3072 lds\n\t"
    "v_mfma_f32_16x16x32_bf16 v[140:143], a[140:143], v[88:91], v[140:143]\n\t"
    "s_waitcnt lgkmcnt(8)\n\t"
    "ds_read_b64_tr_b16 a[100:101], v10 offset:4096\n\t"
    "v_mul_f32 v129, v27, v129\n\t"
    "v_mfma_f32_16x16x32_bf16 v[140:143], a[144:147], v[68:71], v[140:143]\n\t"
    "s_waitcnt lgkmcnt(7)\n\t"
    "ds_read_b64_tr_b16 a[102:103], v10 offset:6144\n\t"
    "v_mul_f32 v130, v27, v130\n\t"
    "v_mfma_f32_16x16x32_bf16 v[140:143], a[148:151], v[44:47], v[140:143]\n\t"
    "s_waitcnt lgkmcnt(6)\n\t"
    "v_mul_f32 v131, v27, v131\n\t"
    "v_cvt_pk_bf16_f32 v128, v128, v129\n\t"
    "v_mfma_f32_16x16x32_bf16 v[140:143], a[152:155], v[64:67], v[140:143]\n\t"
    "s_waitcnt lgkmcnt(4)\n\t"
    "ds_read_b64_tr_b16 a[104:105], v10 offset:8192\n\t"
    "v_cvt_pk_bf16_f32 v129, v130, v131\n\t"
    "v_mfma_f32_16x16x32_bf16 v[140:143], a[156:159], v[104:107], v[140:143]\n\t"
    "ds_read_b64_tr_b16 a[106:107], v10 offset:10240\n\t"
    "v_mfma_f32_16x16x32_bf16 v[152:155], a[128:131], v[96:99], v[152:155]\n\t"
    "v_mfma_f32_16x16x32_bf16 v[152:155], a[132:135], v[108:111], v[152:155]\n\t"
    "ds_read_b64_tr_b16 a[108:109], v10 offset:12288\n\t"
    "v_mfma_f32_16x16x32_bf16 v[152:155], a[136:139], v[120:123], v[152:155]\n\t"
    "ds_read_b64_tr_b16 a[110:111], v10 offset:14336\n\t"
    "v_mfma_f32_16x16x32_bf16 v[152:155], a[140:143], v[132:135], v[152:155]\n\t"
    "v_mfma_f32_16x16x32_bf16 v[152:155], a[144:147], v[144:147], v[152:155]\n\t"
    "ds_read_b64_tr_b16 a[112:113], v10 offset:16384\n\t"
    "v_mul_f32 v140, v25, v140\n\t"
    "v_mfma_f32_16x16x32_bf16 v[152:155], a[148:151], v[156:159], v[152:155]\n\t"
    "ds_read_b64_tr_b16 a[114:115], v10 offset:18432\n\t"
    "v_mul_f32 v141, v25, v141\n\t"
    "v_mfma_f32_16x16x32_bf16 v[152:155], a[152:155], v[168:171], v[152:155]\n\t"
    "v_mul_f32 v142, v25, v142\n\t"
    "v_mul_f32 v143, v25, v143\n\t"
    "v_mfma_f32_16x16x32_bf16 v[152:155], a[156:159], v[180:183], v[152:155]\n\t"
    "ds_read_b64_tr_b16 a[116:117], v10 offset:20480\n\t"
    "v_cvt_pk_bf16_f32 v42, v140, v141\n\t"
    "v_mfma_f32_16x16x32_bf16 v[164:167], a[128:131], v[100:103], v[164:167]\n\t"
    "ds_read_b64_tr_b16 a[118:119], v10 offset:22528\n\t"
    "v_cvt_pk_bf16_f32 v43, v142, v143\n\t"
    "v_mfma_f32_16x16x32_bf16 v[164:167], a[132:135], v[112:115], v[164:167]\n\t"
    "v_permlane16_swap_b32 v40, v42\n\t"
    "v_mfma_f32_16x16x32_bf16 v[164:167], a[136:139], v[124:127], v[164:167]\n\t"
    "ds_read_b64_tr_b16 a[120:121], v10 offset:24576\n\t"
    "v_mul_f32 v152, v26, v152\n\t"
    "v_mfma_f32_16x16x32_bf16 v[164:167], a[140:143], v[136:139], v[164:167]\n\t"
    "ds_read_b64_tr_b16 a[122:123], v10 offset:26624\n\t"
    "v_mul_f32 v153, v26, v153\n\t"
    "v_mfma_f32_16x16x32_bf16 v[164:167], a[144:147], v[148:151], v[164:167]\n\t"
    "v_permlane16_swap_b32 v41, v43\n\t"
    "v_mfma_f32_16x16x32_bf16 v[164:167], a[148:151], v[160:163], v[164:167]\n\t"
    "ds_read_b64_tr_b16 a[124:125], v10 offset:28672\n\t"
    "v_mul_f32 v154, v26, v154\n\t"
    "v_mfma_f32_16x16x32_bf16 v[164:167], a[152:155], v[172:175], v[164:167]\n\t"
    "ds_read_b64_tr_b16 a[126:127], v10 offset:30720\n\t"
    "v_mul_f32 v155, v26, v155\n\t"
    "v_mfma_f32_16x16x32_bf16 v[164:167], a[156:159], v[184:187], v[164:167]\n\t"
    "s_waitcnt lgkmcnt(14)\n\t"
    "buffer_store_dwordx4 v[40:43], v21, s[36:39], s70 offen offset:0 sc1\n\t"
    "v_mfma_f32_16x16x32_bf16 v[176:179], a[96:99], v[76:79], v[176:179]\n\t"
    "s_waitcnt lgkmcnt(12)\n\t"
    "ds_read_b64_tr_b16 a[128:129], v11 offset:0\n\t"
    "v_cvt_pk_bf16_f32 v118, v152, v153\n\t"
    "v_mfma_f32_16x16x32_bf16 v[176:179], a[100:103], v[48:51], v[176:179]\n\t"
    "s_waitcnt lgkmcnt(11)\n\t"
    "ds_read_b64_tr_b16 a[130:131], v11 offset:2048\n\t"
    "v_cvt_pk_bf16_f32 v119, v154, v155\n\t"
    "v_mfma_f32_16x16x32_bf16 v[176:179], a[104:107], v[92:95], v[176:179]\n\t"
    "s_waitcnt lgkmcnt(10)\n\t"
    "v_permlane16_swap_b32 v116, v118\n\t"
    "v_mfma_f32_16x16x32_bf16 v[176:179], a[108:111], v[88:91], v[176:179]\n\t"
    "s_waitcnt lgkmcnt(8)\n\t"
    "ds_read_b64_tr_b16 a[132:133], v11 offset:4096\n\t"
    "v_mul_f32 v164, v27, v164\n\t"
    "v_mfma_f32_16x16x32_bf16 v[176:179], a[112:115], v[68:71], v[176:179]\n\t"
    "s_waitcnt lgkmcnt(7)\n\t"
    "ds_read_b64_tr_b16 a[134:135], v11 offset:6144\n\t"
    "v_mul_f32 v165, v27, v165\n\t"
    "v_mfma_f32_16x16x32_bf16 v[176:179], a[116:119], v[44:47], v[176:179]\n\t"
    "s_waitcnt lgkmcnt(6)\n\t"
    "v_permlane16_swap_b32 v117, v119\n\t"
    "v_mfma_f32_16x16x32_bf16 v[176:179], a[120:123], v[64:67], v[176:179]\n\t"
    "s_waitcnt lgkmcnt(4)\n\t"
    "ds_read_b64_tr_b16 a[136:137], v11 offset:8192\n\t"
    "v_mul_f32 v166, v27, v166\n\t"
    "v_mfma_f32_16x16x32_bf16 v[176:179], a[124:127], v[104:107], v[176:179]\n\t"
    "ds_read_b64_tr_b16 a[138:139], v11 offset:10240\n\t"
    "v_mul_f32 v167, v27, v167\n\t"
    "v_mfma_f32_16x16x32_bf16 v[188:191], a[96:99], v[96:99], v[188:191]\n\t"
    "buffer_store_dwordx4 v[116:119], v21, s[36:39], s71 offen offset:0 sc1\n\t"
    "v_mfma_f32_16x16x32_bf16 v[188:191], a[100:103], v[108:111], v[188:191]\n\t"
    "ds_read_b64_tr_b16 a[140:141], v11 offset:12288\n\t"
    "v_cvt_pk_bf16_f32 v130, v164, v165\n\t"
    "v_mfma_f32_16x16x32_bf16 v[188:191], a[104:107], v[120:123], v[188:191]\n\t"
    "ds_read_b64_tr_b16 a[142:143], v11 offset:14336\n\t"
    "v_cvt_pk_bf16_f32 v131, v166, v167\n\t"
    "v_mfma_f32_16x16x32_bf16 v[188:191], a[108:111], v[132:135], v[188:191]\n\t"
    "v_permlane16_swap_b32 v128, v130\n\t"
    ".p2align 12\n\t"
    "Lstub7_%=:\n\t"
    "s_cbranch_execz Lret_%=\n\t"
    "v_mfma_f32_16x16x32_bf16 v[188:191], a[112:115], v[144:147], v[188:191]\n\t"
    "ds_read_b64_tr_b16 a[144:145], v11 offset:16384\n\t"
    "v_mul_f32 v176, v25, v176\n\t"
    "v_mfma_f32_16x16x32_bf16 v[188:191], a[116:119], v[156:159], v[188:191]\n\t"
    "ds_read_b64_tr_b16 a[146:147], v11 offset:18432\n\t"
    "v_mul_f32 v177, v25, v177\n\t"
    "v_mfma_f32_16x16x32_bf16 v[188:191], a[120:123], v[168:171], v[188:191]\n\t"
    "v_permlane16_swap_b32 v129, v131\n\t"
    "v_mfma_f32_16x16x32_bf16 v[188:191], a[124:127], v[180:183], v[188:191]\n\t"
    "ds_read_b64_tr_b16 a[148:149], v11 offset:20480\n\t"
    "v_mul_f32 v178, v25, v178\n\t"
    "v_mfma_f32_16x16x32_bf16 v[192:195], a[96:99], v[100:103], v[192:195]\n\t"
    "ds_read_b64_tr_b16 a[150:151], v11 offset:22528\n\t"
    "v_mul_f32 v179, v25, v179\n\t"
    "v_mfma_f32_16x16x32_bf16 v[192:195], a[100:103], v[112:115], v[192:195]\n\t"
    "buffer_store_dwordx4 v[128:131], v21, s[36:39], s72 offen offset:0 sc1\n\t"
    "v_mfma_f32_16x16x32_bf16 v[192:195], a[104:107], v[124:127], v[192:195]\n\t"
    "ds_read_b64_tr_b16 a[152:153], v11 offset:24576\n\t"
    "v_cvt_pk_bf16_f32 v176, v176, v177\n\t"
    "v_mfma_f32_16x16x32_bf16 v[192:195], a[108:111], v[136:139], v[192:195]\n\t"
    "ds_read_b64_tr_b16 a[154:155], v11 offset:26624\n\t"
    "v_cvt_pk_bf16_f32 v177, v178, v179\n\t"
    "v_mfma_f32_16x16x32_bf16 v[192:195], a[112:115], v[148:151], v[192:195]\n\t"
    "s_waitcnt vmcnt(3)\n\t"
    "s_barrier\n\t"
    "v_mul_f32 v188, v26, v188\n\t"
    "v_mfma_f32_16x16x32_bf16 v[192:195], a[116:119], v[160:163], v[192:195]\n\t"
    "ds_read_b64_tr_b16 a[156:157], v11 offset:28672\n\t"
    "v_mul_f32 v189, v26, v189\n\t"
    "v_mfma_f32_16x16x32_bf16 v[192:195], a[120:123], v[172:175], v[192:195]\n\t"
    "ds_read_b64_tr_b16 a[158:159], v11 offset:30720\n\t"
    "v_mul_f32 v190, v26, v190\n\t"
    "v_mfma_f32_16x16x32_bf16 v[192:195], a[124:127], v[184:187], v[192:195]\n\t"
    "s_waitcnt lgkmcnt(14)\n\t"
    "ds_read_b128 v[12:15], v30 offset:32768\n\t"
    "v_mfma_f32_16x16x32_bf16 v[196:199], a[128:131], v[76:79], v[196:199]\n\t"
    "s_waitcnt lgkmcnt(13)\n\t"
    "ds_read_b64_tr_b16 a[96:97], v4 offset:32768\n\t"
    "v_mul_f32 v191, v26, v191\n\t"
    "v_mfma_f32_16x16x32_bf16 v[196:199], a[132:135], v[48:51], v[196:199]\n\t"
    "s_waitcnt lgkmcnt(12)\n\t"
    "ds_read_b64_tr_b16 a[98:99], v4 offset:34816\n\t"
    "v_cvt_pk_bf16_f32 v188, v188, v189\n\t"
    "v_mfma_f32_16x16x32_bf16 v[196:199], a[136:139], v[92:95], v[196:199]\n\t"
    "s_waitcnt lgkmcnt(11)\n\t"
    "v_cvt_pk_bf16_f32 v189, v190, v191\n\t"
    "v_mul_f32 v192, v27, v192\n\t"
    "v_mfma_f32_16x16x32_bf16 v[196:199], a[140:143], v[88:91], v[196:199]\n\t"
    "s_waitcnt lgkmcnt(9)\n\t"
    "ds_read_b64_tr_b16 a[100:101], v4 offset:36864\n\t"
    "v_mul_f32 v193, v27, v193\n\t"
    "v_mfma_f32_16x16x32_bf16 v[196:199], a[144:147], v[68:71], v[196:199]\n\t"
    "s_waitcnt lgkmcnt(8)\n\t"
    "ds_read_b64_tr_b16 a[102:103], v4 offset:38912\n\t"
    "v_mul_f32 v194, v27, v194\n\t"
    "v_mfma_f32_16x16x32_bf16 v[196:199], a[148:151], v[44:47], v[196:199]\n\t"
    "s_waitcnt lgkmcnt(7)\n\t"
    "v_mul_f32 v195, v27, v195\n\t"
    "v_cvt_pk_bf16_f32 v192, v192, v193\n\t"
    "v_mfma_f32_16x16x32_bf16 v[196:199], a[152:155], v[64:67], v[196:199]\n\t"
    "s_waitcnt lgkmcnt(5)\n\t"
    "ds_read_b64_tr_b16 a[104:105], v4 offset:40960\n\t"
    "v_cvt_pk_bf16_f32 v193, v194, v195\n\t"
    "v_mfma_f32_16x16x32_bf16 v[196:199], a[156:159], v[104:107], v[196:199]\n\t"
    "ds_read_b64_tr_b16 a[106:107], v4 offset:43008\n\t"
    "v_mfma_f32_16x16x32_bf16 v[200:203], a[128:131], v[96:99], v[200:203]\n\t"
    "v_mfma_f32_16x16x32_bf16 v[200:203], a[132:135], v[108:111], v[200:203]\n\t"
    "ds_read_b64_tr_b16 a[108:109], v4 offset:45056\n\t"
    "v_mfma_f32_16x16x32_bf16 v[200:203], a[136:139], v[120:123], v[200:203]\n\t"
    "ds_read_b64_tr_b16 a[110:111], v4 offset:47104\n\t"
    "v_mfma_f32_16x16x32_bf16 v[200:203], a[140:143], v[132:135], v[200:203]\n\t"
    "v_mfma_f32_16x16x32_bf16 v[200:203], a[144:147], v[144:147], v[200:203]\n\t"
    "ds_read_b64_tr_b16 a[112:113], v4 offset:49152\n\t"
    "v_mul_f32 v196, v25, v196\n\t"
    "v_mfma_f32_16x16x32_bf16 v[200:203], a[148:151], v[156:159], v[200:203]\n\t"
    "ds_read_b64_tr_b16 a[114:115], v4 offset:51200\n\t"
    "v_mul_f32 v197, v25, v197\n\t"
    "v_mfma_f32_16x16x32_bf16 v[200:203], a[152:155], v[168:171], v[200:203]\n\t"
    "v_mul_f32 v198, v25, v198\n\t"
    "v_mul_f32 v199, v25, v199\n\t"
    "v_mfma_f32_16x16x32_bf16 v[200:203], a[156:159], v[180:183], v[200:203]\n\t"
    "ds_read_b64_tr_b16 a[116:117], v4 offset:53248\n\t"
    "v_cvt_pk_bf16_f32 v178, v196, v197\n\t"
    "v_mfma_f32_16x16x32_bf16 v[212:215], a[128:131], v[100:103], v[212:215]\n\t"
    "ds_read_b64_tr_b16 a[118:119], v4 offset:55296\n\t"
    "v_cvt_pk_bf16_f32 v179, v198, v199\n\t"
    "v_mfma_f32_16x16x32_bf16 v[212:215], a[132:135], v[112:115], v[212:215]\n\t"
    "v_permlane16_swap_b32 v176, v178\n\t"
    "v_mfma_f32_16x16x32_bf16 v[212:215], a[136:139], v[124:127], v[212:215]\n\t"
    "ds_read_b64_tr_b16 a[120:121], v4 offset:57344\n\t"
    "v_mul_f32 v200, v26, v200\n\t"
    "v_mfma_f32_16x16x32_bf16 v[212:215], a[140:143], v[136:139], v[212:215]\n\t"
    "ds_read_b64_tr_b16 a[122:123], v4 offset:59392\n\t"
    "v_mul_f32 v201, v26, v201\n\t"
    "v_mfma_f32_16x16x32_bf16 v[212:215], a[144:147], v[148:151], v[212:215]\n\t"
    "v_permlane16_swap_b32 v177, v179\n\t"
    "v_mfma_f32_16x16x32_bf16 v[212:215], a[148:151], v[160:163], v[212:215]\n\t"
    "ds_read_b64_tr_b16 a[124:125], v4 offset:61440\n\t"
    "v_mul_f32 v202, v26, v202\n\t"
    "v_mfma_f32_16x16x32_bf16 v[212:215], a[152:155], v[172:175], v[212:215]\n\t"
    "ds_read_b64_tr_b16 a[126:127], v4 offset:63488\n\t"
    "v_mul_f32 v203, v26, v203\n\t"
    "v_mfma_f32_16x16x32_bf16 v[212:215], a[156:159], v[184:187], v[212:215]\n\t"
    "s_waitcnt lgkmcnt(14)\n\t"
    "buffer_store_dwordx4 v[176:179], v21, s[36:39], s70 offen offset:64 sc1\n\t"
    "v_mfma_f32_16x16x32_bf16 v[40:43], a[96:99], a[176:179], 0\n\t"
    "s_waitcnt lgkmcnt(12)\n\t"
    "ds_read_b64_tr_b16 a[128:129], v5 offset:32768\n\t"
    "v_cvt_pk_bf16_f32 v190, v200, v201\n\t"
    "v_mfma_f32_16x16x32_bf16 v[40:43], a[100:103], a[188:191], v[40:43]\n\t"
    "s_waitcnt lgkmcnt(11)\n\t"
    "ds_read_b64_tr_b16 a[130:131], v5 offset:34816\n\t"
    "v_cvt_pk_bf16_f32 v191, v202, v203\n\t"
    "v_mfma_f32_16x16x32_bf16 v[40:43], a[104:107], a[200:203], v[40:43]\n\t"
    "s_waitcnt lgkmcnt(10)\n\t"
    "v_permlane16_swap_b32 v188, v190\n\t"
    "v_mfma_f32_16x16x32_bf16 v[40:43], a[108:111], a[212:215], v[40:43]\n\t"
    "s_waitcnt lgkmcnt(8)\n\t"
    "ds_read_b64_tr_b16 a[132:133], v5 offset:36864\n\t"
    "v_mul_f32 v212, v27, v212\n\t"
    "v_mfma_f32_16x16x32_bf16 v[40:43], a[112:115], v[228:231], v[40:43]\n\t"
    "s_waitcnt lgkmcnt(7)\n\t"
    "ds_read_b64_tr_b16 a[134:135], v5 offset:38912\n\t"
    "v_mul_f32 v213, v27, v213\n\t"
    "v_mfma_f32_16x16x32_bf16 v[40:43], a[116:119], v[252:255], v[40:43]\n\t"
    "s_waitcnt lgkmcnt(6)\n\t"
    "v_permlane16_swap_b32 v189, v191\n\t"
    "v_mfma_f32_16x16x32_bf16 v[40:43], a[120:123], v[80:83], v[40:43]\n\t"
    "s_waitcnt lgkmcnt(4)\n\t"
    "ds_read_b64_tr_b16 a[136:137], v5 offset:40960\n\t"
    "v_mul_f32 v214, v27, v214\n\t"
    "v_mfma_f32_16x16x32_bf16 v[40:43], a[124:127], v[52:55], v[40:43]\n\t"
    "ds_read_b64_tr_b16 a[138:139], v5 offset:43008\n\t"
    "v_mul_f32 v215, v27, v215\n\t"
    "v_mfma_f32_16x16x32_bf16 v[116:119], a[96:99], a[180:183], 0\n\t"
    "buffer_store_dwordx4 v[188:191], v21, s[36:39], s71 offen offset:64 sc1\n\t"
    "v_mfma_f32_16x16x32_bf16 v[116:119], a[100:103], a[192:195], v[116:119]\n\t"
    "ds_read_b64_tr_b16 a[140:141], v5 offset:45056\n\t"
    "v_cvt_pk_bf16_f32 v194, v212, v213\n\t"
    "v_mfma_f32_16x16x32_bf16 v[116:119], a[104:107], a[204:207], v[116:119]\n\t"
    "ds_read_b64_tr_b16 a[142:143], v5 offset:47104\n\t"
    "v_cvt_pk_bf16_f32 v195, v214, v215\n\t"
    "v_mfma_f32_16x16x32_bf16 v[116:119], a[108:111], a[216:219], v[116:119]\n\t"
    "v_permlane16_swap_b32 v192, v194\n\t"
    "v_mfma_f32_16x16x32_bf16 v[116:119], a[112:115], v[204:207], v[116:119]\n\t"
    "ds_read_b64_tr_b16 a[144:145], v5 offset:49152\n\t"
    "v_mfma_f32_16x16x32_bf16 v[116:119], a[116:119], v[208:211], v[116:119]\n\t"
    "ds_read_b64_tr_b16 a[146:147], v5 offset:51200\n\t"
    "v_mfma_f32_16x16x32_bf16 v[116:119], a[120:123], v[36:39], v[116:119]\n\t"
    "v_permlane16_swap_b32 v193, v195\n\t"
    "v_mfma_f32_16x16x32_bf16 v[116:119], a[124:127], v[56:59], v[116:119]\n\t"
    "ds_read_b64_tr_b16 a[148:149], v5 offset:53248\n\t"
    "v_mfma_f32_16x16x32_bf16 v[128:131], a[96:99], a[184:187], 0\n\t"
    "ds_read_b64_tr_b16 a[150:151], v5 offset:55296\n\t"
    "v_mfma_f32_16x16x32_bf16 v[128:131], a[100:103], a[196:199], v[128:131]\n\t"
    "buffer_store_dwordx4 v[192:195], v21, s[36:39], s72 offen offset:64 sc1\n\t"
    "v_mfma_f32_16x16x32_bf16 v[128:131], a[104:107], a[208:211], v[128:131]\n\t"
    "ds_read_b64_tr_b16 a[152:153], v5 offset:57344\n\t"
    "v_mfma_f32_16x16x32_bf16 v[128:131], a[108:111], a[220:223], v[128:131]\n\t"
    "ds_read_b64_tr_b16 a[154:155], v5 offset:59392\n\t"
    "v_mfma_f32_16x16x32_bf16 v[128:131], a[112:115], v[32:35], v[128:131]\n\t"
    "v_mfma_f32_16x16x32_bf16 v[128:131], a[116:119], v[72:75], v[128:131]\n\t"
    "ds_read_b64_tr_b16 a[156:157], v5 offset:61440\n\t"
    "v_mfma_f32_16x16x32_bf16 v[128:131], a[120:123], v[84:87], v[128:131]\n\t"
    "ds_read_b64_tr_b16 a[158:159], v5 offset:63488\n\t"
    "v_mfma_f32_16x16x32_bf16 v[128:131], a[124:127], v[60:63], v[128:131]\n\t"
    "s_waitcnt lgkmcnt(14)\n\t"
    "v_mfma_f32_16x16x32_bf16 v[140:143], a[128:131], a[176:179], 0\n\t"
    "s_waitcnt lgkmcnt(12)\n\t"
    "ds_read_b64_tr_b16 a[96:97], v6 offset:32768\n\t"
    "v_mfma_f32_16x16x32_bf16 v[140:143], a[132:135], a[188:191], v[140:143]\n\t"
    "s_waitcnt lgkmcnt(11)\n\t"
    "ds_read_b64_tr_b16 a[98:99], v6 offset:34816\n\t"
    "v_mfma_f32_16x16x32_bf16 v[140:143], a[136:139], a[200:203], v[140:143]\n\t"
    "s_waitcnt lgkmcnt(10)\n\t"
    "v_mfma_f32_16x16x32_bf16 v[140:143], a[140:143], a[212:215], v[140:143]\n\t"
    "s_waitcnt lgkmcnt(8)\n\t"
    "ds_read_b64_tr_b16 a[100:101], v6 offset:36864\n\t"
    "v_mfma_f32_16x16x32_bf16 v[140:143], a[144:147], v[228:231], v[140:143]\n\t"
    "s_waitcnt lgkmcnt(7)\n\t"
    "ds_read_b64_tr_b16 a[102:103], v6 offset:38912\n\t"
    "v_mfma_f32_16x16x32_bf16 v[140:143], a[148:151], v[252:255], v[140:143]\n\t"
    "s_waitcnt lgkmcnt(6)\n\t"
    "v_mfma_f32_16x16x32_bf16 v[140:143], a[152:155], v[80:83], v[140:143]\n\t"
    "s_waitcnt lgkmcnt(4)\n\t"
    "ds_read_b64_tr_b16 a[104:105], v6 offset:40960\n\t"
    "v_mfma_f32_16x16x32_bf16 v[140:143], a[156:159], v[52:55], v[140:143]\n\t"
    "ds_read_b64_tr_b16 a[106:107], v6 offset:43008\n\t"
    "v_mfma_f32_16x16x32_bf16 v[152:155], a[128:131], a[180:183], 0\n\t"
    "v_mfma_f32_16x16x32_bf16 v[152:155], a[132:135], a[192:195], v[152:155]\n\t"
    "ds_read_b64_tr_b16 a[108:109], v6 offset:45056\n\t"
    "v_mfma_f32_16x16x32_bf16 v[152:155], a[136:139], a[204:207], v[152:155]\n\t"
    "ds_read_b64_tr_b16 a[110:111], v6 offset:47104\n\t"
    "v_mfma_f32_16x16x32_bf16 v[152:155], a[140:143], a[216:219], v[152:155]\n\t"
    "v_mfma_f32_16x16x32_bf16 v[152:155], a[144:147], v[204:207], v[152:155]\n\t"
    "ds_read_b64_tr_b16 a[112:113], v6 offset:49152\n\t"
    "v_mfma_f32_16x16x32_bf16 v[152:155], a[148:151], v[208:211], v[152:155]\n\t"
    "ds_read_b64_tr_b16 a[114:115], v6 offset:51200\n\t"
    "v_mfma_f32_16x16x32_bf16 v[152:155], a[152:155], v[36:39], v[152:155]\n\t"
    "v_mfma_f32_16x16x32_bf16 v[152:155], a[156:159], v[56:59], v[152:155]\n\t"
    "ds_read_b64_tr_b16 a[116:117], v6 offset:53248\n\t"
    "v_mfma_f32_16x16x32_bf16 v[164:167], a[128:131], a[184:187], 0\n\t"
    "ds_read_b64_tr_b16 a[118:119], v6 offset:55296\n\t"
    "v_mfma_f32_16x16x32_bf16 v[164:167], a[132:135], a[196:199], v[164:167]\n\t"
    "v_mfma_f32_16x16x32_bf16 v[164:167], a[136:139], a[208:211], v[164:167]\n\t"
    "ds_read_b64_tr_b16 a[120:121], v6 offset:57344\n\t"
    "v_mfma_f32_16x16x32_bf16 v[164:167], a[140:143], a[220:223], v[164:167]\n\t"
    "ds_read_b64_tr_b16 a[122:123], v6 offset:59392\n\t"
    "v_mfma_f32_16x16x32_bf16 v[164:167], a[144:147], v[32:35], v[164:167]\n\t"
    "v_mfma_f32_16x16x32_bf16 v[164:167], a[148:151], v[72:75], v[164:167]\n\t"
    "ds_read_b64_tr_b16 a[124:125], v6 offset:61440\n\t"
    "v_mfma_f32_16x16x32_bf16 v[164:167], a[152:155], v[84:87], v[164:167]\n\t"
    "ds_read_b64_tr_b16 a[126:127], v6 offset:63488\n\t"
    "v_mfma_f32_16x16x32_bf16 v[164:167], a[156:159], v[60:63], v[164:167]\n\t"
    "s_waitcnt lgkmcnt(14)\n\t"
    "v_mfma_f32_16x16x32_bf16 v[176:179], a[96:99], a[176:179], 0\n\t"
    "s_waitcnt lgkmcnt(12)\n\t"
    "ds_read_b64_tr_b16 a[128:129], v7 offset:32768\n\t"
    "v_mfma_f32_16x16x32_bf16 v[176:179], a[100:103], a[188:191], v[176:179]\n\t"
    "s_waitcnt lgkmcnt(11)\n\t"
    "ds_read_b64_tr_b16 a[130:131], v7 offset:34816\n\t"
    "v_mfma_f32_16x16x32_bf16 v[176:179], a[104:107], a[200:203], v[176:179]\n\t"
    "s_waitcnt lgkmcnt(10)\n\t"
    "v_mfma_f32_16x16x32_bf16 v[176:179], a[108:111], a[212:215], v[176:179]\n\t"
    "s_waitcnt lgkmcnt(8)\n\t"
    "ds_read_b64_tr_b16 a[132:133], v7 offset:36864\n\t"
    "v_mfma_f32_16x16x32_bf16 v[176:179], a[112:115], v[228:231], v[176:179]\n\t"
    "s_waitcnt lgkmcnt(7)\n\t"
    "ds_read_b64_tr_b16 a[134:135], v7 offset:38912\n\t"
    "v_mfma_f32_16x16x32_bf16 v[176:179], a[116:119], v[252:255], v[176:179]\n\t"
    "s_waitcnt lgkmcnt(6)\n\t"
    "v_mfma_f32_16x16x32_bf16 v[176:179], a[120:123], v[80:83], v[176:179]\n\t"
    "s_waitcnt lgkmcnt(4)\n\t"
    "ds_read_b64_tr_b16 a[136:137], v7 offset:40960\n\t"
    "v_mfma_f32_16x16x32_bf16 v[176:179], a[124:127], v[52:55], v[176:179]\n\t"
    "ds_read_b64_tr_b16 a[138:139], v7 offset:43008\n\t"
    "v_mfma_f32_16x16x32_bf16 v[188:191], a[96:99], a[180:183], 0\n\t"
    "v_mfma_f32_16x16x32_bf16 v[188:191], a[100:103], a[192:195], v[188:191]\n\t"
    "ds_read_b64_tr_b16 a[140:141], v7 offset:45056\n\t"
    "v_mfma_f32_16x16x32_bf16 v[188:191], a[104:107], a[204:207], v[188:191]\n\t"
    "ds_read_b64_tr_b16 a[142:143], v7 offset:47104\n\t"
    "v_mfma_f32_16x16x32_bf16 v[188:191], a[108:111], a[216:219], v[188:191]\n\t"
    "v_mfma_f32_16x16x32_bf16 v[188:191], a[112:115], v[204:207], v[188:191]\n\t"
    "ds_read_b64_tr_b16 a[144:145], v7 offset:49152\n\t"
    "v_mfma_f32_16x16x32_bf16 v[188:191], a[116:119], v[208:211], v[188:191]\n\t"
    "ds_read_b64_tr_b16 a[146:147], v7 offset:51200\n\t"
    "v_mfma_f32_16x16x32_bf16 v[188:191], a[120:123], v[36:39], v[188:191]\n\t"
    "v_mfma_f32_16x16x32_bf16 v[188:191], a[124:127], v[56:59], v[188:191]\n\t"
    "ds_read_b64_tr_b16 a[148:149], v7 offset:53248\n\t"
    "v_mfma_f32_16x16x32_bf16 v[192:195], a[96:99], a[184:187], 0\n\t"
    "ds_read_b64_tr_b16 a[150:151], v7 offset:55296\n\t"
    "v_mfma_f32_16x16x32_bf16 v[192:195], a[100:103], a[196:199], v[192:195]\n\t"
    "v_mfma_f32_16x16x32_bf16 v[192:195], a[104:107], a[208:211], v[192:195]\n\t"
    "ds_read_b64_tr_b16 a[152:153], v7 offset:57344\n\t"
    "v_mfma_f32_16x16x32_bf16 v[192:195], a[108:111], a[220:223], v[192:195]\n\t"
    "ds_read_b64_tr_b16 a[154:155], v7 offset:59392\n\t"
    "v_mfma_f32_16x16x32_bf16 v[192:195], a[112:115], v[32:35], v[192:195]\n\t"
    "v_mfma_f32_16x16x32_bf16 v[192:195], a[116:119], v[72:75], v[192:195]\n\t"
    "ds_read_b64_tr_b16 a[156:157], v7 offset:61440\n\t"
    "v_mfma_f32_16x16x32_bf16 v[192:195], a[120:123], v[84:87], v[192:195]\n\t"
    "ds_read_b64_tr_b16 a[158:159], v7 offset:63488\n\t"
    "v_mfma_f32_16x16x32_bf16 v[192:195], a[124:127], v[60:63], v[192:195]\n\t"
    "s_waitcnt lgkmcnt(14)\n\t"
    "v_mfma_f32_16x16x32_bf16 v[196:199], a[128:131], a[176:179], 0\n\t"
    "s_waitcnt lgkmcnt(12)\n\t"
    "v_add_u32 v8, 0xffff0000, v8\n\t"
    "v_add_u32 v9, 0xffff0000, v9\n\t"
    "v_add_u32 v10, 0xffff0000, v10\n\t"
    "v_add_u32 v11, 0xffff0000, v11\n\t"
    "v_mfma_f32_16x16x32_bf16 v[196:199], a[132:135], a[188:191], v[196:199]\n\t"
    "s_waitcnt lgkmcnt(10)\n\t"
    "ds_read_b64_tr_b16 a[96:97], v8 offset:0\n\t"
    "v_mfma_f32_16x16x32_bf16 v[196:199], a[136:139], a[200:203], v[196:199]\n\t"
    "s_waitcnt lgkmcnt(9)\n\t"
    "ds_read_b64_tr_b16 a[98:99], v8 offset:2048\n\t"
    "v_mfma_f32_16x16x32_bf16 v[196:199], a[140:143], a[212:215], v[196:199]\n\t"
    "s_waitcnt lgkmcnt(8)\n\t"
    "ds_read_b64_tr_b16 a[100:101], v8 offset:4096\n\t"
    "v_mfma_f32_16x16x32_bf16 v[196:199], a[144:147], v[228:231], v[196:199]\n\t"
    "s_waitcnt lgkmcnt(7)\n\t"
    "ds_read_b64_tr_b16 a[102:103], v8 offset:6144\n\t"
    "v_mfma_f32_16x16x32_bf16 v[196:199], a[148:151], v[252:255], v[196:199]\n\t"
    "s_waitcnt lgkmcnt(6)\n\t"
    "v_mfma_f32_16x16x32_bf16 v[196:199], a[152:155], v[80:83], v[196:199]\n\t"
    "s_waitcnt lgkmcnt(4)\n\t"
    "ds_read_b64_tr_b16 a[104:105], v8 offset:8192\n\t"
    "v_mfma_f32_16x16x32_bf16 v[196:199], a[156:159], v[52:55], v[196:199]\n\t"
    "ds_read_b64_tr_b16 a[106:107], v8 offset:10240\n\t"
    "v_mfma_f32_16x16x32_bf16 v[200:203], a[128:131], a[180:183], 0\n\t"
    "v_mfma_f32_16x16x32_bf16 v[200:203], a[132:135], a[192:195], v[200:203]\n\t"
    "ds_read_b64_tr_b16 a[108:109], v8 offset:12288\n\t"
    "v_mfma_f32_16x16x32_bf16 v[200:203], a[136:139], a[204:207], v[200:203]\n\t"
    "ds_read_b64_tr_b16 a[110:111], v8 offset:14336\n\t"
    "v_mfma_f32_16x16x32_bf16 v[200:203], a[140:143], a[216:219], v[200:203]\n\t"
    "v_mfma_f32_16x16x32_bf16 v[200:203], a[144:147], v[204:207], v[200:203]\n\t"
    "ds_read_b64_tr_b16 a[112:113], v8 offset:16384\n\t"
    "v_mfma_f32_16x16x32_bf16 v[200:203], a[148:151], v[208:211], v[200:203]\n\t"
    "ds_read_b64_tr_b16 a[114:115], v8 offset:18432\n\t"
    "v_mfma_f32_16x16x32_bf16 v[200:203], a[152:155], v[36:39], v[200:203]\n\t"
    "v_mfma_f32_16x16x32_bf16 v[200:203], a[156:159], v[56:59], v[200:203]\n\t"
    "ds_read_b64_tr_b16 a[116:117], v8 offset:20480\n\t"
    "v_mfma_f32_16x16x32_bf16 v[212:215], a[128:131], a[184:187], 0\n\t"
    "ds_read_b64_tr_b16 a[118:119], v8 offset:22528\n\t"
    "v_mfma_f32_16x16x32_bf16 v[212:215], a[132:135], a[196:199], v[212:215]\n\t"
    "v_mfma_f32_16x16x32_bf16 v[212:215], a[136:139], a[208:211], v[212:215]\n\t"
    "ds_read_b64_tr_b16 a[120:121], v8 offset:24576\n\t"
    "v_mfma_f32_16x16x32_bf16 v[212:215], a[140:143], a[220:223], v[212:215]\n\t"
    "ds_read_b64_tr_b16 a[122:123], v8 offset:26624\n\t"
    "v_mfma_f32_16x16x32_bf16 v[212:215], a[144:147], v[32:35], v[212:215]\n\t"
    "v_mfma_f32_16x16x32_bf16 v[212:215], a[148:151], v[72:75], v[212:215]\n\t"
    "ds_read_b64_tr_b16 a[124:125], v8 offset:28672\n\t"
    "v_mfma_f32_16x16x32_bf16 v[212:215], a[152:155], v[84:87], v[212:215]\n\t"
    "ds_read_b64_tr_b16 a[126:127], v8 offset:30720\n\t"
    "v_mfma_f32_16x16x32_bf16 v[212:215], a[156:159], v[60:63], v[212:215]\n\t"
    "s_waitcnt lgkmcnt(14)\n\t"
    "v_mfma_f32_16x16x32_bf16 v[40:43], a[96:99], v[76:79], v[40:43]\n\t"
    "s_waitcnt lgkmcnt(12)\n\t"
    "ds_read_b64_tr_b16 a[128:129], v9 offset:0\n\t"
    "v_mfma_f32_16x16x32_bf16 v[40:43], a[100:103], v[48:51], v[40:43]\n\t"
    "s_waitcnt lgkmcnt(11)\n\t"
    "ds_read_b64_tr_b16 a[130:131], v9 offset:2048\n\t"
    "v_mfma_f32_16x16x32_bf16 v[40:43], a[104:107], v[92:95], v[40:43]\n\t"
    "s_waitcnt lgkmcnt(10)\n\t"
    "v_mfma_f32_16x16x32_bf16 v[40:43], a[108:111], v[88:91], v[40:43]\n\t"
    "s_waitcnt lgkmcnt(8)\n\t"
    "ds_read_b64_tr_b16 a[132:133], v9 offset:4096\n\t"
    "v_mfma_f32_16x16x32_bf16 v[40:43], a[112:115], v[68:71], v[40:43]\n\t"
    "s_waitcnt lgkmcnt(7)\n\t"
    "ds_read_b64_tr_b16 a[134:135], v9 offset:6144\n\t"
    "v_mfma_f32_16x16x32_bf16 v[40:43], a[116:119], v[44:47], v[40:43]\n\t"
    "s_waitcnt lgkmcnt(6)\n\t"
    "v_mfma_f32_16x16x32_bf16 v[40:43], a[120:123], v[64:67], v[40:43]\n\t"
    "s_waitcnt lgkmcnt(4)\n\t"
    "ds_read_b64_tr_b16 a[136:137], v9 offset:8192\n\t"
    "v_mfma_f32_16x16x32_bf16 v[40:43], a[124:127], v[104:107], v[40:43]\n\t"
    "ds_read_b64_tr_b16 a[138:139], v9 offset:10240\n\t"
    "v_mfma_f32_16x16x32_bf16 v[116:119], a[96:99], v[96:99], v[116:119]\n\t"
    "v_mfma_f32_16x16x32_bf16 v[116:119], a[100:103], v[108:111], v[116:119]\n\t"
    "ds_read_b64_tr_b16 a[140:141], v9 offset:12288\n\t"
    "v_mfma_f32_16x16x32_bf16 v[116:119], a[104:107], v[120:123], v[116:119]\n\t"
    "ds_read_b64_tr_b16 a[142:143], v9 offset:14336\n\t"
    "v_mfma_f32_16x16x32_bf16 v[116:119], a[108:111], v[132:135], v[116:119]\n\t"
    "v_mfma_f32_16x16x32_bf16 v[116:119], a[112:115], v[144:147], v[116:119]\n\t"
    "ds_read_b64_tr_b16 a[144:145], v9 offset:16384\n\t"
    "v_mul_f32 v40, v25, v40\n\t"
    "v_mfma_f32_16x16x32_bf16 v[116:119], a[116:119], v[156:159], v[116:119]\n\t"
    "ds_read_b64_tr_b16 a[146:147], v9 offset:18432\n\t"
    "v_mul_f32 v41, v25, v41\n\t"
    "v_mfma_f32_16x16x32_bf16 v[116:119], a[120:123], v[168:171], v[116:119]\n\t"
    "v_mul_f32 v42, v25, v42\n\t"
    "v_mul_f32 v43, v25, v43\n\t"
    "v_mfma_f32_16x16x32_bf16 v[116:119], a[124:127], v[180:183], v[116:119]\n\t"
    "ds_read_b64_tr_b16 a[148:149], v9 offset:20480\n\t"
    "v_cvt_pk_bf16_f32 v40, v40, v41\n\t"
    "v_mfma_f32_16x16x32_bf16 v[128:131], a[96:99], v[100:103], v[128:131]\n\t"
    "ds_read_b64_tr_b16 a[150:151], v9 offset:22528\n\t"
    "v_cvt_pk_bf16_f32 v41, v42, v43\n\t"
    "v_mfma_f32_16x16x32_bf16 v[128:131], a[100:103], v[112:115], v[128:131]\n\t"
    "v_mfma_f32_16x16x32_bf16 v[128:131], a[104:107], v[124:127], v[128:131]\n\t"
    "ds_read_b64_tr_b16 a[152:153], v9 offset:24576\n\t"
    "v_mul_f32 v116, v26, v116\n\t"
    "v_mfma_f32_16x16x32_bf16 v[128:131], a[108:111], v[136:139], v[128:131]\n\t"
    "ds_read_b64_tr_b16 a[154:155], v9 offset:26624\n\t"
    "v_mul_f32 v117, v26, v117\n\t"
    "v_mfma_f32_16x16x32_bf16 v[128:131], a[112:115], v[148:151], v[128:131]\n\t"
    "v_mul_f32 v118, v26, v118\n\t"
    "v_mul_f32 v119, v26, v119\n\t"
    "v_mfma_f32_16x16x32_bf16 v[128:131], a[116:119], v[160:163], v[128:131]\n\t"
    "ds_read_b64_tr_b16 a[156:157], v9 offset:28672\n\t"
    "v_cvt_pk_bf16_f32 v116, v116, v117\n\t"
    "v_mfma_f32_16x16x32_bf16 v[128:131], a[120:123], v[172:175], v[128:131]\n\t"
    "ds_read_b64_tr_b16 a[158:159], v9 offset:30720\n\t"
    "v_cvt_pk_bf16_f32 v117, v118, v119\n\t"
    "v_mfma_f32_16x16x32_bf16 v[128:131], a[124:127], v[184:187], v[128:131]\n\t"
    "s_waitcnt lgkmcnt(14)\n\t"
    "v_mfma_f32_16x16x32_bf16 v[140:143], a[128:131], v[76:79], v[140:143]\n\t"
    "s_waitcnt lgkmcnt(12)\n\t"
    "ds_read_b64_tr_b16 a[96:97], v10 offset:0\n\t"
    "v_mfma_f32_16x16x32_bf16 v[140:143], a[132:135], v[48:51], v[140:143]\n\t"
    "s_waitcnt lgkmcnt(11)\n\t"
    "ds_read_b64_tr_b16 a[98:99], v10 offset:2048\n\t"
    "v_mfma_f32_16x16x32_bf16 v[140:143], a[136:139], v[92:95], v[140:143]\n\t"
    "s_waitcnt lgkmcnt(10)\n\t"
    "v_mul_f32 v128, v27, v128\n\t"
    "v_mul_f32 v129, v27, v129\n\t"
    "v_mfma_f32_16x16x32_bf16 v[140:143], a[140:143], v[88:91], v[140:143]\n\t"
    "s_waitcnt lgkmcnt(8)\n\t"
    "ds_read_b64_tr_b16 a[100:101], v10 offset:4096\n\t"
    "v_mul_f32 v130, v27, v130\n\t"
    "v_mfma_f32_16x16x32_bf16 v[140:143], a[144:147], v[68:71], v[140:143]\n\t"
    "s_waitcnt lgkmcnt(7)\n\t"
    "ds_read_b64_tr_b16 a[102:103], v10 offset:6144\n\t"
    "v_mul_f32 v131, v27, v131\n\t"
    "v_mfma_f32_16x16x32_bf16 v[140:143], a[148:151], v[44:47], v[140:143]\n\t"
    "s_waitcnt lgkmcnt(6)\n\t"
    "v_cvt_pk_bf16_f32 v128, v128, v129\n\t"
    "v_cvt_pk_bf16_f32 v129, v130, v131\n\t"
    "v_mfma_f32_16x16x32_bf16 v[140:143], a[152:155], v[64:67], v[140:143]\n\t"
    "s_waitcnt lgkmcnt(4)\n\t"
    "ds_read_b64_tr_b16 a[104:105], v10 offset:8192\n\t"
    "v_mfma_f32_16x16x32_bf16 v[140:143], a[156:159], v[104:107], v[140:143]\n\t"
    "ds_read_b64_tr_b16 a[106:107], v10 offset:10240\n\t"
    "v_mfma_f32_16x16x32_bf16 v[152:155], a[128:131], v[96:99], v[152:155]\n\t"
    "v_mfma_f32_16x16x32_bf16 v[152:155], a[132:135], v[108:111], v[152:155]\n\t"
    "ds_read_b64_tr_b16 a[108:109], v10 offset:12288\n\t"
    "v_mfma_f32_16x16x32_bf16 v[152:155], a[136:139], v[120:123], v[152:155]\n\t"
    "ds_read_b64_tr_b16 a[110:111], v10 offset:14336\n\t"
    "v_mfma_f32_16x16x32_bf16 v[152:155], a[140:143], v[132:135], v[152:155]\n\t"
    "v_mfma_f32_16x16x32_bf16 v[152:155], a[144:147], v[144:147], v[152:155]\n\t"
    "ds_read_b64_tr_b16 a[112:113], v10 offset:16384\n\t"
    "v_mul_f32 v140, v25, v140\n\t"
    "v_mfma_f32_16x16x32_bf16 v[152:155], a[148:151], v[156:159], v[152:155]\n\t"
    "ds_read_b64_tr_b16 a[114:115], v10 offset:18432\n\t"
    "v_mul_f32 v141, v25, v141\n\t"
    "v_mfma_f32_16x16x32_bf16 v[152:155], a[152:155], v[168:171], v[152:155]\n\t"
    "v_mul_f32 v142, v25, v142\n\t"
    "v_mul_f32 v143, v25, v143\n\t"
    "v_mfma_f32_16x16x32_bf16 v[152:155], a[156:159], v[180:183], v[152:155]\n\t"
    "ds_read_b64_tr_b16 a[116:117], v10 offset:20480\n\t"
    "v_cvt_pk_bf16_f32 v42, v140, v141\n\t"
    "v_mfma_f32_16x16x32_bf16 v[164:167], a[128:131], v[100:103], v[164:167]\n\t"
    "ds_read_b64_tr_b16 a[118:119], v10 offset:22528\n\t"
    "v_cvt_pk_bf16_f32 v43, v142, v143\n\t"
    "v_mfma_f32_16x16x32_bf16 v[164:167], a[132:135], v[112:115], v[164:167]\n\t"
    "v_permlane16_swap_b32 v40, v42\n\t"
    "v_mfma_f32_16x16x32_bf16 v[164:167], a[136:139], v[124:127], v[164:167]\n\t"
    "ds_read_b64_tr_b16 a[120:121], v10 offset:24576\n\t"
    "v_mul_f32 v152, v26, v152\n\t"
    "v_mfma_f32_16x16x32_bf16 v[164:167], a[140:143], v[136:139], v[164:167]\n\t"
    "ds_read_b64_tr_b16 a[122:123], v10 offset:26624\n\t"
    "v_mul_f32 v153, v26, v153\n\t"
    "v_mfma_f32_16x16x32_bf16 v[164:167], a[144:147], v[148:151], v[164:167]\n\t"
    "v_permlane16_swap_b32 v41, v43\n\t"
    "v_mfma_f32_16x16x32_bf16 v[164:167], a[148:151], v[160:163], v[164:167]\n\t"
    "ds_read_b64_tr_b16 a[124:125], v10 offset:28672\n\t"
    "v_mul_f32 v154, v26, v154\n\t"
    "v_mfma_f32_16x16x32_bf16 v[164:167], a[152:155], v[172:175], v[164:167]\n\t"
    "ds_read_b64_tr_b16 a[126:127], v10 offset:30720\n\t"
    "v_mul_f32 v155, v26, v155\n\t"
    "v_mfma_f32_16x16x32_bf16 v[164:167], a[156:159], v[184:187], v[164:167]\n\t"
    "s_waitcnt lgkmcnt(14)\n\t"
    "buffer_store_dwordx4 v[40:43], v21, s[36:39], s73 offen offset:0 sc1\n\t"
    "v_mfma_f32_16x16x32_bf16 v[176:179], a[96:99], v[76:79], v[176:179]\n\t"
    "s_waitcnt lgkmcnt(12)\n\t"
    "ds_read_b64_tr_b16 a[128:129], v11 offset:0\n\t"
    "v_cvt_pk_bf16_f32 v118, v152, v153\n\t"
    "v_mfma_f32_16x16x32_bf16 v[176:179], a[100:103], v[48:51], v[176:179]\n\t"
    "s_waitcnt lgkmcnt(11)\n\t"
    "ds_read_b64_tr_b16 a[130:131], v11 offset:2048\n\t"
    "v_cvt_pk_bf16_f32 v119, v154, v155\n\t"
    "v_mfma_f32_16x16x32_bf16 v[176:179], a[104:107], v[92:95], v[176:179]\n\t"
    "s_waitcnt lgkmcnt(10)\n\t"
    "v_permlane16_swap_b32 v116, v118\n\t"
    "v_mfma_f32_16x16x32_bf16 v[176:179], a[108:111], v[88:91], v[176:179]\n\t"
    "s_waitcnt lgkmcnt(8)\n\t"
    "ds_read_b64_tr_b16 a[132:133], v11 offset:4096\n\t"
    "v_mul_f32 v164, v27, v164\n\t"
    "v_mfma_f32_16x16x32_bf16 v[176:179], a[112:115], v[68:71], v[176:179]\n\t"
    "s_waitcnt lgkmcnt(7)\n\t"
    "ds_read_b64_tr_b16 a[134:135], v11 offset:6144\n\t"
    "v_mul_f32 v165, v27, v165\n\t"
    "v_mfma_f32_16x16x32_bf16 v[176:179], a[116:119], v[44:47], v[176:179]\n\t"
    "s_waitcnt lgkmcnt(6)\n\t"
    "v_permlane16_swap_b32 v117, v119\n\t"
    "v_mfma_f32_16x16x32_bf16 v[176:179], a[120:123], v[64:67], v[176:179]\n\t"
    "s_waitcnt lgkmcnt(4)\n\t"
    "ds_read_b64_tr_b16 a[136:137], v11 offset:8192\n\t"
    "v_mul_f32 v166, v27, v166\n\t"
    "v_mfma_f32_16x16x32_bf16 v[176:179], a[124:127], v[104:107], v[176:179]\n\t"
    "ds_read_b64_tr_b16 a[138:139], v11 offset:10240\n\t"
    "v_mul_f32 v167, v27, v167\n\t"
    "v_mfma_f32_16x16x32_bf16 v[188:191], a[96:99], v[96:99], v[188:191]\n\t"
    "buffer_store_dwordx4 v[116:119], v21, s[36:39], s74 offen offset:0 sc1\n\t"
    "v_mfma_f32_16x16x32_bf16 v[188:191], a[100:103], v[108:111], v[188:191]\n\t"
    "ds_read_b64_tr_b16 a[140:141], v11 offset:12288\n\t"
    "v_cvt_pk_bf16_f32 v130, v164, v165\n\t"
    "v_mfma_f32_16x16x32_bf16 v[188:191], a[104:107], v[120:123], v[188:191]\n\t"
    "ds_read_b64_tr_b16 a[142:143], v11 offset:14336\n\t"
    "v_cvt_pk_bf16_f32 v131, v166, v167\n\t"
    "v_mfma_f32_16x16x32_bf16 v[188:191], a[108:111], v[132:135], v[188:191]\n\t"
    "v_permlane16_swap_b32 v128, v130\n\t"
    "v_mfma_f32_16x16x32_bf16 v[188:191], a[112:115], v[144:147], v[188:191]\n\t"
    "ds_read_b64_tr_b16 a[144:145], v11 offset:16384\n\t"
    "v_mul_f32 v176, v25, v176\n\t"
    "v_mfma_f32_16x16x32_bf16 v[188:191], a[116:119], v[156:159], v[188:191]\n\t"
    "ds_read_b64_tr_b16 a[146:147], v11 offset:18432\n\t"
    "v_mul_f32 v177, v25, v177\n\t"
    "v_mfma_f32_16x16x32_bf16 v[188:191], a[120:123], v[168:171], v[188:191]\n\t"
    "v_permlane16_swap_b32 v129, v131\n\t"
    "v_mfma_f32_16x16x32_bf16 v[188:191], a[124:127], v[180:183], v[188:191]\n\t"
    "ds_read_b64_tr_b16 a[148:149], v11 offset:20480\n\t"
    "v_mul_f32 v178, v25, v178\n\t"
    "v_mfma_f32_16x16x32_bf16 v[192:195], a[96:99], v[100:103], v[192:195]\n\t"
    "ds_read_b64_tr_b16 a[150:151], v11 offset:22528\n\t"
    "v_mul_f32 v179, v25, v179\n\t"
    "v_mfma_f32_16x16x32_bf16 v[192:195], a[100:103], v[112:115], v[192:195]\n\t"
    "buffer_store_dwordx4 v[128:131], v21, s[36:39], s75 offen offset:0 sc1\n\t"
    "v_mfma_f32_16x16x32_bf16 v[192:195], a[104:107], v[124:127], v[192:195]\n\t"
    "ds_read_b64_tr_b16 a[152:153], v11 offset:24576\n\t"
    "v_cvt_pk_bf16_f32 v176, v176, v177\n\t"
    "v_mfma_f32_16x16x32_bf16 v[192:195], a[108:111], v[136:139], v[192:195]\n\t"
    "ds_read_b64_tr_b16 a[154:155], v11 offset:26624\n\t"
    "v_cvt_pk_bf16_f32 v177, v178, v179\n\t"
    "v_mfma_f32_16x16x32_bf16 v[192:195], a[112:115], v[148:151], v[192:195]\n\t"
    "v_mul_f32 v188, v26, v188\n\t"
    "v_mul_f32 v189, v26, v189\n\t"
    "v_mfma_f32_16x16x32_bf16 v[192:195], a[116:119], v[160:163], v[192:195]\n\t"
    "ds_read_b64_tr_b16 a[156:157], v11 offset:28672\n\t"
    "v_mul_f32 v190, v26, v190\n\t"
    "v_mfma_f32_16x16x32_bf16 v[192:195], a[120:123], v[172:175], v[192:195]\n\t"
    "ds_read_b64_tr_b16 a[158:159], v11 offset:30720\n\t"
    "v_mul_f32 v191, v26, v191\n\t"
    "v_mfma_f32_16x16x32_bf16 v[192:195], a[124:127], v[184:187], v[192:195]\n\t"
    "s_waitcnt lgkmcnt(14)\n\t"
    "v_cvt_pk_bf16_f32 v188, v188, v189\n\t"
    "v_cvt_pk_bf16_f32 v189, v190, v191\n\t"
    "v_mfma_f32_16x16x32_bf16 v[196:199], a[128:131], v[76:79], v[196:199]\n\t"
    "s_waitcnt lgkmcnt(12)\n\t"
    "v_mfma_f32_16x16x32_bf16 v[196:199], a[132:135], v[48:51], v[196:199]\n\t"
    "s_waitcnt lgkmcnt(10)\n\t"
    "v_mfma_f32_16x16x32_bf16 v[196:199], a[136:139], v[92:95], v[196:199]\n\t"
    "s_waitcnt lgkmcnt(8)\n\t"
    "v_mul_f32 v192, v27, v192\n\t"
    "v_mul_f32 v193, v27, v193\n\t"
    "v_mfma_f32_16x16x32_bf16 v[196:199], a[140:143], v[88:91], v[196:199]\n\t"
    "s_waitcnt lgkmcnt(6)\n\t"
    "v_mul_f32 v194, v27, v194\n\t"
    "v_mul_f32 v195, v27, v195\n\t"
    "v_mfma_f32_16x16x32_bf16 v[196:199], a[144:147], v[68:71], v[196:199]\n\t"
    "s_waitcnt lgkmcnt(4)\n\t"
    "v_cvt_pk_bf16_f32 v192, v192, v193\n\t"
    "v_cvt_pk_bf16_f32 v193, v194, v195\n\t"
    "v_mfma_f32_16x16x32_bf16 v[196:199], a[148:151], v[44:47], v[196:199]\n\t"
    "s_waitcnt lgkmcnt(2)\n\t"
    "v_mfma_f32_16x16x32_bf16 v[196:199], a[152:155], v[64:67], v[196:199]\n\t"
    "s_waitcnt lgkmcnt(0)\n\t"
    "v_mfma_f32_16x16x32_bf16 v[196:199], a[156:159], v[104:107], v[196:199]\n\t"
    "v_mfma_f32_16x16x32_bf16 v[200:203], a[128:131], v[96:99], v[200:203]\n\t"
    "v_mfma_f32_16x16x32_bf16 v[200:203], a[132:135], v[108:111], v[200:203]\n\t"
    "v_mfma_f32_16x16x32_bf16 v[200:203], a[136:139], v[120:123], v[200:203]\n\t"
    "v_mfma_f32_16x16x32_bf16 v[200:203], a[140:143], v[132:135], v[200:203]\n\t"
    "v_mfma_f32_16x16x32_bf16 v[200:203], a[144:147], v[144:147], v[200:203]\n\t"
    "v_mfma_f32_16x16x32_bf16 v[200:203], a[148:151], v[156:159], v[200:203]\n\t"
    "v_mfma_f32_16x16x32_bf16 v[200:203], a[152:155], v[168:171], v[200:203]\n\t"
    "v_mfma_f32_16x16x32_bf16 v[200:203], a[156:159], v[180:183], v[200:203]\n\t"
    "v_mul_f32 v196, v25, v196\n\t"
    "v_mul_f32 v197, v25, v197\n\t"
    "v_mfma_f32_16x16x32_bf16 v[212:215], a[128:131], v[100:103], v[212:215]\n\t"
    "v_mul_f32 v198, v25, v198\n\t"
    "v_mul_f32 v199, v25, v199\n\t"
    "v_mfma_f32_16x16x32_bf16 v[212:215], a[132:135], v[112:115], v[212:215]\n\t"
    "v_cvt_pk_bf16_f32 v178, v196, v197\n\t"
    "v_cvt_pk_bf16_f32 v179, v198, v199\n\t"
    "v_mfma_f32_16x16x32_bf16 v[212:215], a[136:139], v[124:127], v[212:215]\n\t"
    "v_permlane16_swap_b32 v176, v178\n\t"
    "v_mfma_f32_16x16x32_bf16 v[212:215], a[140:143], v[136:139], v[212:215]\n\t"
    "v_permlane16_swap_b32 v177, v179\n\t"
    "v_mfma_f32_16x16x32_bf16 v[212:215], a[144:147], v[148:151], v[212:215]\n\t"
    "buffer_store_dwordx4 v[176:179], v21, s[36:39], s73 offen offset:64 sc1\n\t"
    "v_mfma_f32_16x16x32_bf16 v[212:215], a[148:151], v[160:163], v[212:215]\n\t"
    "v_mul_f32 v200, v26, v200\n\t"
    "v_mul_f32 v201, v26, v201\n\t"
    "v_mfma_f32_16x16x32_bf16 v[212:215], a[152:155], v[172:175], v[212:215]\n\t"
    "v_mul_f32 v202, v26, v202\n\t"
    "v_mul_f32 v203, v26, v203\n\t"
    "v_mfma_f32_16x16x32_bf16 v[212:215], a[156:159], v[184:187], v[212:215]\n\t"
    "v_cvt_pk_bf16_f32 v190, v200, v201\n\t"
    "v_cvt_pk_bf16_f32 v191, v202, v203\n\t"
    "s_nop 0\n\t"
    "v_permlane16_swap_b32 v188, v190\n\t"
    "v_permlane16_swap_b32 v189, v191\n\t"
    "s_nop 0\n\t"
    "buffer_store_dwordx4 v[188:191], v21, s[36:39], s74 offen offset:64 sc1\n\t"
    "s_nop 0\n\t"
    "v_mul_f32 v212, v27, v212\n\t"
    "v_mul_f32 v213, v27, v213\n\t"
    "v_mul_f32 v214, v27, v214\n\t"
    "v_mul_f32 v215, v27, v215\n\t"
    "v_cvt_pk_bf16_f32 v194, v212, v213\n\t"
    "v_cvt_pk_bf16_f32 v195, v214, v215\n\t"
    "s_nop 0\n\t"
    "v_permlane16_swap_b32 v192, v194\n\t"
    "v_permlane16_swap_b32 v193, v195\n\t"
    "s_nop 0\n\t"
    "buffer_store_dwordx4 v[192:195], v21, s[36:39], s75 offen offset:64 sc1\n\t"
    "v_or3_b32 v12, v12, v13, v14\n\t"
    "v_or_b32 v12, v12, v15\n\t"
    "s_nop 1\n\t"
    "v_readfirstlane_b32 s56, v12\n\t"
    "s_nop 4\n\t"
    "s_andn2_b32 s56, s56, s50\n\t"
    "s_cbranch_scc1 Lrare_%=\n\t"
    "Lend_%=:\n\t"
    :
    : "s"(__builtin_amdgcn_kernarg_segment_ptr()), "s"(blockIdx.x), "v"(threadIdx.x), "s"((unsigned)(size_t)lds)
    : "s4", "s5", "s6", "s7", "s8", "v1", "v2", "v3", "v4", "v5", "v6", "v7", "v8", "v9", "v10", "v11", "v12", "v13", "v14", "v15", "v16", "v17", "v18", "v19", "v20", "v21", "v22", "v23", "v24", "v25", "v26", "v27", "v28", "v29", "v30", "v31", "v32", "v33", "v34", "v35", "v36", "v37", "v38", "v39", "v40", "v41", "v42", "v43", "v44", "v45", "v46", "v47", "v48", "v49", "v50", "v51", "v52", "v53", "v54", "v55", "v56", "v57", "v58", "v59", "v60", "v61", "v62", "v63", "v64", "v65", "v66", "v67", "v68", "v69", "v70", "v71", "v72", "v73", "v74", "v75", "v76", "v77", "v78", "v79", "v80", "v81", "v82", "v83", "v84", "v85", "v86", "v87", "v88", "v89", "v90", "v91", "v92", "v93", "v94", "v95", "v96", "v97", "v98", "v99", "v100", "v101", "v102", "v103", "v104", "v105", "v106", "v107", "v108", "v109", "v110", "v111", "v112", "v113", "v114", "v115", "v116", "v117", "v118", "v119", "v120", "v121", "v122", "v123", "v124", "v125", "v126", "v127", "v128", "v129", "v130", "v131", "v132", "v133", "v134", "v135", "v136", "v137", "v138", "v139", "v140", "v141", "v142", "v143", "v144", "v145", "v146", "v147", "v148", "v149", "v150", "v151", "v152", "v153", "v154", "v155", "v156", "v157", "v158", "v159", "v160", "v161", "v162", "v163", "v164", "v165", "v166", "v167", "v168", "v169", "v170", "v171", "v172", "v173", "v174", "v175", "v176", "v177", "v178", "v179", "v180", "v181", "v182", "v183", "v184", "v185", "v186", "v187", "v188", "v189", "v190", "v191", "v192", "v193", "v194", "v195", "v196", "v197", "v198", "v199", "v200", "v201", "v202", "v203", "v204", "v205", "v206", "v207", "v208", "v209", "v210", "v211", "v212", "v213", "v214", "v215", "v216", "v217", "v218", "v219", "v220", "v221", "v222", "v223", "v224", "v225", "v226", "v227", "v228", "v229", "v230", "v231", "v232", "v233", "v234", "v235", "v236", "v237", "v238", "v239", "v240", "v241", "v242", "v243", "v244", "v245", "v246", "v247", "v248", "v249", "v250", "v251", "v252", "v253", "v254", "v255", "a0", "a1", "a2", "a3", "a4", "a5", "a6", "a7", "a8", "a9", "a10", "a11", "a12", "a13", "a14", "a15", "a16", "a17", "a18", "a19", "a20", "a21", "a22", "a23", "a24", "a25", "a26", "a27", "a28", "a29", "a30", "a31", "a32", "a33", "a34", "a35", "a36", "a37", "a38", "a39", "a40", "a41", "a42", "a43", "a44", "a45", "a46", "a47", "a48", "a49", "a50", "a51", "a52", "a53", "a54", "a55", "a56", "a57", "a58", "a59", "a60", "a61", "a62", "a63", "a64", "a65", "a66", "a67", "a68", "a69", "a70", "a71", "a72", "a73", "a74", "a75", "a76", "a77", "a78", "a79", "a80", "a81", "a82", "a83", "a84", "a85", "a86", "a87", "a88", "a89", "a90", "a91", "a92", "a93", "a94", "a95", "a96", "a97", "a98", "a99", "a100", "a101", "a102", "a103", "a104", "a105", "a106", "a107", "a108", "a109", "a110", "a111", "a112", "a113", "a114", "a115", "a116", "a117", "a118", "a119", "a120", "a121", "a122", "a123", "a124", "a125", "a126", "a127", "a128", "a129", "a130", "a131", "a132", "a133", "a134", "a135", "a136", "a137", "a138", "a139", "a140", "a141", "a142", "a143", "a144", "a145", "a146", "a147", "a148", "a149", "a150", "a151", "a152", "a153", "a154", "a155", "a156", "a157", "a158", "a159", "a160", "a161", "a162", "a163", "a164", "a165", "a166", "a167", "a168", "a169", "a170", "a171", "a172", "a173", "a174", "a175", "a176", "a177", "a178", "a179", "a180", "a181", "a182", "a183", "a184", "a185", "a186", "a187", "a188", "a189", "a190", "a191", "a192", "a193", "a194", "a195", "a196", "a197", "a198", "a199", "a200", "a201", "a202", "a203", "a204", "a205", "a206", "a207", "a208", "a209", "a210", "a211", "a212", "a213", "a214", "a215", "a216", "a217", "a218", "a219", "a220", "a221", "a222", "a223", "a224", "a225", "a226", "a227", "a228", "a229", "a230", "a231", "a232", "a233", "a234", "a235", "a236", "a237", "a238", "a239", "a240", "a241", "a242", "a243", "a244", "a245", "a246", "a247", "a248", "a249", "a250", "a251", "a252", "a253", "a254", "a255", "s20", "s21", "s22", "s23", "s24", "s25", "s26", "s27", "s28", "s29", "s30", "s31", "s34", "s35", "s36", "s37", "s38", "s39", "s40", "s41", "s42", "s43", "s44", "s45", "s46", "s47", "s48", "s49", "s50", "s51", "s52", "s53", "s54", "s55", "s56", "s57", "s58", "s59", "s60", "s61", "s62", "s63", "s64", "s65", "s66", "s67", "s68", "s69", "s70", "s71", "s72", "s73", "s74", "s75", "s76", "s77", "s78", "s79", "s80", "s81", "s82", "s83", "s84", "s85", "s86", "s87", "s88", "s89", "s90", "s91", "s92", "s93", "s94", "s95", "s96", "s97", "s98", "s99", "vcc", "scc", "memory");
}

extern "C" void kernel_launch(void* const* d_in, const int* in_sizes, int n_in, void* d_out, int out_size, void* d_ws, size_t ws_size,
                              hipStream_t stream) {
  hipLaunchKernelGGL(gqa_attn_kernel, dim3(256), dim3(256), 0, stream, (const unsigned short*)d_in[0], (const unsigned short*)d_in[1],
                     (const unsigned short*)d_in[2], (unsigned short*)d_out
  );
}
```
